```python
import torch

def _softmax(x, axis=-1):
    return torch.softmax(x, dim=axis)
def _gelu(x):
    return torch.nn.functional.gelu(x, approximate="tanh")
def _silu(x):
    return torch.nn.functional.silu(x)
def _sigmoid(x):
    return torch.sigmoid(x)
def _layernorm(x, axis=-1, eps=1e-5):
    m = x.mean(dim=axis, keepdim=True)
    v = x.var(dim=axis, keepdim=True, unbiased=False)
    return (x - m) / torch.sqrt(v + eps)
def _scatter_add(tgt, idx, src, axis=0):
    return tgt.clone().index_add_(axis, idx, src)
def _segment_sum(src, seg, n):
    out = torch.zeros((n,) + src.shape[1:], dtype=src.dtype)
    return out.index_add_(0, seg, src)
def _pad(x, pw):
    flat = tuple(v for pair in reversed(pw) for v in pair)
    return torch.nn.functional.pad(x, flat)

kernel_name = 'interleave_layers__sliding_local_dilated__glu_gated_bilinear__prompt'

def setup_inputs():

    _w = (lambda a: (lambda t: t.to(torch.bfloat16) if t.is_floating_point() else t)(torch.as_tensor(a)))
    def _A_setup():
        B, S, D = 1, 2048, 640
        return {
            "x": _w((lambda *s: torch.randn(*s, dtype=torch.bfloat16) * 0.05)(B, S, D)),
            "wqkv": _w((lambda *s: torch.randn(*s, dtype=torch.bfloat16) * 0.05)(D, 3 * D)),
            "wo": _w((lambda *s: torch.randn(*s, dtype=torch.bfloat16) * 0.05)(D, D)),
        }
    _A_in = _A_setup()

    def _B_setup():
        B, S, D, F = 1, 2048, 640, 1728
        return {
            "x": _w((lambda *s: torch.randn(*s, dtype=torch.bfloat16) * 0.05)(B, S, D)),
            "w_gate": _w((lambda *s: torch.randn(*s, dtype=torch.bfloat16) * 0.8)(D, F) * (D ** -0.5)),
            "w_up": _w((lambda *s: torch.randn(*s, dtype=torch.bfloat16) * 0.8)(D, F) * (D ** -0.5)),
            "w_down": _w((lambda *s: torch.randn(*s, dtype=torch.bfloat16) * 0.8)(F, D) * (F ** -0.5)),
        }
    _B_in = _B_setup()

    return {
        "A_x": _A_in["x"],
        "A_wqkv": _A_in["wqkv"],
        "A_wo": _A_in["wo"],
        "B_x": _B_in["x"],
        "B_w_gate": _B_in["w_gate"],
        "B_w_up": _B_in["w_up"],
        "B_w_down": _B_in["w_down"],
    }

def reference(A_x, A_wqkv, A_wo, B_x, B_w_gate, B_w_up, B_w_down):
    def _A_ref(x, wqkv, wo):
        B, S, D = x.shape
        H = 32; Dh = D // H
        W, dil = 8, 2
        qkv = (lambda x, *d: x.permute(*d))((x @ wqkv).reshape(B, S, 3, H, Dh), 2, 0, 3, 1, 4)
        q, k, v = qkv[0], qkv[1], qkv[2]
        scale = Dh ** -0.5
        pos = torch.arange(S)
        off = torch.arange(W) * dil
        idx = pos[:, None] - off[None, :]
        mask = idx >= 0
        idx_c = torch.where(mask, idx, 0).reshape(-1)
        kg = k[:, :, idx_c, :].reshape(B, H, S, W, Dh)
        vg = v[:, :, idx_c, :].reshape(B, H, S, W, Dh)
        logits = torch.einsum("bhsd,bhswd->bhsw", q, kg) * scale
        logits = torch.where(mask[None, None, :, :], logits, -1e9)
        attn = _softmax(logits, axis=-1)
        o = torch.einsum("bhsw,bhswd->bhsd", attn, vg)
        return (lambda x, *d: x.permute(*d))(o, 0, 2, 1, 3).reshape(B, S, D) @ wo

    def _B_ref(x, w_gate, w_up, w_down):
        g = torch.einsum("bsd,df->bsf", x, w_gate)
        u = torch.einsum("bsd,df->bsf", x, w_up)
        h = g * u
        return torch.einsum("bsf,fd->bsd", h, w_down)

    x = A_x
    x = x + _A_ref(_layernorm(x, axis=-1), A_wqkv, A_wo)
    x = x + _B_ref(_layernorm(x, axis=-1), B_w_gate, B_w_up, B_w_down)
    x = x + _A_ref(_layernorm(x, axis=-1), A_wqkv, A_wo)
    x = x + _B_ref(_layernorm(x, axis=-1), B_w_gate, B_w_up, B_w_down)
    return x
```

```cpp
#include <hip/hip_runtime.h>
#include <hip/hip_bf16.h>
typedef unsigned short u16;
constexpr int S=2048, D=640, H=32, DH=20, F=1728;
__device__ __forceinline__ float bf2f(u16 v){ return __uint_as_float(((unsigned)v)<<16); }
__device__ __forceinline__ u16 f2bf(float f){ unsigned u=__float_as_uint(f); u += 0x7FFF + ((u>>16)&1); return (u16)(u>>16); }
__device__ __forceinline__ float rbf(float f){ return bf2f(f2bf(f)); }

__global__ void k_ln(const u16* x, u16* y){
  int row=blockIdx.x; __shared__ float red[256];
  float s=0; for(int i=threadIdx.x;i<D;i+=256) s+=bf2f(x[row*D+i]);
  red[threadIdx.x]=s; __syncthreads();
  for(int o=128;o>0;o>>=1){ if(threadIdx.x<o) red[threadIdx.x]+=red[threadIdx.x+o]; __syncthreads(); }
  float mean=red[0]/D; __syncthreads();
  float v=0; for(int i=threadIdx.x;i<D;i+=256){ float d=bf2f(x[row*D+i])-mean; v+=d*d; }
  red[threadIdx.x]=v; __syncthreads();
  for(int o=128;o>0;o>>=1){ if(threadIdx.x<o) red[threadIdx.x]+=red[threadIdx.x+o]; __syncthreads(); }
  float var=rbf(red[0]/D); float m=rbf(mean);
  float sd=rbf(sqrtf(rbf(var+1e-5f)));
  for(int i=threadIdx.x;i<D;i+=256){ float d=rbf(bf2f(x[row*D+i])-m); y[row*D+i]=f2bf(d/sd); }
}
__global__ void k_gemm(const u16* A,const u16* B,u16* C,const u16* res,int M,int N,int K){
  __shared__ float sa[16][17], sb[16][17];
  int tx=threadIdx.x&15, ty=threadIdx.x>>4;
  int row=blockIdx.y*16+ty, col=blockIdx.x*16+tx; float acc=0;
  for(int k0=0;k0<K;k0+=16){
    sa[ty][tx]=bf2f(A[row*K+k0+tx]); sb[ty][tx]=bf2f(B[(k0+ty)*N+col]); __syncthreads();
    for(int k=0;k<16;k++) acc+=sa[ty][k]*sb[k][tx];
    __syncthreads();
  }
  float r=rbf(acc); if(res) r=bf2f(res[row*N+col])+r;
  C[row*N+col]=f2bf(r);
}
__global__ void k_attn(const u16* qkv,u16* o){
  int idx=blockIdx.x*blockDim.x+threadIdx.x; if(idx>=S*H) return;
  int h=idx%H, t=idx/H;
  const u16* q=qkv+t*3*D+h*DH; float lg[8]; float mx=-1e30f;
  float scale=0.22360679774997896f;
  for(int w=0;w<8;w++){ int p=t-2*w; if(p<0){lg[w]=rbf(-1e9f);} else { const u16* k=qkv+p*3*D+D+h*DH; float s=0; for(int d=0;d<DH;d++) s+=bf2f(q[d])*bf2f(k[d]); lg[w]=rbf(rbf(s)*scale);} mx=fmaxf(mx,lg[w]); }
  float sum=0; for(int w=0;w<8;w++){ lg[w]=expf(lg[w]-mx); sum+=lg[w]; }
  for(int w=0;w<8;w++) lg[w]=rbf(lg[w]/sum);
  for(int d=0;d<DH;d++){ float s=0; for(int w=0;w<8;w++){ int p=t-2*w; if(p<0)p=0; s+=lg[w]*bf2f(qkv[p*3*D+2*D+h*DH+d]); } o[t*D+h*DH+d]=f2bf(s); }
}
__global__ void k_mul(const u16* g,const u16* u,u16* h,int n){ int i=blockIdx.x*blockDim.x+threadIdx.x; if(i<n) h[i]=f2bf(bf2f(g[i])*bf2f(u[i])); }

extern "C" void kernel_launch(void* const* d_in, const int* in_sizes, int n_in, void* d_out, int out_size, void* d_ws, size_t ws_size, hipStream_t stream) {
  const u16* x=(const u16*)d_in[0]; const u16* wqkv=(const u16*)d_in[1]; const u16* wo=(const u16*)d_in[2];
  const u16* wg=(const u16*)d_in[4]; const u16* wu=(const u16*)d_in[5]; const u16* wd=(const u16*)d_in[6];
  u16* ws=(u16*)d_ws;
  u16* ln=ws; u16* qkv=ln+S*D; u16* o=qkv+S*3*D; u16* x1=o+S*D; u16* x2=x1+S*D; u16* g=x2+S*D; u16* u=g+S*F; u16* h=u+S*F;
  const u16* cur=x;
  for(int rep=0;rep<2;rep++){
    k_ln<<<S,256,0,stream>>>(cur,ln);
    k_gemm<<<dim3(3*D/16,S/16),256,0,stream>>>(ln,wqkv,qkv,nullptr,S,3*D,D);
    k_attn<<<S*H/256,256,0,stream>>>(qkv,o);
    k_gemm<<<dim3(D/16,S/16),256,0,stream>>>(o,wo,x1,cur,S,D,D);
    k_ln<<<S,256,0,stream>>>(x1,ln);
    k_gemm<<<dim3(F/16,S/16),256,0,stream>>>(ln,wg,g,nullptr,S,F,D);
    k_gemm<<<dim3(F/16,S/16),256,0,stream>>>(ln,wu,u,nullptr,S,F,D);
    k_mul<<<(S*F+255)/256,256,0,stream>>>(g,u,h,S*F);
    u16* dst = rep==0 ? x2 : (u16*)d_out;
    k_gemm<<<dim3(D/16,S/16),256,0,stream>>>(h,wd,dst,x1,S,D,F);
    cur=x2;
  }
}
```

```cpp
#include <hip/hip_runtime.h>
#include <hip/hip_bf16.h>
typedef unsigned short u16;
typedef unsigned long long u64;
typedef unsigned u32x4 __attribute__((ext_vector_type(4)));
typedef unsigned u32x2 __attribute__((ext_vector_type(2)));
#define GAS __attribute__((address_space(1)))
constexpr unsigned GEN_OFF=0u;
constexpr unsigned PF_OFF=4096u;
constexpr unsigned WQ_OFF=65536u;
constexpr unsigned WO_OFF=2686976u;
constexpr unsigned WG_OFF=3506176u;
constexpr unsigned WD_OFF=7929856u;
constexpr unsigned CSQ_OFF=10223616u;
constexpr unsigned CSG_OFF=10354688u;
constexpr unsigned RR0=23027712u;
constexpr unsigned REPSZ=2785280u;
constexpr unsigned R_XIN=0u;
constexpr unsigned R_STIN=2621440u;
constexpr unsigned R_FL=2752512u;
constexpr unsigned WS_USED=28598272u;
struct Params { const u16* x; const u16* wqkv; const u16* wo; const u16* wg; const u16* wu; const u16* wd; u16* out; char* ws; };

#define LAS __attribute__((address_space(3)))
typedef short bf16x8 __attribute__((ext_vector_type(8)));
typedef short v4s __attribute__((ext_vector_type(4)));
typedef float f32x4 __attribute__((ext_vector_type(4)));
__device__ __forceinline__ void dma16(const u16* src, LAS char* dst){
  __builtin_amdgcn_global_load_lds((const GAS void*)src,(LAS void*)dst,16,0,0);
}
#define DMA_WAIT() asm volatile("s_waitcnt vmcnt(0)":::"memory")
__device__ __forceinline__ u32x4 trfrag(LAS char* a, int step4){
  const v4s lo=__builtin_amdgcn_ds_read_tr16_b64_v4i16((LAS v4s*)a), hi=__builtin_amdgcn_ds_read_tr16_b64_v4i16((LAS v4s*)(a+step4));
  const u32x2 l=__builtin_bit_cast(u32x2,lo), h=__builtin_bit_cast(u32x2,hi);
  return u32x4{l[0],l[1],h[0],h[1]};
}
__global__ __launch_bounds__(256,1) void fused_kernel(Params p){
  extern __shared__ __attribute__((aligned(16))) char lds[];
  const int b=blockIdx.x, tid=threadIdx.x, w=tid>>6, lane=tid&63, r=lane&15, g4=lane>>4;
  const int wv=__builtin_amdgcn_readfirstlane(w);
  { unsigned long long pc; unsigned len;
    asm volatile("s_getpc_b64 %0\nLPCREF:\n\ts_mov_b32 %1, LCODEEND-LPCREF":"=s"(pc),"=s"(len));
    unsigned off=((b>>3)&15)<<12; const unsigned last=(len-4096u)&~15u; off=off<last?off:last;
    dma16((const u16*)((const char*)pc+(off&~15u)+tid*16-((unsigned)pc&15u)),(LAS char*)lds+98304+wv*1024); }
  const __amdgpu_buffer_rsrc_t rs=__builtin_amdgcn_make_buffer_rsrc(p.ws,0,WS_USED,0x00020000);
  const u64 gen=__hip_atomic_load((GAS u64*)(p.ws+GEN_OFF),__ATOMIC_RELAXED,__HIP_MEMORY_SCOPE_AGENT);
  const u64 tag=gen+1;
  const int xcd=b&7, ii=b>>3, grp=4*(xcd>>1)+(ii>>3), slot=8*(xcd&1)+(ii&7);
  LAS char* imgA=(LAS char*)lds+wv*24576;
  LAS char* img=imgA+8192;
  const int q4=(lane>>2)&3, p4=lane&3;
  const int qb=(wv-2)*16+grp;
  const bool qjob=wv>=2&&qb<20, xjob=wv<2;
  const int qa=slot;
  const int xid=2*slot+wv, xMT=(xid>>4)*64+4*grp+((xid>>2)&3), xkq=xid&3;
  const int jb0= wv==3 ? (grp>=4?(grp-4)*16+slot:-1) : (wv<2 ? 192+wv*256+grp*16+slot : -1);
  const int jt=(jb0<0||jb0>=574)?0:(jb0<270?1:(jb0<350?2:(jb0<566?3:4)));
  const int jid=jb0-(jt==1?0:(jt==2?270:(jt==3?350:566)));
  int ja,jb;
  if(jt==3){ ja=jid/27; jb=jid-27*ja; } else { ja=jid/10; jb=jid-10*ja; }
  u32x4 xv0,xv1,xv2,xv3,xv4;
  if(qjob){
    const int kp=qb>>1, th=qb&1;
    const u16* src=p.wqkv+(size_t)(64*kp)*1920+40*qa;
#pragma unroll
    for(int i=0;i<8;i++){ const int c=64*i+lane, row=c>>3, ch=c&7;
      const int col= th==0 ? (ch<5?640+8*ch:1280+8*(ch-5)) : (ch<2?1280+8*(ch+3):(ch<7?8*(ch-2):0));
      dma16(src+row*1920+col,imgA+i*1024); }
  }
  { const int t=xjob?2*(16*(xMT&63)+r)+(xMT>>6):0; const u16* xs=p.x+t*640+(xjob?160*xkq:0)+8*g4;
    asm volatile("global_load_dwordx4 %0, %5, off\n\tglobal_load_dwordx4 %1, %5, off offset:64\n\tglobal_load_dwordx4 %2, %5, off offset:128\n\tglobal_load_dwordx4 %3, %5, off offset:192\n\tglobal_load_dwordx4 %4, %5, off offset:256"
      :"=&v"(xv0),"=&v"(xv1),"=&v"(xv2),"=&v"(xv3),"=&v"(xv4):"v"(xs):"memory"); }
  asm volatile("s_waitcnt vmcnt(0)":"+v"(xv0),"+v"(xv1),"+v"(xv2),"+v"(xv3),"+v"(xv4)::"memory");
  if(jt==1){
    const int o=(64*jb+(lane>>3))*1728+64*ja+8*(lane&7);
#pragma unroll
    for(int i=0;i<8;i++){ dma16(p.wg+o+i*8*1728,img+i*1024); dma16(p.wu+o+i*8*1728,img+8192+i*1024); }
  } else if(jt==2){
#pragma unroll
    for(int i=0;i<10;i++){ const int c=64*i+lane, rho=(c*205)>>11, ch=c-10*rho;
      const int C=4*(2*jb+(rho>>5))+((rho>>3)&3), e=rho&7, jp=(C*205)>>10, cc=C-5*jp, k=40*jp+20*(e>>2)+(cc<4?4*cc:16)+(e&3);
      dma16(p.wo+k*640+80*ja+8*ch,img+i*1024); }
  } else if(jt==3){
#pragma unroll
    for(int i=0;i<10;i++){ const int c=64*i+lane, rho=(c*205)>>11, ch=c-10*rho;
      dma16(p.wd+(64*jb+rho)*640+80*ja+8*ch,img+i*1024); }
  }
  const bf16x8 ones={0x3f80,0x3f80,0x3f80,0x3f80,0x3f80,0x3f80,0x3f80,0x3f80};
  if(qjob){
    const int kp=qb>>1, th=qb&1;
    u32x4 f[4][2];
#pragma unroll
    for(int tt=0;tt<4;tt++)
#pragma unroll
      for(int s=0;s<2;s++) f[tt][s]=trfrag(imgA+(32*s+8*g4+q4)*128+(4*tt+p4)*8,4*128);
#pragma unroll
    for(int tt=0;tt<4;tt++){ const int T=4*th+tt; f32x4 cs={0.f,0.f,0.f,0.f};
#pragma unroll
      for(int s=0;s<2;s++){ if(tt==3&&th==1&&r>=8) f[tt][s]=u32x4{0u,0u,0u,0u};
        __builtin_amdgcn_raw_buffer_store_b128(f[tt][s],rs,WQ_OFF+((qa*4+(T>>1))*40+(2*kp+s)*2+(T&1))*1024+lane*16,0,16);
        cs=__builtin_amdgcn_mfma_f32_16x16x32_bf16(__builtin_bit_cast(bf16x8,f[tt][s]),ones,cs,0,0,0); }
      if(r==0) __builtin_amdgcn_raw_buffer_store_b128(__builtin_bit_cast(u32x4,cs),rs,CSQ_OFF+(((qa*4+(T>>1))*2+(T&1))*16+kp)*64+g4*16,0,16); }
  }
  if(xjob){
    const u32x4 xv[5]={xv0,xv1,xv2,xv3,xv4};
    float s=0.f, ss=0.f;
#pragma unroll
    for(int i=0;i<5;i++){
      __builtin_amdgcn_raw_buffer_store_b128(xv[i],rs,RR0+R_XIN+(xMT*20+5*xkq+i)*1024+lane*16,0,16);
#pragma unroll
      for(int e=0;e<4;e++){ float a=__uint_as_float(xv[i][e]<<16), c=__uint_as_float(xv[i][e]&0xffff0000u); s+=a+c; ss+=a*a+c*c; }
    }
    s+=__shfl_xor(s,16); s+=__shfl_xor(s,32); ss+=__shfl_xor(ss,16); ss+=__shfl_xor(ss,32);
    if(g4<2){ u32x2 o; o[0]=g4?0u:__float_as_uint(s); o[1]=g4?0u:__float_as_uint(ss);
      __builtin_amdgcn_raw_buffer_store_b64(o,rs,RR0+R_STIN+xMT*1024+(xkq+4*g4)*128+r*8,0,16); }
  }
  u32x2 tg; tg[0]=(unsigned)tag; tg[1]=(unsigned)(tag>>32);
  asm volatile("s_waitcnt vmcnt(0)":::"memory");
  __builtin_amdgcn_s_barrier();
  if(wv==0&&lane<5) __builtin_amdgcn_raw_buffer_store_b64(tg,rs,lane<4?RR0+R_FL+((3*2+(lane&1))*16+grp)*256+(lane>>1)*128+slot*8:PF_OFF+slot*128+grp*8,0,16);
  if(jt==1){
#pragma unroll
    for(int uu=0;uu<2;uu++){ u32x4 f[4][2];
#pragma unroll
      for(int t=0;t<4;t++)
#pragma unroll
        for(int s=0;s<2;s++) f[t][s]=trfrag(img+(t&1)*8192+(32*s+8*g4+q4)*128+64*uu+16*p4+8*(t>>1),4*128);
      const int u=2*ja+uu;
#pragma unroll
      for(int t=0;t<4;t++){ f32x4 cs={0.f,0.f,0.f,0.f};
#pragma unroll
        for(int s=0;s<2;s++){
          __builtin_amdgcn_raw_buffer_store_b128(f[t][s],rs,WG_OFF+(u*80+(2*jb+s)*4+t)*1024+lane*16,0,16);
          cs=__builtin_amdgcn_mfma_f32_16x16x32_bf16(__builtin_bit_cast(bf16x8,f[t][s]),ones,cs,0,0,0); }
        if(r==0) __builtin_amdgcn_raw_buffer_store_b128(__builtin_bit_cast(u32x4,cs),rs,CSG_OFF+((u*4+t)*16+jb)*64+g4*16,0,16); } }
  } else if(jt==2||jt==3){
    u32x4 f[2][5];
#pragma unroll
    for(int s_=0;s_<2;s_++)
#pragma unroll
      for(int T=0;T<5;T++) f[s_][T]=trfrag(img+(32*s_+8*g4+q4)*160+32*T+8*p4,4*160);
#pragma unroll
    for(int s_=0;s_<2;s_++){ const int s=2*jb+s_; unsigned dst;
      if(jt==2) dst=WO_OFF+((ja*4+s/5)*25+(s%5)*5)*1024;
      else { const int wq=s<14?0:(s<28?1:(s<41?2:3)), s0=wq==0?0:(wq==1?14:(wq==2?28:41)); dst=WD_OFF+((ja*4+wq)*70+(s-s0)*5)*1024; }
#pragma unroll
      for(int T=0;T<5;T++) __builtin_amdgcn_raw_buffer_store_b128(f[s_][T],rs,dst+T*1024+lane*16,0,16); }
  } else if(jt==4){
    u32x4 z={0u,0u,0u,0u};
#pragma unroll
    for(int f=0;f<10;f++) __builtin_amdgcn_raw_buffer_store_b128(z,rs,WD_OFF+((jid*4+2+f/5)*70+65+f%5)*1024+lane*16,0,16);
  }
  if(wv==0){
    for(unsigned spins=0;spins<(1u<<18);++spins){
      const u32x2 a=__builtin_amdgcn_raw_buffer_load_b64(rs,PF_OFF+slot*128+r*8,0,16);
      if(__all(a[0]==tg[0]&&a[1]==tg[1])) break;
      __builtin_amdgcn_s_sleep(1);
    }
  }
  asm volatile("s_waitcnt lgkmcnt(0)":::"memory");
  __builtin_amdgcn_s_barrier();
  {
    const void* ka=(const void*)__builtin_amdgcn_kernarg_segment_ptr();
    const unsigned tlo=__builtin_amdgcn_readfirstlane((unsigned)tag), thi=__builtin_amdgcn_readfirstlane((unsigned)(tag>>32));
    asm volatile(
      "s_load_dwordx2 s[16:17], %0, 56\n"
      "s_load_dwordx2 s[20:21], %0, 0\n"
      "s_load_dwordx2 s[24:25], %0, 48\n"
      "s_mov_b32 s36, %1\n"
      "s_mov_b32 s37, %2\n"
      "s_mov_b32 s79, %3\n"
      "s_mov_b32 s28, %4\n"
      "s_waitcnt lgkmcnt(0)\n"
      "s_and_b32 s17, s17, 0xffff\n"
      "s_mov_b32 s18, 28598272\n"
      "s_mov_b32 s19, 131072\n"
      "s_and_b32 s21, s21, 0xffff\n"
      "s_mov_b32 s22, 2621440\n"
      "s_mov_b32 s23, 131072\n"
      "s_and_b32 s25, s25, 0xffff\n"
      "s_mov_b32 s26, 2621440\n"
      "s_mov_b32 s27, 131072\n"
      "v_mbcnt_lo_u32_b32 v148, -1, 0\n"
      "v_mbcnt_hi_u32_b32 v148, -1, v148\n"
      "v_lshlrev_b32 v1, 4, v148\n"
      "s_and_b32 s90, s79, 7\n"
      "s_lshr_b32 s91, s79, 3\n"
      "s_lshr_b32 s92, s90, 1\n"
      "s_lshl_b32 s92, s92, 2\n"
      "s_lshr_b32 s93, s91, 3\n"
      "s_add_u32 s29, s92, s93\n"
      "s_and_b32 s92, s90, 1\n"
      "s_lshl_b32 s92, s92, 3\n"
      "s_and_b32 s93, s91, 7\n"
      "s_add_u32 s30, s92, s93\n"
      "s_lshl_b32 s92, s30, 2\n"
      "s_add_u32 s92, s92, s28\n"
      "s_mul_i32 s68, s92, 40960\n"
      "s_add_u32 s68, s68, 65536\n"
      "s_mov_b32 s35, s68\n"
      "buffer_load_dwordx4 a[0:3], v1, s[16:19], s35 offen sc1\n"
      "buffer_load_dwordx4 a[4:7], v1, s[16:19], s35 offen offset:1024 sc1\n"
      "buffer_load_dwordx4 a[8:11], v1, s[16:19], s35 offen offset:2048 sc1\n"
      "buffer_load_dwordx4 a[12:15], v1, s[16:19], s35 offen offset:3072 sc1\n"
      "s_add_u32 s35, s35, 4096\n"
      "buffer_load_dwordx4 a[16:19], v1, s[16:19], s35 offen sc1\n"
      "buffer_load_dwordx4 a[20:23], v1, s[16:19], s35 offen offset:1024 sc1\n"
      "buffer_load_dwordx4 a[24:27], v1, s[16:19], s35 offen offset:2048 sc1\n"
      "buffer_load_dwordx4 a[28:31], v1, s[16:19], s35 offen offset:3072 sc1\n"
      "s_add_u32 s35, s35, 4096\n"
      "buffer_load_dwordx4 a[32:35], v1, s[16:19], s35 offen sc1\n"
      "buffer_load_dwordx4 a[36:39], v1, s[16:19], s35 offen offset:1024 sc1\n"
      "buffer_load_dwordx4 a[40:43], v1, s[16:19], s35 offen offset:2048 sc1\n"
      "buffer_load_dwordx4 a[44:47], v1, s[16:19], s35 offen offset:3072 sc1\n"
      "s_add_u32 s35, s35, 4096\n"
      "buffer_load_dwordx4 a[48:51], v1, s[16:19], s35 offen sc1\n"
      "buffer_load_dwordx4 a[52:55], v1, s[16:19], s35 offen offset:1024 sc1\n"
      "buffer_load_dwordx4 a[56:59], v1, s[16:19], s35 offen offset:2048 sc1\n"
      "buffer_load_dwordx4 a[60:63], v1, s[16:19], s35 offen offset:3072 sc1\n"
      "s_add_u32 s35, s35, 4096\n"
      "buffer_load_dwordx4 a[64:67], v1, s[16:19], s35 offen sc1\n"
      "buffer_load_dwordx4 a[68:71], v1, s[16:19], s35 offen offset:1024 sc1\n"
      "buffer_load_dwordx4 a[72:75], v1, s[16:19], s35 offen offset:2048 sc1\n"
      "buffer_load_dwordx4 a[76:79], v1, s[16:19], s35 offen offset:3072 sc1\n"
      "s_add_u32 s35, s35, 4096\n"
      "buffer_load_dwordx4 a[80:83], v1, s[16:19], s35 offen sc1\n"
      "buffer_load_dwordx4 a[84:87], v1, s[16:19], s35 offen offset:1024 sc1\n"
      "buffer_load_dwordx4 a[88:91], v1, s[16:19], s35 offen offset:2048 sc1\n"
      "buffer_load_dwordx4 a[92:95], v1, s[16:19], s35 offen offset:3072 sc1\n"
      "v_and_b32 v149, 15, v148\n"
      "v_lshrrev_b32 v150, 4, v148\n"
      "v_lshlrev_b32 v19, 3, v149\n"
      "v_add_u32 v19, 256, v19\n"
      "v_and_b32 v151, 16, v148\n"
      "v_lshlrev_b32 v151, 4, v151\n"
      "v_sub_u32 v151, v19, v151\n"
      "s_cmp_eq_u32 s29, 0\n"
      "s_cselect_b64 vcc, -1, 0\n"
      "v_cndmask_b32 v20, v151, v19, vcc\n"
      "s_lshl_b32 s67, s29, 8\n"
      "s_sub_u32 s67, s67, 256\n"
      "s_mov_b32 s66, s67\n"
      "s_add_u32 s66, s66, 23027712\n"
      "s_add_u32 s67, s67, 23027712\n"
      "s_add_u32 s87, s66, 2777088\n"
      "buffer_load_dwordx2 v[30:31], v20, s[16:19], s87 offen sc1\n"
      "v_and_b32 v154, 7, v148\n"
      "v_lshrrev_b32 v155, 3, v148\n"
      "s_lshr_b32 s92, s30, 3\n"
      "s_lshl_b32 s92, s92, 2\n"
      "s_and_b32 s93, s30, 3\n"
      "s_add_u32 s76, s92, s93\n"
      "s_lshr_b32 s92, s30, 2\n"
      "s_and_b32 s77, s92, 1\n"
      "s_mov_b32 s34, 10575872\n"
      "s_mov_b32 s38, 0\n"
      "s_mov_b32 s42, 0\n"
      "s_mov_b32 s39, s79\n"
      "s_mov_b32 s78, 1065369472\n"
      "s_lshl_b32 s90, s29, 2\n"
      "s_add_u32 s91, s90, s28\n"
      "s_mul_i32 s56, s91, 20480\n"
      "s_lshl_b32 s64, s91, 10\n"
      "s_sub_u32 s91, s90, 1\n"
      "s_max_i32 s91, s91, 0\n"
      "s_lshl_b32 s65, s91, 10\n"
      "s_mul_i32 s91, s91, 20\n"
      "s_add_u32 s91, s91, s28\n"
      "s_lshl_b32 s57, s91, 10\n"
      "s_lshl_b32 s91, s77, 1\n"
      "s_add_u32 s91, s91, s90\n"
      "s_mul_i32 s92, s91, 20\n"
      "s_mul_i32 s93, s28, 5\n"
      "s_add_u32 s92, s92, s93\n"
      "s_lshl_b32 s58, s92, 10\n"
      "s_mul_i32 s93, s28, 14\n"
      "s_cmp_eq_u32 s28, 3\n"
      "s_cselect_b32 s31, 1, 0\n"
      "s_sub_u32 s93, s93, s31\n"
      "s_mul_i32 s92, s91, 54\n"
      "s_add_u32 s92, s92, s93\n"
      "s_lshl_b32 s59, s92, 10\n"
      "s_cmp_ge_u32 s28, 2\n"
      "s_cselect_b32 s31, 12, 13\n"
      "s_lshl_b32 s31, s31, 10\n"
      "s_add_u32 s60, s59, s31\n"
      "s_lshr_b32 s92, s28, 1\n"
      "s_add_u32 s92, s92, s91\n"
      "s_mul_i32 s62, s92, 20480\n"
      "s_lshl_b32 s63, s92, 10\n"
      "s_mov_b32 s86, s92\n"
      "s_and_b32 s92, s30, 7\n"
      "s_lshl_b32 s93, s92, 2\n"
      "s_add_u32 s93, s93, s28\n"
      "s_mul_i32 s31, s92, 3\n"
      "s_add_u32 s31, s31, 3\n"
      "s_min_u32 s48, s28, 2\n"
      "s_add_u32 s31, s31, s48\n"
      "s_cmp_lt_u32 s92, 3\n"
      "s_cselect_b32 s85, s93, s31\n"
      "s_lshr_b32 s93, s30, 3\n"
      "s_mul_i32 s93, s93, 27\n"
      "s_add_u32 s85, s85, s93\n"
      "s_cmp_ge_u32 s92, 3\n"
      "s_cselect_b32 s31, 1, 0\n"
      "s_cmp_eq_u32 s28, 3\n"
      "s_cselect_b32 s48, s31, 0\n"
      "s_mul_i32 s93, s90, 54\n"
      "s_add_u32 s93, s93, s85\n"
      "s_lshl_b32 s61, s93, 10\n"
      "s_cmp_eq_u32 s48, 1\n"
      "s_cselect_b32 s61, 1073741824, s61\n"
      "s_lshl_b32 s92, s30, 2\n"
      "s_add_u32 s92, s92, s28\n"
      "s_lshl_b32 s72, s92, 11\n"
      "s_add_u32 s72, s72, 10223616\n"
      "s_lshl_b32 s92, s76, 2\n"
      "s_add_u32 s92, s92, s28\n"
      "s_mul_i32 s69, s92, 25600\n"
      "s_add_u32 s69, s69, 2686976\n"
      "s_mul_i32 s71, s92, 71680\n"
      "s_add_u32 s71, s71, 7929856\n"
      "s_mul_i32 s70, s85, 81920\n"
      "s_add_u32 s70, s70, 3506176\n"
      "s_cmp_eq_u32 s48, 1\n"
      "s_cselect_b32 s70, 1073741824, s70\n"
      "s_lshl_b32 s73, s85, 12\n"
      "s_add_u32 s73, s73, 10354688\n"
      "s_lshl_b32 s74, s28, 12\n"
      "s_lshl_b32 s75, s28, 10\n"
      "s_add_u32 s75, s75, 16384\n"
      "s_lshl_b32 s92, s79, 2\n"
      "s_add_u32 s92, s92, s28\n"
      "s_lshl_b32 s84, s92, 9\n"
      "s_add_u32 s84, s84, 28598272\n"
      "v_add_u32 v2, 61440, v1\n"
      "v_mul_u32_u24 v151, 280, v149\n"
      "v_lshl_add_u32 v151, v150, 3, v151\n"
      "s_lshl_b32 s92, s28, 6\n"
      "s_add_u32 s92, s92, 102400\n"
      "v_add_u32 v3, s92, v151\n"
      "s_mul_i32 s92, s28, 4480\n"
      "s_add_u32 s92, s92, 102400\n"
      "v_mul_u32_u24 v151, 280, v149\n"
      "v_lshl_add_u32 v151, v150, 4, v151\n"
      "s_add_u32 s93, s92, 2240\n"
      "v_add_u32 v4, s93, v151\n"
      "s_add_u32 s93, s92, 4480\n"
      "v_add_u32 v6, s93, v151\n"
      "v_and_b32 v152, 7, v149\n"
      "v_mul_u32_u24 v152, 280, v152\n"
      "v_lshl_add_u32 v152, v150, 4, v152\n"
      "s_add_u32 s93, s92, 6720\n"
      "v_add_u32 v5, s93, v152\n"
      "v_bfe_u32 v151, v148, 2, 2\n"
      "v_and_b32 v152, 3, v148\n"
      "v_lshl_add_u32 v153, v150, 2, v151\n"
      "v_mul_u32_u24 v153, 280, v153\n"
      "v_lshl_add_u32 v153, v152, 3, v153\n"
      "s_add_u32 s93, s92, 2240\n"
      "v_add_u32 v7, s93, v153\n"
      "v_and_b32 v153, 1, v150\n"
      "v_lshl_add_u32 v153, v153, 2, v151\n"
      "v_mul_u32_u24 v153, 280, v153\n"
      "v_lshl_add_u32 v153, v152, 3, v153\n"
      "s_add_u32 s93, s92, 6720\n"
      "v_add_u32 v8, s93, v153\n"
      "v_cmp_gt_u32 vcc, 3, v150\n"
      "s_nop 1\n"
      "v_cndmask_b32 v9, 0, -1, vcc\n"
      "v_cmp_gt_u32 vcc, 2, v150\n"
      "s_nop 1\n"
      "v_cndmask_b32 v10, 0, -1, vcc\n"
      "v_mov_b32 v157, 0x80000000\n"
      "s_mul_i32 s92, s30, 5\n"
      "v_add_u32 v151, s92, v150\n"
      "v_lshrrev_b32 v152, 2, v151\n"
      "v_and_b32 v151, 3, v151\n"
      "v_lshlrev_b32 v152, 10, v152\n"
      "v_lshl_add_u32 v152, v151, 8, v152\n"
      "v_lshl_add_u32 v11, v149, 4, v152\n"
      "s_add_u32 s92, s92, 4\n"
      "s_lshr_b32 s93, s92, 2\n"
      "s_lshl_b32 s93, s93, 10\n"
      "s_and_b32 s92, s92, 3\n"
      "s_lshl_b32 s92, s92, 8\n"
      "s_add_u32 s92, s92, s93\n"
      "v_lshlrev_b32 v151, 4, v149\n"
      "v_add_u32 v151, s92, v151\n"
      "v_cmp_eq_u32 vcc, 0, v150\n"
      "s_nop 1\n"
      "v_cndmask_b32 v12, v157, v151, vcc\n"
      "s_mul_i32 s92, s28, 11520\n"
      "s_add_u32 s92, s92, 102400\n"
      "v_add_u32 v13, s92, v1\n"
      "s_and_b32 s92, s28, 1\n"
      "s_lshl_b32 s92, s92, 3\n"
      "v_add_u32 v156, s92, v154\n"
      "v_min_u32 v151, 4, v155\n"
      "v_mul_u32_u24 v151, 1152, v151\n"
      "v_lshl_add_u32 v151, v156, 4, v151\n"
      "s_lshr_b32 s92, s28, 1\n"
      "s_mul_i32 s92, s92, 5760\n"
      "s_add_u32 s92, s92, 102400\n"
      "v_add_u32 v14, s92, v151\n"
      "v_cmp_gt_u32 s[82:83], 40, v148\n"
      "s_mul_i32 s92, s76, 10\n"
      "v_lshl_add_u32 v151, v155, 1, s92\n"
      "v_lshrrev_b32 v152, 2, v151\n"
      "v_and_b32 v151, 3, v151\n"
      "v_lshlrev_b32 v152, 10, v152\n"
      "v_lshl_add_u32 v152, v151, 8, v152\n"
      "v_lshl_add_u32 v152, v156, 4, v152\n"
      "v_cndmask_b32 v15, v157, v152, s[82:83]\n"
      "s_lshl_b32 s92, s76, 7\n"
      "v_lshlrev_b32 v151, 3, v156\n"
      "v_add_u32 v151, s92, v151\n"
      "v_cmp_gt_u32 vcc, 8, v148\n"
      "s_nop 1\n"
      "v_cndmask_b32 v16, v157, v151, vcc\n"
      "v_lshlrev_b32 v151, 3, v149\n"
      "v_lshl_add_u32 v17, v150, 8, v151\n"
      "s_lshl_b32 s92, s86, 4\n"
      "v_add_u32 v151, s92, v156\n"
      "v_mul_u32_u24 v151, 2560, v151\n"
      "s_mul_i32 s92, s76, 160\n"
      "v_lshl_add_u32 v152, v155, 5, s92\n"
      "v_add_u32 v151, v151, v152\n"
      "v_cndmask_b32 v18, v157, v151, s[82:83]\n"
      "s_lshl_b32 s92, s30, 3\n"
      "s_add_u32 s92, s92, 256\n"
      "v_lshlrev_b32 v151, 7, v148\n"
      "v_add_u32 v151, s92, v151\n"
      "v_cmp_gt_u32 vcc, 1, v148\n"
      "s_nop 1\n"
      "v_cndmask_b32 v21, v157, v151, vcc\n"
      "s_cmp_eq_u32 s28, 0\n"
      "s_cselect_b64 vcc, -1, 0\n"
      "v_cndmask_b32 v21, v157, v21, vcc\n"
      "v_lshlrev_b32 v151, 6, v149\n"
      "v_lshl_add_u32 v151, v150, 4, v151\n"
      "v_cmp_gt_u32 vcc, 10, v149\n"
      "s_nop 1\n"
      "v_cndmask_b32 v23, v157, v151, vcc\n"
      "s_mul_i32 s92, s28, 384\n"
      "s_add_u32 s92, s92, 149760\n"
      "v_lshlrev_b32 v151, 4, v150\n"
      "v_add_u32 v24, s92, v151\n"
      "v_lshlrev_b32 v151, 3, v149\n"
      "v_add_u32 v25, 148480, v151\n"
      "s_lshl_b32 s92, s28, 7\n"
      "s_add_u32 s92, s92, 128\n"
      "v_add_u32 v26, s92, v25\n"
      "v_mov_b32 v28, s36\n"
      "v_mov_b32 v29, s37\n"
      "s_cmp_eq_u32 s29, 0\n"
      "s_cselect_b32 s92, 1, 0\n"
      "s_cmp_eq_u32 s28, 0\n"
      "s_cselect_b32 s92, s92, 0\n"
      "s_cmp_eq_u32 s92, 1\n"
      "s_cselect_b32 s94, 0, -1\n"
      "s_mov_b32 s95, -1\n"
      "v_lshl_add_u32 v151, v150, 2, -1\n"
      "v_sub_u32 v151, v151, v149\n"
      "v_cmp_gt_u32 s[40:41], 8, v151\n"
      "s_nop 2\n"
      "s_and_b64 s[40:41], s[40:41], s[94:95]\n"
      "v_mov_b32 v152, 0xf149f2ca\n"
      "v_cndmask_b32 v168, v152, 0, s[40:41]\n"
      "v_lshl_add_u32 v151, v150, 2, 0\n"
      "v_sub_u32 v151, v151, v149\n"
      "v_cmp_gt_u32 s[40:41], 8, v151\n"
      "s_nop 2\n"
      "s_and_b64 s[40:41], s[40:41], s[94:95]\n"
      "v_mov_b32 v152, 0xf149f2ca\n"
      "v_cndmask_b32 v169, v152, 0, s[40:41]\n"
      "v_lshl_add_u32 v151, v150, 2, 1\n"
      "v_sub_u32 v151, v151, v149\n"
      "v_cmp_gt_u32 s[40:41], 8, v151\n"
      "s_nop 2\n"
      "s_and_b64 s[40:41], s[40:41], s[94:95]\n"
      "v_mov_b32 v152, 0xf149f2ca\n"
      "v_cndmask_b32 v170, v152, 0, s[40:41]\n"
      "v_lshl_add_u32 v151, v150, 2, 2\n"
      "v_sub_u32 v151, v151, v149\n"
      "v_cmp_gt_u32 s[40:41], 8, v151\n"
      "s_nop 2\n"
      "s_and_b64 s[40:41], s[40:41], s[94:95]\n"
      "v_mov_b32 v152, 0xf149f2ca\n"
      "v_cndmask_b32 v171, v152, 0, s[40:41]\n"
      "v_lshl_add_u32 v151, v150, 2, 15\n"
      "v_sub_u32 v151, v151, v149\n"
      "v_cmp_gt_u32 s[40:41], 8, v151\n"
      "v_mov_b32 v152, 0xf149f2ca\n"
      "s_nop 0\n"
      "v_cndmask_b32 v172, v152, 0, s[40:41]\n"
      "v_lshl_add_u32 v151, v150, 2, 16\n"
      "v_sub_u32 v151, v151, v149\n"
      "v_cmp_gt_u32 s[40:41], 8, v151\n"
      "v_mov_b32 v152, 0xf149f2ca\n"
      "s_nop 0\n"
      "v_cndmask_b32 v173, v152, 0, s[40:41]\n"
      "v_lshl_add_u32 v151, v150, 2, 17\n"
      "v_sub_u32 v151, v151, v149\n"
      "v_cmp_gt_u32 s[40:41], 8, v151\n"
      "v_mov_b32 v152, 0xf149f2ca\n"
      "s_nop 0\n"
      "v_cndmask_b32 v174, v152, 0, s[40:41]\n"
      "v_lshl_add_u32 v151, v150, 2, 18\n"
      "v_sub_u32 v151, v151, v149\n"
      "v_cmp_gt_u32 s[40:41], 8, v151\n"
      "v_mov_b32 v152, 0xf149f2ca\n"
      "s_nop 0\n"
      "v_cndmask_b32 v175, v152, 0, s[40:41]\n"
      "s_add_u32 s44, s56, 23027712\n"
      "s_add_u32 s45, s64, 23027712\n"
      "s_add_u32 s46, s62, 23027712\n"
      "s_add_u32 s47, s63, 23027712\n"
      "s_add_u32 s57, s57, 23027712\n"
      "s_add_u32 s65, s65, 23027712\n"
      "buffer_load_dwordx4 v[32:35], v18, s[20:23], 0 offen\n"
      "buffer_load_dwordx4 v[36:39], v18, s[20:23], 0 offen offset:16\n"
      "buffer_load_dwordx4 v[40:43], v18, s[20:23], 0 offen offset:1280\n"
      "buffer_load_dwordx4 v[44:47], v18, s[20:23], 0 offen offset:1296\n"
      "buffer_load_dwordx4 v[48:51], v23, s[16:19], s72 offen sc1\n"
      "buffer_load_dwordx4 v[52:55], v23, s[16:19], s72 offen offset:1024 sc1\n"
      "s_waitcnt vmcnt(6)\n"
      "v_cmp_eq_u64 vcc, s[36:37], v[30:31]\n"
      "s_nop 3\n"
      "s_cmp_eq_u64 vcc, -1\n"
      "s_cbranch_scc1 Lpd_1\n"
      "Lps_2:\n"
      "s_sleep 1\n"
      "buffer_load_dwordx2 v[30:31], v20, s[16:19], s87 offen sc1\n"
      "s_waitcnt vmcnt(0)\n"
      "v_cmp_eq_u64 vcc, s[36:37], v[30:31]\n"
      "s_nop 3\n"
      "s_cmp_eq_u64 vcc, -1\n"
      "s_cbranch_scc1 Lpd_1\n"
      "s_add_u32 s42, s42, 1\n"
      "s_cmp_lt_u32 s42, 0x40000\n"
      "s_cbranch_scc1 Lps_2\n"
      "Lpd_1:\n"
      "s_add_u32 s98, s44, 0\n"
      "s_add_u32 m0, s74, 0\n"
      "s_nop 0\n"
      "buffer_load_dwordx4 v1, s[16:19], s98 offen sc1 lds\n"
      "buffer_load_dwordx4 v1, s[16:19], s98 offen offset:1024 sc1 lds\n"
      "buffer_load_dwordx4 v1, s[16:19], s98 offen offset:2048 sc1 lds\n"
      "buffer_load_dwordx4 v1, s[16:19], s98 offen offset:3072 sc1 lds\n"
      "s_add_u32 s98, s57, 0\n"
      "s_add_u32 m0, s75, 0\n"
      "s_nop 0\n"
      "buffer_load_dwordx4 v1, s[16:19], s98 offen sc1 lds\n"
      "s_add_u32 s98, s44, 4096\n"
      "s_add_u32 m0, s74, 20480\n"
      "s_nop 0\n"
      "buffer_load_dwordx4 v1, s[16:19], s98 offen sc1 lds\n"
      "buffer_load_dwordx4 v1, s[16:19], s98 offen offset:1024 sc1 lds\n"
      "buffer_load_dwordx4 v1, s[16:19], s98 offen offset:2048 sc1 lds\n"
      "buffer_load_dwordx4 v1, s[16:19], s98 offen offset:3072 sc1 lds\n"
      "s_add_u32 s98, s57, 4096\n"
      "s_add_u32 m0, s75, 20480\n"
      "s_nop 0\n"
      "buffer_load_dwordx4 v1, s[16:19], s98 offen sc1 lds\n"
      "s_waitcnt vmcnt(11)\n"
      "v_add_f32_dpp v48, v48, v48 row_ror:8 row_mask:0xf bank_mask:0xf\n"
      "v_add_f32_dpp v49, v49, v49 row_ror:8 row_mask:0xf bank_mask:0xf\n"
      "v_add_f32_dpp v50, v50, v50 row_ror:8 row_mask:0xf bank_mask:0xf\n"
      "v_add_f32_dpp v51, v51, v51 row_ror:8 row_mask:0xf bank_mask:0xf\n"
      "s_waitcnt vmcnt(10)\n"
      "v_add_f32_dpp v52, v52, v52 row_ror:8 row_mask:0xf bank_mask:0xf\n"
      "v_add_f32_dpp v53, v53, v53 row_ror:8 row_mask:0xf bank_mask:0xf\n"
      "v_add_f32_dpp v54, v54, v54 row_ror:8 row_mask:0xf bank_mask:0xf\n"
      "v_add_f32_dpp v55, v55, v55 row_ror:8 row_mask:0xf bank_mask:0xf\n"
      "v_add_f32_dpp v48, v48, v48 row_ror:4 row_mask:0xf bank_mask:0xf\n"
      "v_add_f32_dpp v49, v49, v49 row_ror:4 row_mask:0xf bank_mask:0xf\n"
      "v_add_f32_dpp v50, v50, v50 row_ror:4 row_mask:0xf bank_mask:0xf\n"
      "v_add_f32_dpp v51, v51, v51 row_ror:4 row_mask:0xf bank_mask:0xf\n"
      "v_add_f32_dpp v52, v52, v52 row_ror:4 row_mask:0xf bank_mask:0xf\n"
      "v_add_f32_dpp v53, v53, v53 row_ror:4 row_mask:0xf bank_mask:0xf\n"
      "v_add_f32_dpp v54, v54, v54 row_ror:4 row_mask:0xf bank_mask:0xf\n"
      "v_add_f32_dpp v55, v55, v55 row_ror:4 row_mask:0xf bank_mask:0xf\n"
      "v_add_f32_dpp v48, v48, v48 row_ror:2 row_mask:0xf bank_mask:0xf\n"
      "v_add_f32_dpp v49, v49, v49 row_ror:2 row_mask:0xf bank_mask:0xf\n"
      "v_add_f32_dpp v50, v50, v50 row_ror:2 row_mask:0xf bank_mask:0xf\n"
      "v_add_f32_dpp v51, v51, v51 row_ror:2 row_mask:0xf bank_mask:0xf\n"
      "v_add_f32_dpp v52, v52, v52 row_ror:2 row_mask:0xf bank_mask:0xf\n"
      "v_add_f32_dpp v53, v53, v53 row_ror:2 row_mask:0xf bank_mask:0xf\n"
      "v_add_f32_dpp v54, v54, v54 row_ror:2 row_mask:0xf bank_mask:0xf\n"
      "v_add_f32_dpp v55, v55, v55 row_ror:2 row_mask:0xf bank_mask:0xf\n"
      "v_add_f32_dpp v48, v48, v48 row_ror:1 row_mask:0xf bank_mask:0xf\n"
      "v_add_f32_dpp v49, v49, v49 row_ror:1 row_mask:0xf bank_mask:0xf\n"
      "v_add_f32_dpp v50, v50, v50 row_ror:1 row_mask:0xf bank_mask:0xf\n"
      "v_add_f32_dpp v51, v51, v51 row_ror:1 row_mask:0xf bank_mask:0xf\n"
      "v_add_f32_dpp v52, v52, v52 row_ror:1 row_mask:0xf bank_mask:0xf\n"
      "v_add_f32_dpp v53, v53, v53 row_ror:1 row_mask:0xf bank_mask:0xf\n"
      "v_add_f32_dpp v54, v54, v54 row_ror:1 row_mask:0xf bank_mask:0xf\n"
      "v_add_f32_dpp v55, v55, v55 row_ror:1 row_mask:0xf bank_mask:0xf\n"
      "ds_write_b128 v24, v[48:51]\n"
      "ds_write_b128 v24, v[52:55] offset:64\n"
      "LOOP:\n"
      "; ==== Q0\n"
      "s_waitcnt lgkmcnt(0)\n"
      "s_waitcnt vmcnt(5)\n"
      "s_barrier\n"
      "ds_read_b128 v[128:131], v1\n"
      "ds_read_b128 v[132:135], v1 offset:4096\n"
      "ds_read_b128 v[136:139], v1 offset:8192\n"
      "ds_read_b128 v[140:143], v1 offset:12288\n"
      "ds_read_b128 v[144:147], v1 offset:16384\n"
      "s_waitcnt lgkmcnt(4)\n"
      "v_mfma_f32_16x16x32_bf16 v[48:51], a[0:3], v[128:131], 0\n"
      "s_barrier\n"
      "v_lshlrev_b32 v244, 7, v1\n"
      "s_lshl_b32 s76, s39, 3\n"
      "v_add_u32 v244, s76, v244\n"
      "v_cmp_gt_u32 vcc, 128, v1\n"
      "v_mov_b32 v245, 0x80000000\n"
      "s_nop 0\n"
      "v_cndmask_b32 v244, v245, v244, vcc\n"
      "s_cmp_eq_u32 s28, 0\n"
      "s_cselect_b32 s76, 20480, 1073741824\n"
      "buffer_store_dwordx2 v[28:29], v244, s[16:19], s76 offen sc1\n"
      "v_mfma_f32_16x16x32_bf16 v[52:55], a[4:7], v[128:131], 0\n"
      "ds_read_b128 v[128:131], v1 offset:1024\n"
      "s_waitcnt lgkmcnt(4)\n"
      "v_mfma_f32_16x16x32_bf16 v[56:59], a[0:3], v[132:135], 0\n"
      "v_mfma_f32_16x16x32_bf16 v[60:63], a[4:7], v[132:135], 0\n"
      "ds_read_b128 v[132:135], v1 offset:5120\n"
      "s_waitcnt lgkmcnt(4)\n"
      "v_mfma_f32_16x16x32_bf16 v[64:67], a[0:3], v[136:139], 0\n"
      "s_add_u32 s35, s35, 4096\n"
      "buffer_load_dwordx4 a[96:99], v1, s[16:19], s35 offen sc1\n"
      "v_mfma_f32_16x16x32_bf16 v[68:71], a[4:7], v[136:139], 0\n"
      "ds_read_b128 v[136:139], v1 offset:9216\n"
      "s_waitcnt lgkmcnt(4)\n"
      "v_mfma_f32_16x16x32_bf16 v[72:75], a[0:3], v[140:143], 0\n"
      "v_mfma_f32_16x16x32_bf16 v[76:79], a[4:7], v[140:143], 0\n"
      "ds_read_b128 v[140:143], v1 offset:13312\n"
      "s_waitcnt lgkmcnt(4)\n"
      "v_mfma_f32_16x16x32_bf16 v[80:83], a[0:3], v[144:147], 0\n"
      "s_add_u32 s98, s44, 8192\n"
      "s_add_u32 m0, s74, 40960\n"
      "s_nop 0\n"
      "buffer_load_dwordx4 v1, s[16:19], s98 offen sc1 lds\n"
      "v_mfma_f32_16x16x32_bf16 v[84:87], a[4:7], v[144:147], 0\n"
      "ds_read_b128 v[144:147], v1 offset:17408\n"
      "s_waitcnt lgkmcnt(4)\n"
      "v_mfma_f32_16x16x32_bf16 v[48:51], a[8:11], v[128:131], v[48:51]\n"
      "v_mfma_f32_16x16x32_bf16 v[52:55], a[12:15], v[128:131], v[52:55]\n"
      "ds_read_b128 v[128:131], v1 offset:2048\n"
      "s_waitcnt lgkmcnt(4)\n"
      "v_mfma_f32_16x16x32_bf16 v[56:59], a[8:11], v[132:135], v[56:59]\n"
      "buffer_load_dwordx4 v1, s[16:19], s98 offen offset:1024 sc1 lds\n"
      "v_mfma_f32_16x16x32_bf16 v[60:63], a[12:15], v[132:135], v[60:63]\n"
      "s_barrier\n"
      "s_add_u32 s76, s67, 2781184\n"
      "buffer_store_dwordx2 v[28:29], v21, s[16:19], s76 offen sc1\n"
      "ds_read_b128 v[132:135], v1 offset:6144\n"
      "s_waitcnt lgkmcnt(4)\n"
      "v_mfma_f32_16x16x32_bf16 v[64:67], a[8:11], v[136:139], v[64:67]\n"
      "v_mfma_f32_16x16x32_bf16 v[68:71], a[12:15], v[136:139], v[68:71]\n"
      "ds_read_b128 v[136:139], v1 offset:10240\n"
      "s_waitcnt lgkmcnt(4)\n"
      "v_mfma_f32_16x16x32_bf16 v[72:75], a[8:11], v[140:143], v[72:75]\n"
      "v_mfma_f32_16x16x32_bf16 v[76:79], a[12:15], v[140:143], v[76:79]\n"
      "buffer_load_dwordx4 v1, s[16:19], s98 offen offset:2048 sc1 lds\n"
      "ds_read_b128 v[140:143], v1 offset:14336\n"
      "s_waitcnt lgkmcnt(4)\n"
      "v_mfma_f32_16x16x32_bf16 v[80:83], a[8:11], v[144:147], v[80:83]\n"
      "v_mfma_f32_16x16x32_bf16 v[84:87], a[12:15], v[144:147], v[84:87]\n"
      "ds_read_b128 v[144:147], v1 offset:18432\n"
      "s_waitcnt lgkmcnt(4)\n"
      "v_mfma_f32_16x16x32_bf16 v[48:51], a[16:19], v[128:131], v[48:51]\n"
      "v_mfma_f32_16x16x32_bf16 v[52:55], a[20:23], v[128:131], v[52:55]\n"
      "buffer_load_dwordx4 v1, s[16:19], s98 offen offset:3072 sc1 lds\n"
      "ds_read_b128 v[128:131], v1 offset:3072\n"
      "s_waitcnt lgkmcnt(4)\n"
      "v_mfma_f32_16x16x32_bf16 v[56:59], a[16:19], v[132:135], v[56:59]\n"
      "v_mfma_f32_16x16x32_bf16 v[60:63], a[20:23], v[132:135], v[60:63]\n"
      "ds_read_b128 v[132:135], v1 offset:7168\n"
      "s_waitcnt lgkmcnt(4)\n"
      "v_mfma_f32_16x16x32_bf16 v[64:67], a[16:19], v[136:139], v[64:67]\n"
      "v_mfma_f32_16x16x32_bf16 v[68:71], a[20:23], v[136:139], v[68:71]\n"
      "s_add_u32 s98, s57, 8192\n"
      "s_add_u32 m0, s75, 40960\n"
      "s_nop 0\n"
      "buffer_load_dwordx4 v1, s[16:19], s98 offen sc1 lds\n"
      "ds_read_b128 v[136:139], v1 offset:11264\n"
      "s_waitcnt lgkmcnt(4)\n"
      "v_mfma_f32_16x16x32_bf16 v[72:75], a[16:19], v[140:143], v[72:75]\n"
      "v_mfma_f32_16x16x32_bf16 v[76:79], a[20:23], v[140:143], v[76:79]\n"
      "ds_read_b128 v[140:143], v1 offset:15360\n"
      "s_waitcnt lgkmcnt(4)\n"
      "v_mfma_f32_16x16x32_bf16 v[80:83], a[16:19], v[144:147], v[80:83]\n"
      "v_mfma_f32_16x16x32_bf16 v[84:87], a[20:23], v[144:147], v[84:87]\n"
      "buffer_load_dwordx4 a[100:103], v1, s[16:19], s35 offen offset:1024 sc1\n"
      "ds_read_b128 v[144:147], v1 offset:19456\n"
      "s_waitcnt lgkmcnt(4)\n"
      "v_mfma_f32_16x16x32_bf16 v[48:51], a[24:27], v[128:131], v[48:51]\n"
      "v_mfma_f32_16x16x32_bf16 v[52:55], a[28:31], v[128:131], v[52:55]\n"
      "s_waitcnt lgkmcnt(3)\n"
      "v_mfma_f32_16x16x32_bf16 v[56:59], a[24:27], v[132:135], v[56:59]\n"
      "v_mfma_f32_16x16x32_bf16 v[60:63], a[28:31], v[132:135], v[60:63]\n"
      "s_add_u32 s77, s45, 2621440\n"
      "buffer_load_dwordx2 v[220:221], v17, s[16:19], s77 offen sc1\n"
      "buffer_load_dwordx2 v[222:223], v17, s[16:19], s77 offen offset:128 sc1\n"
      "s_waitcnt lgkmcnt(0)\n"
      "s_waitcnt vmcnt(11)\n"
      "s_barrier\n"
      "ds_read_b128 v[128:131], v1 offset:20480\n"
      "ds_read_b128 v[132:135], v1 offset:24576\n"
      "v_mfma_f32_16x16x32_bf16 v[64:67], a[24:27], v[136:139], v[64:67]\n"
      "s_add_u32 s77, s65, 2621440\n"
      "buffer_load_dwordx2 v[224:225], v17, s[16:19], s77 offen sc1\n"
      "buffer_load_dwordx2 v[226:227], v17, s[16:19], s77 offen offset:128 sc1\n"
      "v_mfma_f32_16x16x32_bf16 v[68:71], a[28:31], v[136:139], v[68:71]\n"
      "ds_read_b128 v[136:139], v1 offset:28672\n"
      "v_mfma_f32_16x16x32_bf16 v[72:75], a[24:27], v[140:143], v[72:75]\n"
      "v_mfma_f32_16x16x32_bf16 v[76:79], a[28:31], v[140:143], v[76:79]\n"
      "ds_read_b128 v[140:143], v1 offset:32768\n"
      "v_mfma_f32_16x16x32_bf16 v[80:83], a[24:27], v[144:147], v[80:83]\n"
      "buffer_load_dwordx4 a[104:107], v1, s[16:19], s35 offen offset:2048 sc1\n"
      "v_mfma_f32_16x16x32_bf16 v[84:87], a[28:31], v[144:147], v[84:87]\n"
      "ds_read_b128 v[144:147], v1 offset:36864\n"
      "s_waitcnt lgkmcnt(4)\n"
      "v_mfma_f32_16x16x32_bf16 v[48:51], a[32:35], v[128:131], v[48:51]\n"
      "v_mfma_f32_16x16x32_bf16 v[52:55], a[36:39], v[128:131], v[52:55]\n"
      "ds_read_b128 v[128:131], v1 offset:21504\n"
      "s_waitcnt lgkmcnt(4)\n"
      "v_mfma_f32_16x16x32_bf16 v[56:59], a[32:35], v[132:135], v[56:59]\n"
      "buffer_load_dwordx4 a[108:111], v1, s[16:19], s35 offen offset:3072 sc1\n"
      "v_mfma_f32_16x16x32_bf16 v[60:63], a[36:39], v[132:135], v[60:63]\n"
      "ds_read_b128 v[132:135], v1 offset:25600\n"
      "s_waitcnt lgkmcnt(4)\n"
      "v_mfma_f32_16x16x32_bf16 v[64:67], a[32:35], v[136:139], v[64:67]\n"
      "v_mfma_f32_16x16x32_bf16 v[68:71], a[36:39], v[136:139], v[68:71]\n"
      "ds_read_b128 v[136:139], v1 offset:29696\n"
      "s_waitcnt lgkmcnt(4)\n"
      "v_mfma_f32_16x16x32_bf16 v[72:75], a[32:35], v[140:143], v[72:75]\n"
      "s_add_u32 s35, s35, 4096\n"
      "buffer_load_dwordx4 a[112:115], v1, s[16:19], s35 offen sc1\n"
      "v_mfma_f32_16x16x32_bf16 v[76:79], a[36:39], v[140:143], v[76:79]\n"
      "ds_read_b128 v[140:143], v1 offset:33792\n"
      "s_waitcnt lgkmcnt(4)\n"
      "v_mfma_f32_16x16x32_bf16 v[80:83], a[32:35], v[144:147], v[80:83]\n"
      "v_mfma_f32_16x16x32_bf16 v[84:87], a[36:39], v[144:147], v[84:87]\n"
      "ds_read_b128 v[144:147], v1 offset:37888\n"
      "s_waitcnt lgkmcnt(4)\n"
      "v_mfma_f32_16x16x32_bf16 v[48:51], a[40:43], v[128:131], v[48:51]\n"
      "buffer_load_dwordx4 a[116:119], v1, s[16:19], s35 offen offset:1024 sc1\n"
      "v_mfma_f32_16x16x32_bf16 v[52:55], a[44:47], v[128:131], v[52:55]\n"
      "ds_read_b128 v[128:131], v1 offset:22528\n"
      "s_waitcnt lgkmcnt(4)\n"
      "v_mfma_f32_16x16x32_bf16 v[56:59], a[40:43], v[132:135], v[56:59]\n"
      "v_mfma_f32_16x16x32_bf16 v[60:63], a[44:47], v[132:135], v[60:63]\n"
      "ds_read_b128 v[132:135], v1 offset:26624\n"
      "s_waitcnt lgkmcnt(4)\n"
      "v_mfma_f32_16x16x32_bf16 v[64:67], a[40:43], v[136:139], v[64:67]\n"
      "buffer_load_dwordx4 a[120:123], v1, s[16:19], s35 offen offset:2048 sc1\n"
      "v_mfma_f32_16x16x32_bf16 v[68:71], a[44:47], v[136:139], v[68:71]\n"
      "ds_read_b128 v[136:139], v1 offset:30720\n"
      "s_waitcnt lgkmcnt(4)\n"
      "v_mfma_f32_16x16x32_bf16 v[72:75], a[40:43], v[140:143], v[72:75]\n"
      "v_mfma_f32_16x16x32_bf16 v[76:79], a[44:47], v[140:143], v[76:79]\n"
      "ds_read_b128 v[140:143], v1 offset:34816\n"
      "s_waitcnt lgkmcnt(4)\n"
      "v_mfma_f32_16x16x32_bf16 v[80:83], a[40:43], v[144:147], v[80:83]\n"
      "buffer_load_dwordx4 a[124:127], v1, s[16:19], s35 offen offset:3072 sc1\n"
      "v_mfma_f32_16x16x32_bf16 v[84:87], a[44:47], v[144:147], v[84:87]\n"
      "ds_read_b128 v[144:147], v1 offset:38912\n"
      "s_waitcnt lgkmcnt(4)\n"
      "v_mfma_f32_16x16x32_bf16 v[48:51], a[48:51], v[128:131], v[48:51]\n"
      "v_mfma_f32_16x16x32_bf16 v[52:55], a[52:55], v[128:131], v[52:55]\n"
      "ds_read_b128 v[128:131], v1 offset:23552\n"
      "s_waitcnt lgkmcnt(4)\n"
      "v_mfma_f32_16x16x32_bf16 v[56:59], a[48:51], v[132:135], v[56:59]\n"
      "s_add_u32 s35, s35, 4096\n"
      "buffer_load_dwordx4 a[128:131], v1, s[16:19], s35 offen sc1\n"
      "v_mfma_f32_16x16x32_bf16 v[60:63], a[52:55], v[132:135], v[60:63]\n"
      "ds_read_b128 v[132:135], v1 offset:27648\n"
      "s_waitcnt lgkmcnt(4)\n"
      "v_mfma_f32_16x16x32_bf16 v[64:67], a[48:51], v[136:139], v[64:67]\n"
      "v_mfma_f32_16x16x32_bf16 v[68:71], a[52:55], v[136:139], v[68:71]\n"
      "ds_read_b128 v[136:139], v1 offset:31744\n"
      "s_waitcnt lgkmcnt(4)\n"
      "v_mfma_f32_16x16x32_bf16 v[72:75], a[48:51], v[140:143], v[72:75]\n"
      "s_add_u32 s98, s44, 12288\n"
      "s_add_u32 m0, s74, 61440\n"
      "s_nop 0\n"
      "buffer_load_dwordx4 v1, s[16:19], s98 offen sc1 lds\n"
      "v_mfma_f32_16x16x32_bf16 v[76:79], a[52:55], v[140:143], v[76:79]\n"
      "ds_read_b128 v[140:143], v1 offset:35840\n"
      "s_waitcnt lgkmcnt(4)\n"
      "v_mfma_f32_16x16x32_bf16 v[80:83], a[48:51], v[144:147], v[80:83]\n"
      "v_mfma_f32_16x16x32_bf16 v[84:87], a[52:55], v[144:147], v[84:87]\n"
      "ds_read_b128 v[144:147], v1 offset:39936\n"
      "s_waitcnt lgkmcnt(4)\n"
      "v_mfma_f32_16x16x32_bf16 v[48:51], a[56:59], v[128:131], v[48:51]\n"
      "buffer_load_dwordx4 v1, s[16:19], s98 offen offset:1024 sc1 lds\n"
      "v_mfma_f32_16x16x32_bf16 v[52:55], a[60:63], v[128:131], v[52:55]\n"
      "s_waitcnt lgkmcnt(3)\n"
      "v_mfma_f32_16x16x32_bf16 v[56:59], a[56:59], v[132:135], v[56:59]\n"
      "v_mfma_f32_16x16x32_bf16 v[60:63], a[60:63], v[132:135], v[60:63]\n"
      "s_waitcnt lgkmcnt(0)\n"
      "s_waitcnt vmcnt(14)\n"
      "s_barrier\n"
      "ds_read_b128 v[128:131], v1 offset:40960\n"
      "ds_read_b128 v[132:135], v1 offset:45056\n"
      "v_mfma_f32_16x16x32_bf16 v[64:67], a[56:59], v[136:139], v[64:67]\n"
      "buffer_load_dwordx4 v1, s[16:19], s98 offen offset:2048 sc1 lds\n"
      "v_mfma_f32_16x16x32_bf16 v[68:71], a[60:63], v[136:139], v[68:71]\n"
      "ds_read_b128 v[136:139], v1 offset:49152\n"
      "v_mfma_f32_16x16x32_bf16 v[72:75], a[56:59], v[140:143], v[72:75]\n"
      "v_mfma_f32_16x16x32_bf16 v[76:79], a[60:63], v[140:143], v[76:79]\n"
      "ds_read_b128 v[140:143], v1 offset:53248\n"
      "v_mfma_f32_16x16x32_bf16 v[80:83], a[56:59], v[144:147], v[80:83]\n"
      "buffer_load_dwordx4 v1, s[16:19], s98 offen offset:3072 sc1 lds\n"
      "v_mfma_f32_16x16x32_bf16 v[84:87], a[60:63], v[144:147], v[84:87]\n"
      "ds_read_b128 v[144:147], v1 offset:57344\n"
      "s_waitcnt lgkmcnt(4)\n"
      "v_mfma_f32_16x16x32_bf16 v[48:51], a[64:67], v[128:131], v[48:51]\n"
      "v_mfma_f32_16x16x32_bf16 v[52:55], a[68:71], v[128:131], v[52:55]\n"
      "ds_read_b128 v[128:131], v1 offset:41984\n"
      "s_waitcnt lgkmcnt(4)\n"
      "v_mfma_f32_16x16x32_bf16 v[56:59], a[64:67], v[132:135], v[56:59]\n"
      "s_add_u32 s98, s57, 12288\n"
      "s_add_u32 m0, s75, 61440\n"
      "s_nop 0\n"
      "buffer_load_dwordx4 v1, s[16:19], s98 offen sc1 lds\n"
      "v_mfma_f32_16x16x32_bf16 v[60:63], a[68:71], v[132:135], v[60:63]\n"
      "ds_read_b128 v[132:135], v1 offset:46080\n"
      "s_waitcnt lgkmcnt(4)\n"
      "v_mfma_f32_16x16x32_bf16 v[64:67], a[64:67], v[136:139], v[64:67]\n"
      "v_mfma_f32_16x16x32_bf16 v[68:71], a[68:71], v[136:139], v[68:71]\n"
      "ds_read_b128 v[136:139], v1 offset:50176\n"
      "s_waitcnt lgkmcnt(4)\n"
      "v_mfma_f32_16x16x32_bf16 v[72:75], a[64:67], v[140:143], v[72:75]\n"
      "buffer_load_dwordx4 a[132:135], v1, s[16:19], s35 offen offset:1024 sc1\n"
      "v_mfma_f32_16x16x32_bf16 v[76:79], a[68:71], v[140:143], v[76:79]\n"
      "s_waitcnt vmcnt(15)\n"
      "v_add_f32 v220, v220, v222\n"
      "v_add_f32 v221, v221, v223\n"
      "ds_read_b128 v[140:143], v1 offset:54272\n"
      "s_waitcnt lgkmcnt(4)\n"
      "v_mfma_f32_16x16x32_bf16 v[80:83], a[64:67], v[144:147], v[80:83]\n"
      "v_mov_b32 v156, v220\n"
      "v_mov_b32 v157, v221\n"
      "v_mfma_f32_16x16x32_bf16 v[84:87], a[68:71], v[144:147], v[84:87]\n"
      "v_permlane16_swap_b32 v156, v220\n"
      "v_permlane16_swap_b32 v157, v221\n"
      "ds_read_b128 v[144:147], v1 offset:58368\n"
      "s_waitcnt lgkmcnt(4)\n"
      "v_mfma_f32_16x16x32_bf16 v[48:51], a[72:75], v[128:131], v[48:51]\n"
      "buffer_load_dwordx4 a[136:139], v1, s[16:19], s35 offen offset:2048 sc1\n"
      "v_add_f32 v220, v220, v156\n"
      "v_add_f32 v221, v221, v157\n"
      "v_mfma_f32_16x16x32_bf16 v[52:55], a[76:79], v[128:131], v[52:55]\n"
      "v_mov_b32 v156, v220\n"
      "v_mov_b32 v157, v221\n"
      "ds_read_b128 v[128:131], v1 offset:43008\n"
      "s_waitcnt lgkmcnt(4)\n"
      "v_mfma_f32_16x16x32_bf16 v[56:59], a[72:75], v[132:135], v[56:59]\n"
      "v_permlane32_swap_b32 v156, v220\n"
      "v_permlane32_swap_b32 v157, v221\n"
      "v_mfma_f32_16x16x32_bf16 v[60:63], a[76:79], v[132:135], v[60:63]\n"
      "v_add_f32 v220, v220, v156\n"
      "v_add_f32 v221, v221, v157\n"
      "ds_read_b128 v[132:135], v1 offset:47104\n"
      "s_waitcnt lgkmcnt(4)\n"
      "v_mfma_f32_16x16x32_bf16 v[64:67], a[72:75], v[136:139], v[64:67]\n"
      "buffer_load_dwordx4 a[140:143], v1, s[16:19], s35 offen offset:3072 sc1\n"
      "v_mul_f32 v222, 0x3acccccd, v220\n"
      "v_mul_f32 v221, 0x3acccccd, v221\n"
      "v_mfma_f32_16x16x32_bf16 v[68:71], a[76:79], v[136:139], v[68:71]\n"
      "v_sub_f32 v159, 0, v222\n"
      "v_fma_f32 v221, -v222, v222, v221\n"
      "ds_read_b128 v[136:139], v1 offset:51200\n"
      "s_waitcnt lgkmcnt(4)\n"
      "v_mfma_f32_16x16x32_bf16 v[72:75], a[72:75], v[140:143], v[72:75]\n"
      "v_max_f32 v221, 0, v221\n"
      "v_add_f32 v221, 0x3727c5ac, v221\n"
      "v_mfma_f32_16x16x32_bf16 v[76:79], a[76:79], v[140:143], v[76:79]\n"
      "v_rsq_f32 v158, v221\n"
      "ds_read_b128 v[140:143], v1 offset:55296\n"
      "s_waitcnt lgkmcnt(4)\n"
      "v_mfma_f32_16x16x32_bf16 v[80:83], a[72:75], v[144:147], v[80:83]\n"
      "s_add_u32 s35, s35, 4096\n"
      "buffer_load_dwordx4 a[144:147], v1, s[16:19], s35 offen sc1\n"
      "ds_write_b64 v26, v[158:159]\n"
      "s_waitcnt vmcnt(16)\n"
      "v_add_f32 v224, v224, v226\n"
      "v_mfma_f32_16x16x32_bf16 v[84:87], a[76:79], v[144:147], v[84:87]\n"
      "v_add_f32 v225, v225, v227\n"
      "v_mov_b32 v156, v224\n"
      "ds_read_b128 v[144:147], v1 offset:59392\n"
      "s_waitcnt lgkmcnt(5)\n"
      "v_mfma_f32_16x16x32_bf16 v[48:51], a[80:83], v[128:131], v[48:51]\n"
      "v_lshlrev_b32 v244, 1, v1\n"
      "s_and_b32 s87, s39, 7\n"
      "s_lshl_b32 s87, s87, 11\n"
      "s_add_u32 s87, s87, 20480\n"
      "buffer_load_dwordx4 v[236:239], v244, s[16:19], s87 offen sc1\n"
      "buffer_load_dwordx4 v[240:243], v244, s[16:19], s87 offen offset:16 sc1\n"
      "v_mov_b32 v157, v225\n"
      "v_permlane16_swap_b32 v156, v224\n"
      "v_mfma_f32_16x16x32_bf16 v[52:55], a[84:87], v[128:131], v[52:55]\n"
      "v_permlane16_swap_b32 v157, v225\n"
      "v_add_f32 v224, v224, v156\n"
      "ds_read_b128 v[128:131], v1 offset:44032\n"
      "s_waitcnt lgkmcnt(5)\n"
      "v_mfma_f32_16x16x32_bf16 v[56:59], a[80:83], v[132:135], v[56:59]\n"
      "v_add_f32 v225, v225, v157\n"
      "v_mov_b32 v156, v224\n"
      "v_mfma_f32_16x16x32_bf16 v[60:63], a[84:87], v[132:135], v[60:63]\n"
      "v_mov_b32 v157, v225\n"
      "v_permlane32_swap_b32 v156, v224\n"
      "ds_read_b128 v[132:135], v1 offset:48128\n"
      "s_waitcnt lgkmcnt(5)\n"
      "v_mfma_f32_16x16x32_bf16 v[64:67], a[80:83], v[136:139], v[64:67]\n"
      "buffer_load_dwordx4 a[148:151], v1, s[16:19], s35 offen offset:1024 sc1\n"
      "v_permlane32_swap_b32 v157, v225\n"
      "v_add_f32 v224, v224, v156\n"
      "v_mfma_f32_16x16x32_bf16 v[68:71], a[84:87], v[136:139], v[68:71]\n"
      "v_add_f32 v225, v225, v157\n"
      "v_mul_f32 v226, 0x3acccccd, v224\n"
      "ds_read_b128 v[136:139], v1 offset:52224\n"
      "s_waitcnt lgkmcnt(5)\n"
      "v_mfma_f32_16x16x32_bf16 v[72:75], a[80:83], v[140:143], v[72:75]\n"
      "v_mul_f32 v225, 0x3acccccd, v225\n"
      "v_sub_f32 v159, 0, v226\n"
      "v_mfma_f32_16x16x32_bf16 v[76:79], a[84:87], v[140:143], v[76:79]\n"
      "v_fma_f32 v225, -v226, v226, v225\n"
      "v_max_f32 v225, 0, v225\n"
      "ds_read_b128 v[140:143], v1 offset:56320\n"
      "s_waitcnt lgkmcnt(4)\n"
      "v_mfma_f32_16x16x32_bf16 v[80:83], a[80:83], v[144:147], v[80:83]\n"
      "buffer_load_dwordx4 a[152:155], v1, s[16:19], s35 offen offset:2048 sc1\n"
      "v_add_f32 v225, 0x3727c5ac, v225\n"
      "v_rsq_f32 v158, v225\n"
      "v_mfma_f32_16x16x32_bf16 v[84:87], a[84:87], v[144:147], v[84:87]\n"
      "ds_write_b64 v25, v[158:159]\n"
      "ds_read_b128 v[144:147], v1 offset:60416\n"
      "s_waitcnt lgkmcnt(5)\n"
      "v_mfma_f32_16x16x32_bf16 v[48:51], a[88:91], v[128:131], v[48:51]\n"
      "v_mfma_f32_16x16x32_bf16 v[52:55], a[92:95], v[128:131], v[52:55]\n"
      "s_waitcnt lgkmcnt(4)\n"
      "v_mfma_f32_16x16x32_bf16 v[56:59], a[88:91], v[132:135], v[56:59]\n"
      "buffer_load_dwordx4 a[156:159], v1, s[16:19], s35 offen offset:3072 sc1\n"
      "v_mfma_f32_16x16x32_bf16 v[60:63], a[92:95], v[132:135], v[60:63]\n"
      "s_waitcnt lgkmcnt(0)\n"
      "s_waitcnt vmcnt(9)\n"
      "s_barrier\n"
      "ds_read_b128 v[128:131], v2\n"
      "ds_read_b128 v[132:135], v2 offset:4096\n"
      "v_mfma_f32_16x16x32_bf16 v[64:67], a[88:91], v[136:139], v[64:67]\n"
      "s_add_u32 s98, s44, 16384\n"
      "s_add_u32 m0, s74, 81920\n"
      "s_nop 0\n"
      "buffer_load_dwordx4 v1, s[16:19], s98 offen sc1 lds\n"
      "v_mfma_f32_16x16x32_bf16 v[68:71], a[92:95], v[136:139], v[68:71]\n"
      "ds_read_b128 v[136:139], v2 offset:8192\n"
      "v_mfma_f32_16x16x32_bf16 v[72:75], a[88:91], v[140:143], v[72:75]\n"
      "v_mfma_f32_16x16x32_bf16 v[76:79], a[92:95], v[140:143], v[76:79]\n"
      "ds_read_b128 v[140:143], v2 offset:12288\n"
      "v_mfma_f32_16x16x32_bf16 v[80:83], a[88:91], v[144:147], v[80:83]\n"
      "buffer_load_dwordx4 v1, s[16:19], s98 offen offset:1024 sc1 lds\n"
      "v_mfma_f32_16x16x32_bf16 v[84:87], a[92:95], v[144:147], v[84:87]\n"
      "ds_read_b128 v[144:147], v2 offset:16384\n"
      "s_waitcnt lgkmcnt(4)\n"
      "v_mfma_f32_16x16x32_bf16 v[48:51], a[96:99], v[128:131], v[48:51]\n"
      "v_mfma_f32_16x16x32_bf16 v[52:55], a[100:103], v[128:131], v[52:55]\n"
      "ds_read_b128 v[128:131], v2 offset:1024\n"
      "s_waitcnt lgkmcnt(4)\n"
      "v_mfma_f32_16x16x32_bf16 v[56:59], a[96:99], v[132:135], v[56:59]\n"
      "buffer_load_dwordx4 v1, s[16:19], s98 offen offset:2048 sc1 lds\n"
      "v_mfma_f32_16x16x32_bf16 v[60:63], a[100:103], v[132:135], v[60:63]\n"
      "ds_read_b128 v[132:135], v2 offset:5120\n"
      "s_waitcnt lgkmcnt(4)\n"
      "v_mfma_f32_16x16x32_bf16 v[64:67], a[96:99], v[136:139], v[64:67]\n"
      "v_mfma_f32_16x16x32_bf16 v[68:71], a[100:103], v[136:139], v[68:71]\n"
      "ds_read_b128 v[136:139], v2 offset:9216\n"
      "s_waitcnt lgkmcnt(4)\n"
      "v_mfma_f32_16x16x32_bf16 v[72:75], a[96:99], v[140:143], v[72:75]\n"
      "buffer_load_dwordx4 v1, s[16:19], s98 offen offset:3072 sc1 lds\n"
      "v_mfma_f32_16x16x32_bf16 v[76:79], a[100:103], v[140:143], v[76:79]\n"
      "ds_read_b128 v[140:143], v2 offset:13312\n"
      "s_waitcnt lgkmcnt(4)\n"
      "v_mfma_f32_16x16x32_bf16 v[80:83], a[96:99], v[144:147], v[80:83]\n"
      "v_mfma_f32_16x16x32_bf16 v[84:87], a[100:103], v[144:147], v[84:87]\n"
      "ds_read_b128 v[144:147], v2 offset:17408\n"
      "s_waitcnt lgkmcnt(4)\n"
      "v_mfma_f32_16x16x32_bf16 v[48:51], a[104:107], v[128:131], v[48:51]\n"
      "s_add_u32 s98, s57, 16384\n"
      "s_add_u32 m0, s75, 81920\n"
      "s_nop 0\n"
      "buffer_load_dwordx4 v1, s[16:19], s98 offen sc1 lds\n"
      "v_mfma_f32_16x16x32_bf16 v[52:55], a[108:111], v[128:131], v[52:55]\n"
      "ds_read_b128 v[128:131], v2 offset:2048\n"
      "s_waitcnt lgkmcnt(4)\n"
      "v_mfma_f32_16x16x32_bf16 v[56:59], a[104:107], v[132:135], v[56:59]\n"
      "v_mfma_f32_16x16x32_bf16 v[60:63], a[108:111], v[132:135], v[60:63]\n"
      "ds_read_b128 v[132:135], v2 offset:6144\n"
      "s_waitcnt lgkmcnt(4)\n"
      "v_mfma_f32_16x16x32_bf16 v[64:67], a[104:107], v[136:139], v[64:67]\n"
      "v_mfma_f32_16x16x32_bf16 v[68:71], a[108:111], v[136:139], v[68:71]\n"
      "ds_read_b128 v[136:139], v2 offset:10240\n"
      "s_waitcnt lgkmcnt(4)\n"
      "v_mfma_f32_16x16x32_bf16 v[72:75], a[104:107], v[140:143], v[72:75]\n"
      "v_mfma_f32_16x16x32_bf16 v[76:79], a[108:111], v[140:143], v[76:79]\n"
      "ds_read_b128 v[140:143], v2 offset:14336\n"
      "s_waitcnt lgkmcnt(4)\n"
      "v_mfma_f32_16x16x32_bf16 v[80:83], a[104:107], v[144:147], v[80:83]\n"
      "v_mfma_f32_16x16x32_bf16 v[84:87], a[108:111], v[144:147], v[84:87]\n"
      "ds_read_b128 v[144:147], v2 offset:18432\n"
      "s_waitcnt lgkmcnt(4)\n"
      "v_mfma_f32_16x16x32_bf16 v[48:51], a[112:115], v[128:131], v[48:51]\n"
      "v_mfma_f32_16x16x32_bf16 v[52:55], a[116:119], v[128:131], v[52:55]\n"
      "ds_read_b128 v[128:131], v2 offset:3072\n"
      "s_waitcnt lgkmcnt(4)\n"
      "v_mfma_f32_16x16x32_bf16 v[56:59], a[112:115], v[132:135], v[56:59]\n"
      "v_mfma_f32_16x16x32_bf16 v[60:63], a[116:119], v[132:135], v[60:63]\n"
      "ds_read_b128 v[132:135], v2 offset:7168\n"
      "s_waitcnt lgkmcnt(4)\n"
      "v_mfma_f32_16x16x32_bf16 v[64:67], a[112:115], v[136:139], v[64:67]\n"
      "v_mfma_f32_16x16x32_bf16 v[68:71], a[116:119], v[136:139], v[68:71]\n"
      "ds_read_b128 v[136:139], v2 offset:11264\n"
      "s_waitcnt lgkmcnt(4)\n"
      "v_mfma_f32_16x16x32_bf16 v[72:75], a[112:115], v[140:143], v[72:75]\n"
      "v_mfma_f32_16x16x32_bf16 v[76:79], a[116:119], v[140:143], v[76:79]\n"
      "s_waitcnt vmcnt(8)\n"
      "v_cmp_eq_u64 vcc, s[36:37], v[236:237]\n"
      "v_cmp_eq_u64 s[94:95], s[36:37], v[238:239]\n"
      "s_nop 3\n"
      "s_and_b64 vcc, vcc, s[94:95]\n"
      "v_cmp_eq_u64 s[94:95], s[36:37], v[240:241]\n"
      "s_nop 3\n"
      "s_and_b64 vcc, vcc, s[94:95]\n"
      "v_cmp_eq_u64 s[94:95], s[36:37], v[242:243]\n"
      "s_nop 3\n"
      "s_and_b64 vcc, vcc, s[94:95]\n"
      "s_cmp_eq_u64 vcc, -1\n"
      "s_cbranch_scc1 Lpb_3\n"
      "Lpbs_4:\n"
      "s_sleep 1\n"
      "buffer_load_dwordx4 v[236:239], v244, s[16:19], s87 offen sc1\n"
      "buffer_load_dwordx4 v[240:243], v244, s[16:19], s87 offen offset:16 sc1\n"
      "s_waitcnt vmcnt(0)\n"
      "v_cmp_eq_u64 vcc, s[36:37], v[236:237]\n"
      "v_cmp_eq_u64 s[94:95], s[36:37], v[238:239]\n"
      "s_nop 3\n"
      "s_and_b64 vcc, vcc, s[94:95]\n"
      "v_cmp_eq_u64 s[94:95], s[36:37], v[240:241]\n"
      "s_nop 3\n"
      "s_and_b64 vcc, vcc, s[94:95]\n"
      "v_cmp_eq_u64 s[94:95], s[36:37], v[242:243]\n"
      "s_nop 3\n"
      "s_and_b64 vcc, vcc, s[94:95]\n"
      "s_cmp_eq_u64 vcc, -1\n"
      "s_cbranch_scc1 Lpb_3\n"
      "s_add_u32 s42, s42, 1\n"
      "s_cmp_lt_u32 s42, 0x40000\n"
      "s_cbranch_scc1 Lpbs_4\n"
      "Lpb_3:\n"
      "v_mov_b32 v245, 0x80000000\n"
      "v_cmp_eq_u32 vcc, 0, v1\n"
      "s_nop 1\n"
      "v_cndmask_b32 v244, v245, 0, vcc\n"
      "s_or_b32 s76, s39, s28\n"
      "s_cmp_eq_u32 s76, 0\n"
      "s_cselect_b32 s76, 0, 1073741824\n"
      "buffer_store_dwordx2 v[28:29], v244, s[16:19], s76 offen sc1\n"
      "ds_read_b128 v[140:143], v2 offset:15360\n"
      "s_waitcnt lgkmcnt(4)\n"
      "v_mfma_f32_16x16x32_bf16 v[80:83], a[112:115], v[144:147], v[80:83]\n"
      "v_mfma_f32_16x16x32_bf16 v[84:87], a[116:119], v[144:147], v[84:87]\n"
      "ds_read_b128 v[144:147], v2 offset:19456\n"
      "s_waitcnt lgkmcnt(4)\n"
      "v_mfma_f32_16x16x32_bf16 v[48:51], a[120:123], v[128:131], v[48:51]\n"
      "v_mfma_f32_16x16x32_bf16 v[52:55], a[124:127], v[128:131], v[52:55]\n"
      "s_mov_b32 s31, s69\n"
      "buffer_load_dwordx4 a[160:163], v1, s[16:19], s31 offen sc1\n"
      "s_waitcnt lgkmcnt(3)\n"
      "v_mfma_f32_16x16x32_bf16 v[56:59], a[120:123], v[132:135], v[56:59]\n"
      "v_mfma_f32_16x16x32_bf16 v[60:63], a[124:127], v[132:135], v[60:63]\n"
      "s_waitcnt lgkmcnt(0)\n"
      "s_waitcnt vmcnt(2)\n"
      "s_barrier\n"
      "ds_read_b128 v[128:131], v2 offset:20480\n"
      "ds_read_b128 v[132:135], v2 offset:24576\n"
      "v_mfma_f32_16x16x32_bf16 v[64:67], a[120:123], v[136:139], v[64:67]\n"
      "buffer_load_dwordx4 a[164:167], v1, s[16:19], s31 offen offset:1024 sc1\n"
      "v_mfma_f32_16x16x32_bf16 v[68:71], a[124:127], v[136:139], v[68:71]\n"
      "ds_read_b128 v[136:139], v2 offset:28672\n"
      "v_mfma_f32_16x16x32_bf16 v[72:75], a[120:123], v[140:143], v[72:75]\n"
      "v_mfma_f32_16x16x32_bf16 v[76:79], a[124:127], v[140:143], v[76:79]\n"
      "ds_read_b128 v[140:143], v2 offset:32768\n"
      "v_mfma_f32_16x16x32_bf16 v[80:83], a[120:123], v[144:147], v[80:83]\n"
      "buffer_load_dwordx4 a[168:171], v1, s[16:19], s31 offen offset:2048 sc1\n"
      "v_mfma_f32_16x16x32_bf16 v[84:87], a[124:127], v[144:147], v[84:87]\n"
      "ds_read_b128 v[144:147], v2 offset:36864\n"
      "s_waitcnt lgkmcnt(4)\n"
      "v_mfma_f32_16x16x32_bf16 v[48:51], a[128:131], v[128:131], v[48:51]\n"
      "v_mfma_f32_16x16x32_bf16 v[52:55], a[132:135], v[128:131], v[52:55]\n"
      "ds_read_b128 v[128:131], v2 offset:21504\n"
      "s_waitcnt lgkmcnt(4)\n"
      "v_mfma_f32_16x16x32_bf16 v[56:59], a[128:131], v[132:135], v[56:59]\n"
      "buffer_load_dwordx4 a[172:175], v1, s[16:19], s31 offen offset:3072 sc1\n"
      "v_mfma_f32_16x16x32_bf16 v[60:63], a[132:135], v[132:135], v[60:63]\n"
      "ds_read_b128 v[132:135], v2 offset:25600\n"
      "s_waitcnt lgkmcnt(4)\n"
      "v_mfma_f32_16x16x32_bf16 v[64:67], a[128:131], v[136:139], v[64:67]\n"
      "v_mfma_f32_16x16x32_bf16 v[68:71], a[132:135], v[136:139], v[68:71]\n"
      "ds_read_b128 v[136:139], v2 offset:29696\n"
      "s_waitcnt lgkmcnt(4)\n"
      "v_mfma_f32_16x16x32_bf16 v[72:75], a[128:131], v[140:143], v[72:75]\n"
      "s_add_u32 s31, s31, 4096\n"
      "buffer_load_dwordx4 a[176:179], v1, s[16:19], s31 offen sc1\n"
      "v_mfma_f32_16x16x32_bf16 v[76:79], a[132:135], v[140:143], v[76:79]\n"
      "ds_read_b128 v[140:143], v2 offset:33792\n"
      "s_waitcnt lgkmcnt(4)\n"
      "v_mfma_f32_16x16x32_bf16 v[80:83], a[128:131], v[144:147], v[80:83]\n"
      "v_mfma_f32_16x16x32_bf16 v[84:87], a[132:135], v[144:147], v[84:87]\n"
      "ds_read_b128 v[144:147], v2 offset:37888\n"
      "s_waitcnt lgkmcnt(4)\n"
      "v_mfma_f32_16x16x32_bf16 v[48:51], a[136:139], v[128:131], v[48:51]\n"
      "buffer_load_dwordx4 a[180:183], v1, s[16:19], s31 offen offset:1024 sc1\n"
      "v_mfma_f32_16x16x32_bf16 v[52:55], a[140:143], v[128:131], v[52:55]\n"
      "ds_read_b128 v[128:131], v2 offset:22528\n"
      "s_waitcnt lgkmcnt(4)\n"
      "v_mfma_f32_16x16x32_bf16 v[56:59], a[136:139], v[132:135], v[56:59]\n"
      "v_mfma_f32_16x16x32_bf16 v[60:63], a[140:143], v[132:135], v[60:63]\n"
      "ds_read_b128 v[132:135], v2 offset:26624\n"
      "s_waitcnt lgkmcnt(4)\n"
      "v_mfma_f32_16x16x32_bf16 v[64:67], a[136:139], v[136:139], v[64:67]\n"
      "buffer_load_dwordx4 a[184:187], v1, s[16:19], s31 offen offset:2048 sc1\n"
      "v_mfma_f32_16x16x32_bf16 v[68:71], a[140:143], v[136:139], v[68:71]\n"
      "ds_read_b128 v[136:139], v2 offset:30720\n"
      "s_waitcnt lgkmcnt(4)\n"
      "v_mfma_f32_16x16x32_bf16 v[72:75], a[136:139], v[140:143], v[72:75]\n"
      "v_mfma_f32_16x16x32_bf16 v[76:79], a[140:143], v[140:143], v[76:79]\n"
      "ds_read_b128 v[140:143], v2 offset:34816\n"
      "s_waitcnt lgkmcnt(4)\n"
      "v_mfma_f32_16x16x32_bf16 v[80:83], a[136:139], v[144:147], v[80:83]\n"
      "s_add_u32 s87, s66, 2781184\n"
      "buffer_load_dwordx2 v[30:31], v20, s[16:19], s87 offen sc1\n"
      "v_mfma_f32_16x16x32_bf16 v[84:87], a[140:143], v[144:147], v[84:87]\n"
      "ds_read_b128 v[144:147], v2 offset:38912\n"
      "s_waitcnt lgkmcnt(4)\n"
      "v_mfma_f32_16x16x32_bf16 v[48:51], a[144:147], v[128:131], v[48:51]\n"
      "v_mfma_f32_16x16x32_bf16 v[52:55], a[148:151], v[128:131], v[52:55]\n"
      "ds_read_b128 v[128:131], v2 offset:23552\n"
      "s_waitcnt lgkmcnt(4)\n"
      "v_mfma_f32_16x16x32_bf16 v[56:59], a[144:147], v[132:135], v[56:59]\n"
      "buffer_load_dwordx4 a[188:191], v1, s[16:19], s31 offen offset:3072 sc1\n"
      "v_mfma_f32_16x16x32_bf16 v[60:63], a[148:151], v[132:135], v[60:63]\n"
      "ds_read_b128 v[132:135], v2 offset:27648\n"
      "s_waitcnt lgkmcnt(4)\n"
      "v_mfma_f32_16x16x32_bf16 v[64:67], a[144:147], v[136:139], v[64:67]\n"
      "v_mfma_f32_16x16x32_bf16 v[68:71], a[148:151], v[136:139], v[68:71]\n"
      "ds_read_b128 v[136:139], v2 offset:31744\n"
      "s_waitcnt lgkmcnt(4)\n"
      "v_mfma_f32_16x16x32_bf16 v[72:75], a[144:147], v[140:143], v[72:75]\n"
      "s_add_u32 s31, s31, 4096\n"
      "buffer_load_dwordx4 a[192:195], v1, s[16:19], s31 offen sc1\n"
      "v_mfma_f32_16x16x32_bf16 v[76:79], a[148:151], v[140:143], v[76:79]\n"
      "ds_read_b128 v[140:143], v2 offset:35840\n"
      "s_waitcnt lgkmcnt(4)\n"
      "v_mfma_f32_16x16x32_bf16 v[80:83], a[144:147], v[144:147], v[80:83]\n"
      "v_mfma_f32_16x16x32_bf16 v[84:87], a[148:151], v[144:147], v[84:87]\n"
      "ds_read_b128 v[144:147], v2 offset:39936\n"
      "s_waitcnt lgkmcnt(4)\n"
      "v_mfma_f32_16x16x32_bf16 v[48:51], a[152:155], v[128:131], v[48:51]\n"
      "buffer_load_dwordx4 a[196:199], v1, s[16:19], s31 offen offset:1024 sc1\n"
      "v_mfma_f32_16x16x32_bf16 v[52:55], a[156:159], v[128:131], v[52:55]\n"
      "s_waitcnt lgkmcnt(3)\n"
      "v_mfma_f32_16x16x32_bf16 v[56:59], a[152:155], v[132:135], v[56:59]\n"
      "v_mfma_f32_16x16x32_bf16 v[60:63], a[156:159], v[132:135], v[60:63]\n"
      "s_waitcnt lgkmcnt(2)\n"
      "v_mfma_f32_16x16x32_bf16 v[64:67], a[152:155], v[136:139], v[64:67]\n"
      "buffer_load_dwordx4 a[200:203], v1, s[16:19], s31 offen offset:2048 sc1\n"
      "v_mfma_f32_16x16x32_bf16 v[68:71], a[156:159], v[136:139], v[68:71]\n"
      "s_waitcnt lgkmcnt(1)\n"
      "v_mfma_f32_16x16x32_bf16 v[72:75], a[152:155], v[140:143], v[72:75]\n"
      "v_mfma_f32_16x16x32_bf16 v[76:79], a[156:159], v[140:143], v[76:79]\n"
      "s_waitcnt lgkmcnt(0)\n"
      "v_mfma_f32_16x16x32_bf16 v[80:83], a[152:155], v[144:147], v[80:83]\n"
      "buffer_load_dwordx4 a[204:207], v1, s[16:19], s31 offen offset:3072 sc1\n"
      "v_mfma_f32_16x16x32_bf16 v[84:87], a[156:159], v[144:147], v[84:87]\n"
      "s_waitcnt lgkmcnt(0)\n"
      "s_barrier\n"
      "s_add_u32 s31, s31, 4096\n"
      "buffer_load_dwordx4 a[208:211], v1, s[16:19], s31 offen sc1\n"
      "ds_read_b64 v[148:149], v25\n"
      "ds_read_b64 v[150:151], v25 offset:128\n"
      "ds_read_b64 v[152:153], v25 offset:256\n"
      "ds_read_b64 v[154:155], v25 offset:384\n"
      "ds_read_b64 v[156:157], v25 offset:512\n"
      "buffer_load_dwordx4 a[212:215], v1, s[16:19], s31 offen offset:1024 sc1\n"
      "ds_read_b128 v[158:161], v24\n"
      "ds_read_b128 v[162:165], v24 offset:64\n"
      "ds_read_b32 v88, v25 offset:4\n"
      "ds_read_b32 v90, v25 offset:132\n"
      "ds_read_b32 v92, v25 offset:260\n"
      "buffer_load_dwordx4 a[216:219], v1, s[16:19], s31 offen offset:2048 sc1\n"
      "ds_read_b32 v94, v25 offset:388\n"
      "ds_read_b32 v96, v25 offset:516\n"
      "s_waitcnt lgkmcnt(4)\n"
      "v_pk_fma_f32 v[80:81], v[158:159], v[88:89], v[80:81] op_sel_hi:[1,0,1]\n"
      "s_waitcnt lgkmcnt(3)\n"
      "v_pk_fma_f32 v[48:49], v[158:159], v[90:91], v[48:49] op_sel_hi:[1,0,1]\n"
      "v_pk_fma_f32 v[50:51], v[160:161], v[90:91], v[50:51] op_sel_hi:[1,0,1]\n"
      "v_pk_fma_f32 v[52:53], v[162:163], v[90:91], v[52:53] op_sel_hi:[1,0,1]\n"
      "v_pk_mul_f32 v[48:49], v[48:49], v[150:151] op_sel_hi:[1,0]\n"
      "v_pk_fma_f32 v[54:55], v[164:165], v[90:91], v[54:55] op_sel_hi:[1,0,1]\n"
      "v_pk_mul_f32 v[50:51], v[50:51], v[150:151] op_sel_hi:[1,0]\n"
      "s_waitcnt lgkmcnt(2)\n"
      "v_pk_fma_f32 v[56:57], v[158:159], v[92:93], v[56:57] op_sel_hi:[1,0,1]\n"
      "buffer_load_dwordx4 a[220:223], v1, s[16:19], s31 offen offset:3072 sc1\n"
      "v_pk_mul_f32 v[52:53], v[52:53], v[150:151] op_sel_hi:[1,0]\n"
      "v_cvt_pk_bf16_f32 v48, v48, v49\n"
      "v_pk_fma_f32 v[58:59], v[160:161], v[92:93], v[58:59] op_sel_hi:[1,0,1]\n"
      "v_pk_mul_f32 v[54:55], v[54:55], v[150:151] op_sel_hi:[1,0]\n"
      "v_cvt_pk_bf16_f32 v49, v50, v51\n"
      "v_pk_fma_f32 v[60:61], v[162:163], v[92:93], v[60:61] op_sel_hi:[1,0,1]\n"
      "v_pk_mul_f32 v[56:57], v[56:57], v[152:153] op_sel_hi:[1,0]\n"
      "v_cvt_pk_bf16_f32 v52, v52, v53\n"
      "v_pk_fma_f32 v[62:63], v[164:165], v[92:93], v[62:63] op_sel_hi:[1,0,1]\n"
      "v_pk_mul_f32 v[58:59], v[58:59], v[152:153] op_sel_hi:[1,0]\n"
      "v_cvt_pk_bf16_f32 v53, v54, v55\n"
      "ds_write_b64 v3, v[48:49] offset:4480\n"
      "s_add_u32 s31, s31, 4096\n"
      "buffer_load_dwordx4 a[224:227], v1, s[16:19], s31 offen sc1\n"
      "s_waitcnt lgkmcnt(2)\n"
      "v_pk_fma_f32 v[64:65], v[158:159], v[94:95], v[64:65] op_sel_hi:[1,0,1]\n"
      "v_pk_mul_f32 v[60:61], v[60:61], v[152:153] op_sel_hi:[1,0]\n"
      "v_cvt_pk_bf16_f32 v56, v56, v57\n"
      "v_pk_fma_f32 v[66:67], v[160:161], v[94:95], v[66:67] op_sel_hi:[1,0,1]\n"
      "v_pk_mul_f32 v[62:63], v[62:63], v[152:153] op_sel_hi:[1,0]\n"
      "v_cvt_pk_bf16_f32 v57, v58, v59\n"
      "ds_write_b64 v3, v[52:53] offset:4512\n"
      "v_pk_fma_f32 v[68:69], v[162:163], v[94:95], v[68:69] op_sel_hi:[1,0,1]\n"
      "v_pk_mul_f32 v[64:65], v[64:65], v[154:155] op_sel_hi:[1,0]\n"
      "buffer_load_dwordx4 a[228:231], v1, s[16:19], s31 offen offset:1024 sc1\n"
      "v_cvt_pk_bf16_f32 v60, v60, v61\n"
      "v_pk_fma_f32 v[70:71], v[164:165], v[94:95], v[70:71] op_sel_hi:[1,0,1]\n"
      "v_pk_mul_f32 v[66:67], v[66:67], v[154:155] op_sel_hi:[1,0]\n"
      "v_cvt_pk_bf16_f32 v61, v62, v63\n"
      "ds_write_b64 v3, v[56:57] offset:8960\n"
      "s_waitcnt lgkmcnt(3)\n"
      "v_pk_fma_f32 v[72:73], v[158:159], v[96:97], v[72:73] op_sel_hi:[1,0,1]\n"
      "v_pk_mul_f32 v[68:69], v[68:69], v[154:155] op_sel_hi:[1,0]\n"
      "v_cvt_pk_bf16_f32 v64, v64, v65\n"
      "s_waitcnt vmcnt(11)\n"
      "v_cmp_eq_u64 vcc, s[36:37], v[30:31]\n"
      "s_nop 3\n"
      "s_cmp_eq_u64 vcc, -1\n"
      "s_cbranch_scc1 Lpd_5\n"
      "Lps_6:\n"
      "s_sleep 1\n"
      "buffer_load_dwordx2 v[30:31], v20, s[16:19], s87 offen sc1\n"
      "s_waitcnt vmcnt(0)\n"
      "v_cmp_eq_u64 vcc, s[36:37], v[30:31]\n"
      "s_nop 3\n"
      "s_cmp_eq_u64 vcc, -1\n"
      "s_cbranch_scc1 Lpd_5\n"
      "s_add_u32 s42, s42, 1\n"
      "s_cmp_lt_u32 s42, 0x40000\n"
      "s_cbranch_scc1 Lps_6\n"
      "Lpd_5:\n"
      "s_add_u32 s98, s44, 1310720\n"
      "s_add_u32 m0, s74, 0\n"
      "s_nop 0\n"
      "buffer_load_dwordx4 v1, s[16:19], s98 offen sc1 lds\n"
      "v_pk_fma_f32 v[74:75], v[160:161], v[96:97], v[74:75] op_sel_hi:[1,0,1]\n"
      "v_pk_mul_f32 v[70:71], v[70:71], v[154:155] op_sel_hi:[1,0]\n"
      "v_cvt_pk_bf16_f32 v65, v66, v67\n"
      "ds_write_b64 v3, v[60:61] offset:8992\n"
      "v_pk_fma_f32 v[76:77], v[162:163], v[96:97], v[76:77] op_sel_hi:[1,0,1]\n"
      "v_pk_mul_f32 v[72:73], v[72:73], v[156:157] op_sel_hi:[1,0]\n"
      "buffer_load_dwordx4 v1, s[16:19], s98 offen offset:1024 sc1 lds\n"
      "v_cvt_pk_bf16_f32 v68, v68, v69\n"
      "v_pk_fma_f32 v[78:79], v[164:165], v[96:97], v[78:79] op_sel_hi:[1,0,1]\n"
      "v_pk_mul_f32 v[74:75], v[74:75], v[156:157] op_sel_hi:[1,0]\n"
      "v_cvt_pk_bf16_f32 v69, v70, v71\n"
      "ds_write_b64 v3, v[64:65] offset:13440\n"
      "v_pk_mul_f32 v[76:77], v[76:77], v[156:157] op_sel_hi:[1,0]\n"
      "buffer_load_dwordx4 v1, s[16:19], s98 offen offset:2048 sc1 lds\n"
      "v_cvt_pk_bf16_f32 v72, v72, v73\n"
      "v_pk_fma_f32 v[82:83], v[160:161], v[88:89], v[82:83] op_sel_hi:[1,0,1]\n"
      "v_pk_mul_f32 v[78:79], v[78:79], v[156:157] op_sel_hi:[1,0]\n"
      "v_cvt_pk_bf16_f32 v73, v74, v75\n"
      "ds_write_b64 v3, v[68:69] offset:13472\n"
      "v_pk_fma_f32 v[84:85], v[162:163], v[88:89], v[84:85] op_sel_hi:[1,0,1]\n"
      "buffer_load_dwordx4 v1, s[16:19], s98 offen offset:3072 sc1 lds\n"
      "v_pk_mul_f32 v[80:81], v[80:81], v[148:149] op_sel_hi:[1,0]\n"
      "v_cvt_pk_bf16_f32 v76, v76, v77\n"
      "v_pk_fma_f32 v[86:87], v[164:165], v[88:89], v[86:87] op_sel_hi:[1,0,1]\n"
      "v_pk_mul_f32 v[82:83], v[82:83], v[148:149] op_sel_hi:[1,0]\n"
      "v_cvt_pk_bf16_f32 v77, v78, v79\n"
      "ds_write_b64 v3, v[72:73] offset:17920\n"
      "s_add_u32 s98, s57, 1310720\n"
      "s_add_u32 m0, s75, 0\n"
      "s_nop 0\n"
      "buffer_load_dwordx4 v1, s[16:19], s98 offen sc1 lds\n"
      "v_pk_mul_f32 v[84:85], v[84:85], v[148:149] op_sel_hi:[1,0]\n"
      "v_cvt_pk_bf16_f32 v80, v80, v81\n"
      "v_pk_mul_f32 v[86:87], v[86:87], v[148:149] op_sel_hi:[1,0]\n"
      "v_cvt_pk_bf16_f32 v81, v82, v83\n"
      "ds_write_b64 v3, v[76:77] offset:17952\n"
      "v_cvt_pk_bf16_f32 v84, v84, v85\n"
      "s_add_u32 s98, s44, 1314816\n"
      "s_add_u32 m0, s74, 20480\n"
      "s_nop 0\n"
      "buffer_load_dwordx4 v1, s[16:19], s98 offen sc1 lds\n"
      "v_cvt_pk_bf16_f32 v85, v86, v87\n"
      "ds_write_b64 v3, v[80:81]\n"
      "ds_write_b64 v3, v[84:85] offset:32\n"
      "buffer_load_dwordx4 v1, s[16:19], s98 offen offset:1024 sc1 lds\n"
      "s_waitcnt lgkmcnt(0)\n"
      "s_barrier\n"
      "buffer_load_dwordx4 v1, s[16:19], s98 offen offset:2048 sc1 lds\n"
      "ds_read_b64 v[56:57], v6 offset:160\n"
      "ds_read_b64 v[58:59], v6 offset:168\n"
      "ds_read_b64 v[48:49], v4\n"
      "ds_read_b64 v[50:51], v4 offset:8\n"
      "ds_read_b64 v[52:53], v5\n"
      "buffer_load_dwordx4 v1, s[16:19], s98 offen offset:3072 sc1 lds\n"
      "ds_read_b64 v[54:55], v5 offset:8\n"
      "ds_read_b64 v[76:77], v6 offset:200\n"
      "ds_read_b64 v[78:79], v6 offset:208\n"
      "ds_read_b64 v[68:69], v4 offset:40\n"
      "ds_read_b64 v[70:71], v4 offset:48\n"
      "s_add_u32 s98, s57, 1314816\n"
      "s_add_u32 m0, s75, 20480\n"
      "s_nop 0\n"
      "buffer_load_dwordx4 v1, s[16:19], s98 offen sc1 lds\n"
      "ds_read_b64 v[72:73], v5 offset:40\n"
      "ds_read_b64 v[74:75], v5 offset:48\n"
      "s_mov_b32 s40, 0x3ea52b5c\n"
      "s_mov_b32 s41, 0x3ea52b5c\n"
      "s_waitcnt lgkmcnt(11)\n"
      "v_and_b32 v56, v56, v9\n"
      "v_and_b32 v57, v57, v9\n"
      "s_waitcnt lgkmcnt(10)\n"
      "v_and_b32 v58, v58, v10\n"
      "v_and_b32 v59, v59, v10\n"
      "s_waitcnt lgkmcnt(8)\n"
      "s_nop 0\n"
      "v_mfma_f32_16x16x32_bf16 v[60:63], v[48:51], v[56:59], 0\n"
      "s_add_u32 s98, s44, 1318912\n"
      "s_add_u32 m0, s74, 40960\n"
      "s_nop 0\n"
      "buffer_load_dwordx4 v1, s[16:19], s98 offen sc1 lds\n"
      "s_waitcnt lgkmcnt(5)\n"
      "v_and_b32 v76, v76, v9\n"
      "v_and_b32 v77, v77, v9\n"
      "s_waitcnt lgkmcnt(4)\n"
      "v_and_b32 v78, v78, v10\n"
      "v_and_b32 v79, v79, v10\n"
      "v_mfma_f32_16x16x32_bf16 v[64:67], v[52:55], v[56:59], 0\n"
      "s_waitcnt lgkmcnt(2)\n"
      "v_mfma_f32_16x16x32_bf16 v[80:83], v[68:71], v[76:79], 0\n"
      "s_waitcnt lgkmcnt(0)\n"
      "v_mfma_f32_16x16x32_bf16 v[84:87], v[72:75], v[76:79], 0\n"
      "buffer_load_dwordx4 v1, s[16:19], s98 offen offset:1024 sc1 lds\n"
      "ds_read_b64_tr_b16 v[48:49], v7 offset:80\n"
      "v_pk_fma_f32 v[60:61], v[60:61], s[40:41], v[168:169]\n"
      "v_pk_fma_f32 v[62:63], v[62:63], s[40:41], v[170:171]\n"
      "ds_read_b64_tr_b16 v[50:51], v8 offset:80\n"
      "v_max3_f32 v148, v60, v61, v62\n"
      "ds_read_b64_tr_b16 v[52:53], v7 offset:112\n"
      "ds_read_b64_tr_b16 v[54:55], v8 offset:112\n"
      "buffer_load_dwordx4 v1, s[16:19], s98 offen offset:2048 sc1 lds\n"
      "v_pk_fma_f32 v[64:65], v[64:65], s[40:41], v[172:173]\n"
      "v_pk_fma_f32 v[66:67], v[66:67], s[40:41], v[174:175]\n"
      "v_pk_fma_f32 v[80:81], v[80:81], s[40:41], v[168:169]\n"
      "v_pk_fma_f32 v[82:83], v[82:83], s[40:41], v[170:171]\n"
      "v_pk_fma_f32 v[84:85], v[84:85], s[40:41], v[172:173]\n"
      "v_pk_fma_f32 v[86:87], v[86:87], s[40:41], v[174:175]\n"
      "v_max3_f32 v154, v80, v81, v82\n"
      "v_max3_f32 v150, v63, v64, v65\n"
      "v_max3_f32 v148, v148, v66, v67\n"
      "v_max3_f32 v156, v83, v84, v85\n"
      "v_max3_f32 v154, v154, v86, v87\n"
      "v_max_f32 v148, v148, v150\n"
      "v_max_f32 v154, v154, v156\n"
      "buffer_load_dwordx4 v1, s[16:19], s98 offen offset:3072 sc1 lds\n"
      "v_mov_b32 v150, v148\n"
      "v_mov_b32 v156, v154\n"
      "s_nop 0\n"
      "v_permlane16_swap_b32 v150, v148\n"
      "v_permlane16_swap_b32 v156, v154\n"
      "ds_read_b64_tr_b16 v[68:69], v7 offset:120\n"
      "v_max_f32 v148, v148, v150\n"
      "v_max_f32 v154, v154, v156\n"
      "v_mov_b32 v150, v148\n"
      "v_mov_b32 v156, v154\n"
      "s_nop 0\n"
      "v_permlane32_swap_b32 v150, v148\n"
      "s_add_u32 s98, s57, 1318912\n"
      "s_add_u32 m0, s75, 40960\n"
      "s_nop 0\n"
      "buffer_load_dwordx4 v1, s[16:19], s98 offen sc1 lds\n"
      "v_permlane32_swap_b32 v156, v154\n"
      "ds_read_b64_tr_b16 v[70:71], v8 offset:120\n"
      "v_max_f32 v148, v148, v150\n"
      "v_max_f32 v154, v154, v156\n"
      "v_pk_add_f32 v[60:61], v[60:61], v[148:149] op_sel_hi:[1,0] neg_lo:[0,1] neg_hi:[0,1]\n"
      "v_pk_add_f32 v[62:63], v[62:63], v[148:149] op_sel_hi:[1,0] neg_lo:[0,1] neg_hi:[0,1]\n"
      "v_pk_add_f32 v[64:65], v[64:65], v[148:149] op_sel_hi:[1,0] neg_lo:[0,1] neg_hi:[0,1]\n"
      "v_pk_add_f32 v[66:67], v[66:67], v[148:149] op_sel_hi:[1,0] neg_lo:[0,1] neg_hi:[0,1]\n"
      "v_pk_add_f32 v[80:81], v[80:81], v[154:155] op_sel_hi:[1,0] neg_lo:[0,1] neg_hi:[0,1]\n"
      "v_pk_add_f32 v[82:83], v[82:83], v[154:155] op_sel_hi:[1,0] neg_lo:[0,1] neg_hi:[0,1]\n"
      "v_pk_add_f32 v[84:85], v[84:85], v[154:155] op_sel_hi:[1,0] neg_lo:[0,1] neg_hi:[0,1]\n"
      "s_add_u32 s98, s44, 1323008\n"
      "s_add_u32 m0, s74, 61440\n"
      "s_nop 0\n"
      "buffer_load_dwordx4 v1, s[16:19], s98 offen sc1 lds\n"
      "v_pk_add_f32 v[86:87], v[86:87], v[154:155] op_sel_hi:[1,0] neg_lo:[0,1] neg_hi:[0,1]\n"
      "v_exp_f32 v60, v60\n"
      "v_exp_f32 v61, v61\n"
      "v_exp_f32 v62, v62\n"
      "v_exp_f32 v63, v63\n"
      "v_exp_f32 v64, v64\n"
      "v_exp_f32 v65, v65\n"
      "v_exp_f32 v66, v66\n"
      "v_exp_f32 v67, v67\n"
      "buffer_load_dwordx4 v1, s[16:19], s98 offen offset:1024 sc1 lds\n"
      "v_exp_f32 v80, v80\n"
      "v_exp_f32 v81, v81\n"
      "v_exp_f32 v82, v82\n"
      "v_exp_f32 v83, v83\n"
      "v_exp_f32 v84, v84\n"
      "v_exp_f32 v85, v85\n"
      "v_exp_f32 v86, v86\n"
      "v_exp_f32 v87, v87\n"
      "buffer_load_dwordx4 v1, s[16:19], s98 offen offset:2048 sc1 lds\n"
      "v_pk_add_f32 v[152:153], v[60:61], v[62:63]\n"
      "v_pk_add_f32 v[148:149], v[64:65], v[66:67]\n"
      "v_pk_add_f32 v[158:159], v[80:81], v[82:83]\n"
      "v_pk_add_f32 v[154:155], v[84:85], v[86:87]\n"
      "v_pk_add_f32 v[152:153], v[152:153], v[148:149]\n"
      "v_pk_add_f32 v[158:159], v[158:159], v[154:155]\n"
      "ds_read_b64_tr_b16 v[72:73], v7 offset:152\n"
      "ds_read_b64_tr_b16 v[74:75], v8 offset:152\n"
      "v_add_f32 v152, v152, v153\n"
      "v_add_f32 v158, v158, v159\n"
      "buffer_load_dwordx4 v1, s[16:19], s98 offen offset:3072 sc1 lds\n"
      "v_mov_b32 v150, v152\n"
      "v_mov_b32 v156, v158\n"
      "s_nop 0\n"
      "v_permlane16_swap_b32 v150, v152\n"
      "v_permlane16_swap_b32 v156, v158\n"
      "v_add_f32 v152, v152, v150\n"
      "v_add_f32 v158, v158, v156\n"
      "v_mov_b32 v150, v152\n"
      "v_mov_b32 v156, v158\n"
      "s_nop 0\n"
      "v_permlane32_swap_b32 v150, v152\n"
      "v_permlane32_swap_b32 v156, v158\n"
      "v_add_f32 v152, v152, v150\n"
      "s_add_u32 s98, s57, 1323008\n"
      "s_add_u32 m0, s75, 61440\n"
      "s_nop 0\n"
      "buffer_load_dwordx4 v1, s[16:19], s98 offen sc1 lds\n"
      "v_add_f32 v158, v158, v156\n"
      "v_rcp_f32 v152, v152\n"
      "v_rcp_f32 v158, v158\n"
      "v_pk_mul_f32 v[60:61], v[60:61], v[152:153] op_sel_hi:[1,0]\n"
      "v_pk_mul_f32 v[62:63], v[62:63], v[152:153] op_sel_hi:[1,0]\n"
      "v_pk_mul_f32 v[64:65], v[64:65], v[152:153] op_sel_hi:[1,0]\n"
      "v_pk_mul_f32 v[66:67], v[66:67], v[152:153] op_sel_hi:[1,0]\n"
      "v_pk_mul_f32 v[80:81], v[80:81], v[158:159] op_sel_hi:[1,0]\n"
      "v_pk_mul_f32 v[82:83], v[82:83], v[158:159] op_sel_hi:[1,0]\n"
      "v_pk_mul_f32 v[84:85], v[84:85], v[158:159] op_sel_hi:[1,0]\n"
      "v_pk_mul_f32 v[86:87], v[86:87], v[158:159] op_sel_hi:[1,0]\n"
      "v_cvt_pk_bf16_f32 v56, v60, v61\n"
      "s_add_u32 s98, s44, 1327104\n"
      "s_add_u32 m0, s74, 81920\n"
      "s_nop 0\n"
      "buffer_load_dwordx4 v1, s[16:19], s98 offen sc1 lds\n"
      "v_cvt_pk_bf16_f32 v57, v62, v63\n"
      "v_cvt_pk_bf16_f32 v58, v64, v65\n"
      "v_cvt_pk_bf16_f32 v59, v66, v67\n"
      "v_cvt_pk_bf16_f32 v76, v80, v81\n"
      "v_cvt_pk_bf16_f32 v77, v82, v83\n"
      "v_cvt_pk_bf16_f32 v78, v84, v85\n"
      "v_cvt_pk_bf16_f32 v79, v86, v87\n"
      "s_waitcnt lgkmcnt(6)\n"
      "v_mfma_f32_16x16x32_bf16 v[60:63], v[48:51], v[56:59], 0\n"
      "s_waitcnt lgkmcnt(4)\n"
      "v_mfma_f32_16x16x32_bf16 v[64:67], v[52:55], v[56:59], 0\n"
      "buffer_load_dwordx4 v1, s[16:19], s98 offen offset:1024 sc1 lds\n"
      "s_waitcnt lgkmcnt(2)\n"
      "v_mfma_f32_16x16x32_bf16 v[80:83], v[68:71], v[76:79], 0\n"
      "s_waitcnt lgkmcnt(0)\n"
      "v_mfma_f32_16x16x32_bf16 v[84:87], v[72:75], v[76:79], 0\n"
      "s_nop 0\n"
      "v_cvt_pk_bf16_f32 v160, v60, v61\n"
      "v_cvt_pk_bf16_f32 v161, v62, v63\n"
      "v_cvt_pk_bf16_f32 v164, v64, v65\n"
      "v_cvt_pk_bf16_f32 v165, v66, v67\n"
      "s_nop 0\n"
      "v_cvt_pk_bf16_f32 v162, v80, v81\n"
      "v_cvt_pk_bf16_f32 v163, v82, v83\n"
      "buffer_load_dwordx4 v1, s[16:19], s98 offen offset:2048 sc1 lds\n"
      "v_cvt_pk_bf16_f32 v166, v84, v85\n"
      "v_cvt_pk_bf16_f32 v167, v86, v87\n"
      "s_add_u32 s80, s56, 10575872\n"
      "buffer_store_dwordx4 v[160:163], v11, s[16:19], s80 offen sc1\n"
      "buffer_store_dwordx4 v[164:167], v12, s[16:19], s80 offen sc1\n"
      "; ==== Q1\n"
      "s_waitcnt lgkmcnt(0)\n"
      "s_waitcnt vmcnt(20)\n"
      "s_barrier\n"
      "ds_read_b128 v[128:131], v1\n"
      "ds_read_b128 v[132:135], v1 offset:4096\n"
      "ds_read_b128 v[136:139], v1 offset:8192\n"
      "ds_read_b128 v[140:143], v1 offset:12288\n"
      "ds_read_b128 v[144:147], v1 offset:16384\n"
      "s_waitcnt lgkmcnt(4)\n"
      "v_mfma_f32_16x16x32_bf16 v[88:91], a[0:3], v[128:131], 0\n"
      "s_add_u32 s77, s45, 2686976\n"
      "buffer_load_dwordx2 v[228:229], v17, s[16:19], s77 offen sc1\n"
      "buffer_load_dwordx2 v[230:231], v17, s[16:19], s77 offen offset:128 sc1\n"
      "v_mfma_f32_16x16x32_bf16 v[92:95], a[4:7], v[128:131], 0\n"
      "ds_read_b128 v[128:131], v1 offset:1024\n"
      "s_waitcnt lgkmcnt(4)\n"
      "v_mfma_f32_16x16x32_bf16 v[96:99], a[0:3], v[132:135], 0\n"
      "v_mfma_f32_16x16x32_bf16 v[100:103], a[4:7], v[132:135], 0\n"
      "ds_read_b128 v[132:135], v1 offset:5120\n"
      "s_waitcnt lgkmcnt(4)\n"
      "v_mfma_f32_16x16x32_bf16 v[104:107], a[0:3], v[136:139], 0\n"
      "s_add_u32 s77, s65, 2686976\n"
      "buffer_load_dwordx2 v[232:233], v17, s[16:19], s77 offen sc1\n"
      "buffer_load_dwordx2 v[234:235], v17, s[16:19], s77 offen offset:128 sc1\n"
      "v_mfma_f32_16x16x32_bf16 v[108:111], a[4:7], v[136:139], 0\n"
      "ds_read_b128 v[136:139], v1 offset:9216\n"
      "s_waitcnt lgkmcnt(4)\n"
      "v_mfma_f32_16x16x32_bf16 v[112:115], a[0:3], v[140:143], 0\n"
      "v_mfma_f32_16x16x32_bf16 v[116:119], a[4:7], v[140:143], 0\n"
      "ds_read_b128 v[140:143], v1 offset:13312\n"
      "s_waitcnt lgkmcnt(4)\n"
      "v_mfma_f32_16x16x32_bf16 v[120:123], a[0:3], v[144:147], 0\n"
      "buffer_load_dwordx4 v[236:239], v23, s[16:19], s73 offen sc1\n"
      "v_mfma_f32_16x16x32_bf16 v[124:127], a[4:7], v[144:147], 0\n"
      "ds_read_b128 v[144:147], v1 offset:17408\n"
      "s_waitcnt lgkmcnt(4)\n"
      "v_mfma_f32_16x16x32_bf16 v[88:91], a[8:11], v[128:131], v[88:91]\n"
      "v_mfma_f32_16x16x32_bf16 v[92:95], a[12:15], v[128:131], v[92:95]\n"
      "ds_read_b128 v[128:131], v1 offset:2048\n"
      "s_waitcnt lgkmcnt(4)\n"
      "v_mfma_f32_16x16x32_bf16 v[96:99], a[8:11], v[132:135], v[96:99]\n"
      "buffer_load_dwordx4 v[240:243], v23, s[16:19], s73 offen offset:1024 sc1\n"
      "v_mfma_f32_16x16x32_bf16 v[100:103], a[12:15], v[132:135], v[100:103]\n"
      "ds_read_b128 v[132:135], v1 offset:6144\n"
      "s_waitcnt lgkmcnt(4)\n"
      "v_mfma_f32_16x16x32_bf16 v[104:107], a[8:11], v[136:139], v[104:107]\n"
      "v_mfma_f32_16x16x32_bf16 v[108:111], a[12:15], v[136:139], v[108:111]\n"
      "ds_read_b128 v[136:139], v1 offset:10240\n"
      "s_waitcnt lgkmcnt(4)\n"
      "v_mfma_f32_16x16x32_bf16 v[112:115], a[8:11], v[140:143], v[112:115]\n"
      "buffer_load_dwordx4 v[244:247], v23, s[16:19], s73 offen offset:2048 sc1\n"
      "v_mfma_f32_16x16x32_bf16 v[116:119], a[12:15], v[140:143], v[116:119]\n"
      "ds_read_b128 v[140:143], v1 offset:14336\n"
      "s_waitcnt lgkmcnt(4)\n"
      "v_mfma_f32_16x16x32_bf16 v[120:123], a[8:11], v[144:147], v[120:123]\n"
      "v_mfma_f32_16x16x32_bf16 v[124:127], a[12:15], v[144:147], v[124:127]\n"
      "ds_read_b128 v[144:147], v1 offset:18432\n"
      "s_waitcnt lgkmcnt(4)\n"
      "v_mfma_f32_16x16x32_bf16 v[88:91], a[16:19], v[128:131], v[88:91]\n"
      "buffer_load_dwordx4 v[248:251], v23, s[16:19], s73 offen offset:3072 sc1\n"
      "v_mfma_f32_16x16x32_bf16 v[92:95], a[20:23], v[128:131], v[92:95]\n"
      "ds_read_b128 v[128:131], v1 offset:3072\n"
      "s_waitcnt lgkmcnt(4)\n"
      "v_mfma_f32_16x16x32_bf16 v[96:99], a[16:19], v[132:135], v[96:99]\n"
      "v_mfma_f32_16x16x32_bf16 v[100:103], a[20:23], v[132:135], v[100:103]\n"
      "ds_read_b128 v[132:135], v1 offset:7168\n"
      "s_waitcnt lgkmcnt(4)\n"
      "v_mfma_f32_16x16x32_bf16 v[104:107], a[16:19], v[136:139], v[104:107]\n"
      "s_mov_b32 s88, s70\n"
      "buffer_load_dwordx4 a[4:7], v1, s[16:19], s88 offen sc1\n"
      "v_mfma_f32_16x16x32_bf16 v[108:111], a[20:23], v[136:139], v[108:111]\n"
      "ds_read_b128 v[136:139], v1 offset:11264\n"
      "s_waitcnt lgkmcnt(4)\n"
      "v_mfma_f32_16x16x32_bf16 v[112:115], a[16:19], v[140:143], v[112:115]\n"
      "v_mfma_f32_16x16x32_bf16 v[116:119], a[20:23], v[140:143], v[116:119]\n"
      "ds_read_b128 v[140:143], v1 offset:15360\n"
      "s_waitcnt lgkmcnt(4)\n"
      "v_mfma_f32_16x16x32_bf16 v[120:123], a[16:19], v[144:147], v[120:123]\n"
      "buffer_load_dwordx4 a[8:11], v1, s[16:19], s88 offen offset:1024 sc1\n"
      "v_mfma_f32_16x16x32_bf16 v[124:127], a[20:23], v[144:147], v[124:127]\n"
      "ds_read_b128 v[144:147], v1 offset:19456\n"
      "s_waitcnt lgkmcnt(4)\n"
      "v_mfma_f32_16x16x32_bf16 v[88:91], a[24:27], v[128:131], v[88:91]\n"
      "v_mfma_f32_16x16x32_bf16 v[92:95], a[28:31], v[128:131], v[92:95]\n"
      "s_waitcnt lgkmcnt(3)\n"
      "v_mfma_f32_16x16x32_bf16 v[96:99], a[24:27], v[132:135], v[96:99]\n"
      "buffer_load_dwordx4 a[12:15], v1, s[16:19], s88 offen offset:2048 sc1\n"
      "v_mfma_f32_16x16x32_bf16 v[100:103], a[28:31], v[132:135], v[100:103]\n"
      "s_waitcnt lgkmcnt(0)\n"
      "s_waitcnt vmcnt(26)\n"
      "s_barrier\n"
      "ds_read_b128 v[128:131], v1 offset:20480\n"
      "ds_read_b128 v[132:135], v1 offset:24576\n"
      "v_mfma_f32_16x16x32_bf16 v[104:107], a[24:27], v[136:139], v[104:107]\n"
      "s_waitcnt vmcnt(11)\n"
      "s_barrier\n"
      "s_add_u32 s76, s67, 2752512\n"
      "buffer_store_dwordx2 v[28:29], v21, s[16:19], s76 offen sc1\n"
      "v_mfma_f32_16x16x32_bf16 v[108:111], a[28:31], v[136:139], v[108:111]\n"
      "ds_read_b128 v[136:139], v1 offset:28672\n"
      "v_mfma_f32_16x16x32_bf16 v[112:115], a[24:27], v[140:143], v[112:115]\n"
      "v_mfma_f32_16x16x32_bf16 v[116:119], a[28:31], v[140:143], v[116:119]\n"
      "ds_read_b128 v[140:143], v1 offset:32768\n"
      "v_mfma_f32_16x16x32_bf16 v[120:123], a[24:27], v[144:147], v[120:123]\n"
      "buffer_load_dwordx4 a[16:19], v1, s[16:19], s88 offen offset:3072 sc1\n"
      "v_mfma_f32_16x16x32_bf16 v[124:127], a[28:31], v[144:147], v[124:127]\n"
      "ds_read_b128 v[144:147], v1 offset:36864\n"
      "s_waitcnt lgkmcnt(4)\n"
      "v_mfma_f32_16x16x32_bf16 v[88:91], a[32:35], v[128:131], v[88:91]\n"
      "v_mfma_f32_16x16x32_bf16 v[92:95], a[36:39], v[128:131], v[92:95]\n"
      "ds_read_b128 v[128:131], v1 offset:21504\n"
      "s_waitcnt lgkmcnt(4)\n"
      "v_mfma_f32_16x16x32_bf16 v[96:99], a[32:35], v[132:135], v[96:99]\n"
      "s_add_u32 s88, s88, 4096\n"
      "buffer_load_dwordx4 a[20:23], v1, s[16:19], s88 offen sc1\n"
      "v_mfma_f32_16x16x32_bf16 v[100:103], a[36:39], v[132:135], v[100:103]\n"
      "ds_read_b128 v[132:135], v1 offset:25600\n"
      "s_waitcnt lgkmcnt(4)\n"
      "v_mfma_f32_16x16x32_bf16 v[104:107], a[32:35], v[136:139], v[104:107]\n"
      "v_mfma_f32_16x16x32_bf16 v[108:111], a[36:39], v[136:139], v[108:111]\n"
      "ds_read_b128 v[136:139], v1 offset:29696\n"
      "s_waitcnt lgkmcnt(4)\n"
      "v_mfma_f32_16x16x32_bf16 v[112:115], a[32:35], v[140:143], v[112:115]\n"
      "buffer_load_dwordx4 a[24:27], v1, s[16:19], s88 offen offset:1024 sc1\n"
      "v_mfma_f32_16x16x32_bf16 v[116:119], a[36:39], v[140:143], v[116:119]\n"
      "ds_read_b128 v[140:143], v1 offset:33792\n"
      "s_waitcnt lgkmcnt(4)\n"
      "v_mfma_f32_16x16x32_bf16 v[120:123], a[32:35], v[144:147], v[120:123]\n"
      "v_mfma_f32_16x16x32_bf16 v[124:127], a[36:39], v[144:147], v[124:127]\n"
      "ds_read_b128 v[144:147], v1 offset:37888\n"
      "s_waitcnt lgkmcnt(4)\n"
      "v_mfma_f32_16x16x32_bf16 v[88:91], a[40:43], v[128:131], v[88:91]\n"
      "buffer_load_dwordx4 a[28:31], v1, s[16:19], s88 offen offset:2048 sc1\n"
      "v_mfma_f32_16x16x32_bf16 v[92:95], a[44:47], v[128:131], v[92:95]\n"
      "ds_read_b128 v[128:131], v1 offset:22528\n"
      "s_waitcnt lgkmcnt(4)\n"
      "v_mfma_f32_16x16x32_bf16 v[96:99], a[40:43], v[132:135], v[96:99]\n"
      "v_mfma_f32_16x16x32_bf16 v[100:103], a[44:47], v[132:135], v[100:103]\n"
      "ds_read_b128 v[132:135], v1 offset:26624\n"
      "s_waitcnt lgkmcnt(4)\n"
      "v_mfma_f32_16x16x32_bf16 v[104:107], a[40:43], v[136:139], v[104:107]\n"
      "buffer_load_dwordx4 a[32:35], v1, s[16:19], s88 offen offset:3072 sc1\n"
      "v_mfma_f32_16x16x32_bf16 v[108:111], a[44:47], v[136:139], v[108:111]\n"
      "ds_read_b128 v[136:139], v1 offset:30720\n"
      "s_waitcnt lgkmcnt(4)\n"
      "v_mfma_f32_16x16x32_bf16 v[112:115], a[40:43], v[140:143], v[112:115]\n"
      "v_mfma_f32_16x16x32_bf16 v[116:119], a[44:47], v[140:143], v[116:119]\n"
      "ds_read_b128 v[140:143], v1 offset:34816\n"
      "s_waitcnt lgkmcnt(4)\n"
      "v_mfma_f32_16x16x32_bf16 v[120:123], a[40:43], v[144:147], v[120:123]\n"
      "s_add_u32 s88, s88, 4096\n"
      "buffer_load_dwordx4 a[36:39], v1, s[16:19], s88 offen sc1\n"
      "v_mfma_f32_16x16x32_bf16 v[124:127], a[44:47], v[144:147], v[124:127]\n"
      "s_waitcnt vmcnt(16)\n"
      "v_add_f32 v228, v228, v230\n"
      "v_add_f32 v229, v229, v231\n"
      "v_mov_b32 v156, v228\n"
      "ds_read_b128 v[144:147], v1 offset:38912\n"
      "s_waitcnt lgkmcnt(4)\n"
      "v_mfma_f32_16x16x32_bf16 v[88:91], a[48:51], v[128:131], v[88:91]\n"
      "v_mov_b32 v157, v229\n"
      "v_permlane16_swap_b32 v156, v228\n"
      "s_nop 0\n"
      "v_permlane16_swap_b32 v157, v229\n"
      "v_mfma_f32_16x16x32_bf16 v[92:95], a[52:55], v[128:131], v[92:95]\n"
      "v_add_f32 v228, v228, v156\n"
      "v_add_f32 v229, v229, v157\n"
      "v_mov_b32 v156, v228\n"
      "ds_read_b128 v[128:131], v1 offset:23552\n"
      "s_waitcnt lgkmcnt(4)\n"
      "v_mfma_f32_16x16x32_bf16 v[96:99], a[48:51], v[132:135], v[96:99]\n"
      "buffer_load_dwordx4 a[40:43], v1, s[16:19], s88 offen offset:1024 sc1\n"
      "v_mov_b32 v157, v229\n"
      "v_permlane32_swap_b32 v156, v228\n"
      "s_nop 0\n"
      "v_permlane32_swap_b32 v157, v229\n"
      "v_mfma_f32_16x16x32_bf16 v[100:103], a[52:55], v[132:135], v[100:103]\n"
      "v_add_f32 v228, v228, v156\n"
      "v_add_f32 v229, v229, v157\n"
      "v_mul_f32 v230, 0x3acccccd, v228\n"
      "ds_read_b128 v[132:135], v1 offset:27648\n"
      "s_waitcnt lgkmcnt(4)\n"
      "v_mfma_f32_16x16x32_bf16 v[104:107], a[48:51], v[136:139], v[104:107]\n"
      "v_mul_f32 v229, 0x3acccccd, v229\n"
      "v_sub_f32 v159, 0, v230\n"
      "v_fma_f32 v229, -v230, v230, v229\n"
      "v_mfma_f32_16x16x32_bf16 v[108:111], a[52:55], v[136:139], v[108:111]\n"
      "v_max_f32 v229, 0, v229\n"
      "v_add_f32 v229, 0x3727c5ac, v229\n"
      "v_rsq_f32 v158, v229\n"
      "ds_read_b128 v[136:139], v1 offset:31744\n"
      "s_waitcnt lgkmcnt(4)\n"
      "v_mfma_f32_16x16x32_bf16 v[112:115], a[48:51], v[140:143], v[112:115]\n"
      "buffer_load_dwordx4 v1, s[16:19], s98 offen offset:3072 sc1 lds\n"
      "ds_write_b64 v26, v[158:159] offset:640\n"
      "s_waitcnt vmcnt(16)\n"
      "v_add_f32 v232, v232, v234\n"
      "v_add_f32 v233, v233, v235\n"
      "v_mfma_f32_16x16x32_bf16 v[116:119], a[52:55], v[140:143], v[116:119]\n"
      "v_mov_b32 v156, v232\n"
      "v_mov_b32 v157, v233\n"
      "s_nop 0\n"
      "v_permlane16_swap_b32 v156, v232\n"
      "ds_read_b128 v[140:143], v1 offset:35840\n"
      "s_waitcnt lgkmcnt(5)\n"
      "v_mfma_f32_16x16x32_bf16 v[120:123], a[48:51], v[144:147], v[120:123]\n"
      "v_permlane16_swap_b32 v157, v233\n"
      "v_add_f32 v232, v232, v156\n"
      "v_add_f32 v233, v233, v157\n"
      "v_mfma_f32_16x16x32_bf16 v[124:127], a[52:55], v[144:147], v[124:127]\n"
      "v_mov_b32 v156, v232\n"
      "v_mov_b32 v157, v233\n"
      "s_nop 0\n"
      "v_permlane32_swap_b32 v156, v232\n"
      "ds_read_b128 v[144:147], v1 offset:39936\n"
      "s_waitcnt lgkmcnt(5)\n"
      "v_mfma_f32_16x16x32_bf16 v[88:91], a[56:59], v[128:131], v[88:91]\n"
      "s_add_u32 s98, s57, 1327104\n"
      "s_add_u32 m0, s75, 81920\n"
      "s_nop 0\n"
      "buffer_load_dwordx4 v1, s[16:19], s98 offen sc1 lds\n"
      "v_permlane32_swap_b32 v157, v233\n"
      "v_add_f32 v232, v232, v156\n"
      "v_add_f32 v233, v233, v157\n"
      "v_mfma_f32_16x16x32_bf16 v[92:95], a[60:63], v[128:131], v[92:95]\n"
      "v_mul_f32 v234, 0x3acccccd, v232\n"
      "v_mul_f32 v233, 0x3acccccd, v233\n"
      "v_sub_f32 v159, 0, v234\n"
      "s_waitcnt lgkmcnt(4)\n"
      "v_mfma_f32_16x16x32_bf16 v[96:99], a[56:59], v[132:135], v[96:99]\n"
      "v_fma_f32 v233, -v234, v234, v233\n"
      "v_max_f32 v233, 0, v233\n"
      "v_add_f32 v233, 0x3727c5ac, v233\n"
      "v_mfma_f32_16x16x32_bf16 v[100:103], a[60:63], v[132:135], v[100:103]\n"
      "v_rsq_f32 v158, v233\n"
      "s_waitcnt lgkmcnt(0)\n"
      "s_barrier\n"
      "ds_read_b128 v[128:131], v1 offset:40960\n"
      "ds_read_b128 v[132:135], v1 offset:45056\n"
      "v_mfma_f32_16x16x32_bf16 v[104:107], a[56:59], v[136:139], v[104:107]\n"
      "buffer_load_dwordx4 a[44:47], v1, s[16:19], s88 offen offset:2048 sc1\n"
      "ds_write_b64 v25, v[158:159] offset:640\n"
      "v_mfma_f32_16x16x32_bf16 v[108:111], a[60:63], v[136:139], v[108:111]\n"
      "ds_read_b128 v[136:139], v1 offset:49152\n"
      "v_mfma_f32_16x16x32_bf16 v[112:115], a[56:59], v[140:143], v[112:115]\n"
      "v_mfma_f32_16x16x32_bf16 v[116:119], a[60:63], v[140:143], v[116:119]\n"
      "ds_read_b128 v[140:143], v1 offset:53248\n"
      "v_mfma_f32_16x16x32_bf16 v[120:123], a[56:59], v[144:147], v[120:123]\n"
      "buffer_load_dwordx4 a[48:51], v1, s[16:19], s88 offen offset:3072 sc1\n"
      "v_mfma_f32_16x16x32_bf16 v[124:127], a[60:63], v[144:147], v[124:127]\n"
      "ds_read_b128 v[144:147], v1 offset:57344\n"
      "s_waitcnt lgkmcnt(5)\n"
      "v_mfma_f32_16x16x32_bf16 v[88:91], a[64:67], v[128:131], v[88:91]\n"
      "v_mfma_f32_16x16x32_bf16 v[92:95], a[68:71], v[128:131], v[92:95]\n"
      "ds_read_b128 v[128:131], v1 offset:41984\n"
      "s_waitcnt lgkmcnt(5)\n"
      "v_mfma_f32_16x16x32_bf16 v[96:99], a[64:67], v[132:135], v[96:99]\n"
      "s_add_u32 s88, s88, 4096\n"
      "buffer_load_dwordx4 a[52:55], v1, s[16:19], s88 offen sc1\n"
      "v_mfma_f32_16x16x32_bf16 v[100:103], a[68:71], v[132:135], v[100:103]\n"
      "ds_read_b128 v[132:135], v1 offset:46080\n"
      "s_waitcnt lgkmcnt(4)\n"
      "v_mfma_f32_16x16x32_bf16 v[104:107], a[64:67], v[136:139], v[104:107]\n"
      "v_mfma_f32_16x16x32_bf16 v[108:111], a[68:71], v[136:139], v[108:111]\n"
      "ds_read_b128 v[136:139], v1 offset:50176\n"
      "s_waitcnt lgkmcnt(4)\n"
      "v_mfma_f32_16x16x32_bf16 v[112:115], a[64:67], v[140:143], v[112:115]\n"
      "buffer_load_dwordx4 a[56:59], v1, s[16:19], s88 offen offset:1024 sc1\n"
      "v_mfma_f32_16x16x32_bf16 v[116:119], a[68:71], v[140:143], v[116:119]\n"
      "ds_read_b128 v[140:143], v1 offset:54272\n"
      "s_waitcnt lgkmcnt(4)\n"
      "v_mfma_f32_16x16x32_bf16 v[120:123], a[64:67], v[144:147], v[120:123]\n"
      "v_mfma_f32_16x16x32_bf16 v[124:127], a[68:71], v[144:147], v[124:127]\n"
      "ds_read_b128 v[144:147], v1 offset:58368\n"
      "s_waitcnt lgkmcnt(4)\n"
      "v_mfma_f32_16x16x32_bf16 v[88:91], a[72:75], v[128:131], v[88:91]\n"
      "buffer_load_dwordx4 a[60:63], v1, s[16:19], s88 offen offset:2048 sc1\n"
      "v_mfma_f32_16x16x32_bf16 v[92:95], a[76:79], v[128:131], v[92:95]\n"
      "ds_read_b128 v[128:131], v1 offset:43008\n"
      "s_waitcnt lgkmcnt(4)\n"
      "v_mfma_f32_16x16x32_bf16 v[96:99], a[72:75], v[132:135], v[96:99]\n"
      "v_mfma_f32_16x16x32_bf16 v[100:103], a[76:79], v[132:135], v[100:103]\n"
      "ds_read_b128 v[132:135], v1 offset:47104\n"
      "s_waitcnt lgkmcnt(4)\n"
      "v_mfma_f32_16x16x32_bf16 v[104:107], a[72:75], v[136:139], v[104:107]\n"
      "buffer_load_dwordx4 a[64:67], v1, s[16:19], s88 offen offset:3072 sc1\n"
      "v_mfma_f32_16x16x32_bf16 v[108:111], a[76:79], v[136:139], v[108:111]\n"
      "ds_read_b128 v[136:139], v1 offset:51200\n"
      "s_waitcnt lgkmcnt(4)\n"
      "v_mfma_f32_16x16x32_bf16 v[112:115], a[72:75], v[140:143], v[112:115]\n"
      "v_mfma_f32_16x16x32_bf16 v[116:119], a[76:79], v[140:143], v[116:119]\n"
      "ds_read_b128 v[140:143], v1 offset:55296\n"
      "s_waitcnt lgkmcnt(4)\n"
      "v_mfma_f32_16x16x32_bf16 v[120:123], a[72:75], v[144:147], v[120:123]\n"
      "s_add_u32 s88, s88, 4096\n"
      "buffer_load_dwordx4 a[68:71], v1, s[16:19], s88 offen sc1\n"
      "v_mfma_f32_16x16x32_bf16 v[124:127], a[76:79], v[144:147], v[124:127]\n"
      "ds_read_b128 v[144:147], v1 offset:59392\n"
      "s_waitcnt lgkmcnt(4)\n"
      "v_mfma_f32_16x16x32_bf16 v[88:91], a[80:83], v[128:131], v[88:91]\n"
      "v_mfma_f32_16x16x32_bf16 v[92:95], a[84:87], v[128:131], v[92:95]\n"
      "ds_read_b128 v[128:131], v1 offset:44032\n"
      "s_waitcnt lgkmcnt(4)\n"
      "v_mfma_f32_16x16x32_bf16 v[96:99], a[80:83], v[132:135], v[96:99]\n"
      "buffer_load_dwordx4 a[72:75], v1, s[16:19], s88 offen offset:1024 sc1\n"
      "v_mfma_f32_16x16x32_bf16 v[100:103], a[84:87], v[132:135], v[100:103]\n"
      "ds_read_b128 v[132:135], v1 offset:48128\n"
      "s_waitcnt lgkmcnt(4)\n"
      "v_mfma_f32_16x16x32_bf16 v[104:107], a[80:83], v[136:139], v[104:107]\n"
      "v_mfma_f32_16x16x32_bf16 v[108:111], a[84:87], v[136:139], v[108:111]\n"
      "ds_read_b128 v[136:139], v1 offset:52224\n"
      "s_waitcnt lgkmcnt(4)\n"
      "v_mfma_f32_16x16x32_bf16 v[112:115], a[80:83], v[140:143], v[112:115]\n"
      "buffer_load_dwordx4 a[76:79], v1, s[16:19], s88 offen offset:2048 sc1\n"
      "v_mfma_f32_16x16x32_bf16 v[116:119], a[84:87], v[140:143], v[116:119]\n"
      "ds_read_b128 v[140:143], v1 offset:56320\n"
      "s_waitcnt lgkmcnt(4)\n"
      "v_mfma_f32_16x16x32_bf16 v[120:123], a[80:83], v[144:147], v[120:123]\n"
      "v_mfma_f32_16x16x32_bf16 v[124:127], a[84:87], v[144:147], v[124:127]\n"
      "ds_read_b128 v[144:147], v1 offset:60416\n"
      "s_waitcnt lgkmcnt(4)\n"
      "v_mfma_f32_16x16x32_bf16 v[88:91], a[88:91], v[128:131], v[88:91]\n"
      "buffer_load_dwordx4 a[80:83], v1, s[16:19], s88 offen offset:3072 sc1\n"
      "v_mfma_f32_16x16x32_bf16 v[92:95], a[92:95], v[128:131], v[92:95]\n"
      "s_waitcnt lgkmcnt(3)\n"
      "v_mfma_f32_16x16x32_bf16 v[96:99], a[88:91], v[132:135], v[96:99]\n"
      "v_mfma_f32_16x16x32_bf16 v[100:103], a[92:95], v[132:135], v[100:103]\n"
      "s_waitcnt lgkmcnt(0)\n"
      "s_barrier\n"
      "ds_read_b128 v[128:131], v2\n"
      "ds_read_b128 v[132:135], v2 offset:4096\n"
      "v_mfma_f32_16x16x32_bf16 v[104:107], a[88:91], v[136:139], v[104:107]\n"
      "s_add_u32 s88, s88, 4096\n"
      "buffer_load_dwordx4 a[84:87], v1, s[16:19], s88 offen sc1\n"
      "v_mfma_f32_16x16x32_bf16 v[108:111], a[92:95], v[136:139], v[108:111]\n"
      "ds_read_b128 v[136:139], v2 offset:8192\n"
      "v_mfma_f32_16x16x32_bf16 v[112:115], a[88:91], v[140:143], v[112:115]\n"
      "v_mfma_f32_16x16x32_bf16 v[116:119], a[92:95], v[140:143], v[116:119]\n"
      "ds_read_b128 v[140:143], v2 offset:12288\n"
      "v_mfma_f32_16x16x32_bf16 v[120:123], a[88:91], v[144:147], v[120:123]\n"
      "buffer_load_dwordx4 a[232:235], v1, s[16:19], s31 offen offset:2048 sc1\n"
      "v_mfma_f32_16x16x32_bf16 v[124:127], a[92:95], v[144:147], v[124:127]\n"
      "ds_read_b128 v[144:147], v2 offset:16384\n"
      "s_waitcnt lgkmcnt(4)\n"
      "v_mfma_f32_16x16x32_bf16 v[88:91], a[96:99], v[128:131], v[88:91]\n"
      "v_mfma_f32_16x16x32_bf16 v[92:95], a[100:103], v[128:131], v[92:95]\n"
      "ds_read_b128 v[128:131], v2 offset:1024\n"
      "s_waitcnt lgkmcnt(4)\n"
      "v_mfma_f32_16x16x32_bf16 v[96:99], a[96:99], v[132:135], v[96:99]\n"
      "buffer_load_dwordx4 a[88:91], v1, s[16:19], s88 offen offset:1024 sc1\n"
      "v_mfma_f32_16x16x32_bf16 v[100:103], a[100:103], v[132:135], v[100:103]\n"
      "ds_read_b128 v[132:135], v2 offset:5120\n"
      "s_waitcnt lgkmcnt(4)\n"
      "v_mfma_f32_16x16x32_bf16 v[104:107], a[96:99], v[136:139], v[104:107]\n"
      "v_mfma_f32_16x16x32_bf16 v[108:111], a[100:103], v[136:139], v[108:111]\n"
      "ds_read_b128 v[136:139], v2 offset:9216\n"
      "s_waitcnt lgkmcnt(4)\n"
      "v_mfma_f32_16x16x32_bf16 v[112:115], a[96:99], v[140:143], v[112:115]\n"
      "buffer_load_dwordx4 a[92:95], v1, s[16:19], s88 offen offset:2048 sc1\n"
      "v_mfma_f32_16x16x32_bf16 v[116:119], a[100:103], v[140:143], v[116:119]\n"
      "ds_read_b128 v[140:143], v2 offset:13312\n"
      "s_waitcnt lgkmcnt(4)\n"
      "v_mfma_f32_16x16x32_bf16 v[120:123], a[96:99], v[144:147], v[120:123]\n"
      "s_add_u32 s87, s66, 2752512\n"
      "buffer_load_dwordx2 v[30:31], v19, s[16:19], s87 offen sc1\n"
      "v_mfma_f32_16x16x32_bf16 v[124:127], a[100:103], v[144:147], v[124:127]\n"
      "ds_read_b128 v[144:147], v2 offset:17408\n"
      "s_waitcnt lgkmcnt(4)\n"
      "v_mfma_f32_16x16x32_bf16 v[88:91], a[104:107], v[128:131], v[88:91]\n"
      "v_mfma_f32_16x16x32_bf16 v[92:95], a[108:111], v[128:131], v[92:95]\n"
      "ds_read_b128 v[128:131], v2 offset:2048\n"
      "s_waitcnt lgkmcnt(4)\n"
      "v_mfma_f32_16x16x32_bf16 v[96:99], a[104:107], v[132:135], v[96:99]\n"
      "buffer_load_dwordx4 a[96:99], v1, s[16:19], s88 offen offset:3072 sc1\n"
      "v_mfma_f32_16x16x32_bf16 v[100:103], a[108:111], v[132:135], v[100:103]\n"
      "ds_read_b128 v[132:135], v2 offset:6144\n"
      "s_waitcnt lgkmcnt(4)\n"
      "v_mfma_f32_16x16x32_bf16 v[104:107], a[104:107], v[136:139], v[104:107]\n"
      "v_mfma_f32_16x16x32_bf16 v[108:111], a[108:111], v[136:139], v[108:111]\n"
      "ds_read_b128 v[136:139], v2 offset:10240\n"
      "s_waitcnt lgkmcnt(4)\n"
      "v_mfma_f32_16x16x32_bf16 v[112:115], a[104:107], v[140:143], v[112:115]\n"
      "s_add_u32 s88, s88, 4096\n"
      "buffer_load_dwordx4 a[100:103], v1, s[16:19], s88 offen sc1\n"
      "v_mfma_f32_16x16x32_bf16 v[116:119], a[108:111], v[140:143], v[116:119]\n"
      "ds_read_b128 v[140:143], v2 offset:14336\n"
      "s_waitcnt lgkmcnt(4)\n"
      "v_mfma_f32_16x16x32_bf16 v[120:123], a[104:107], v[144:147], v[120:123]\n"
      "v_mfma_f32_16x16x32_bf16 v[124:127], a[108:111], v[144:147], v[124:127]\n"
      "ds_read_b128 v[144:147], v2 offset:18432\n"
      "s_waitcnt lgkmcnt(4)\n"
      "v_mfma_f32_16x16x32_bf16 v[88:91], a[112:115], v[128:131], v[88:91]\n"
      "buffer_load_dwordx4 a[104:107], v1, s[16:19], s88 offen offset:1024 sc1\n"
      "v_mfma_f32_16x16x32_bf16 v[92:95], a[116:119], v[128:131], v[92:95]\n"
      "ds_read_b128 v[128:131], v2 offset:3072\n"
      "s_waitcnt lgkmcnt(4)\n"
      "v_mfma_f32_16x16x32_bf16 v[96:99], a[112:115], v[132:135], v[96:99]\n"
      "v_mfma_f32_16x16x32_bf16 v[100:103], a[116:119], v[132:135], v[100:103]\n"
      "ds_read_b128 v[132:135], v2 offset:7168\n"
      "s_waitcnt lgkmcnt(4)\n"
      "v_mfma_f32_16x16x32_bf16 v[104:107], a[112:115], v[136:139], v[104:107]\n"
      "buffer_load_dwordx4 a[108:111], v1, s[16:19], s88 offen offset:2048 sc1\n"
      "v_mfma_f32_16x16x32_bf16 v[108:111], a[116:119], v[136:139], v[108:111]\n"
      "ds_read_b128 v[136:139], v2 offset:11264\n"
      "s_waitcnt lgkmcnt(4)\n"
      "v_mfma_f32_16x16x32_bf16 v[112:115], a[112:115], v[140:143], v[112:115]\n"
      "v_mfma_f32_16x16x32_bf16 v[116:119], a[116:119], v[140:143], v[116:119]\n"
      "ds_read_b128 v[140:143], v2 offset:15360\n"
      "s_waitcnt lgkmcnt(4)\n"
      "v_mfma_f32_16x16x32_bf16 v[120:123], a[112:115], v[144:147], v[120:123]\n"
      "buffer_load_dwordx4 a[236:239], v1, s[16:19], s31 offen offset:3072 sc1\n"
      "v_mfma_f32_16x16x32_bf16 v[124:127], a[116:119], v[144:147], v[124:127]\n"
      "ds_read_b128 v[144:147], v2 offset:19456\n"
      "s_waitcnt lgkmcnt(4)\n"
      "v_mfma_f32_16x16x32_bf16 v[88:91], a[120:123], v[128:131], v[88:91]\n"
      "v_mfma_f32_16x16x32_bf16 v[92:95], a[124:127], v[128:131], v[92:95]\n"
      "s_waitcnt lgkmcnt(3)\n"
      "v_mfma_f32_16x16x32_bf16 v[96:99], a[120:123], v[132:135], v[96:99]\n"
      "buffer_load_dwordx4 a[112:115], v1, s[16:19], s88 offen offset:3072 sc1\n"
      "v_mfma_f32_16x16x32_bf16 v[100:103], a[124:127], v[132:135], v[100:103]\n"
      "s_waitcnt lgkmcnt(0)\n"
      "s_waitcnt vmcnt(21)\n"
      "s_barrier\n"
      "ds_read_b128 v[128:131], v2 offset:20480\n"
      "ds_read_b128 v[132:135], v2 offset:24576\n"
      "v_mfma_f32_16x16x32_bf16 v[104:107], a[120:123], v[136:139], v[104:107]\n"
      "s_add_u32 s88, s88, 4096\n"
      "buffer_load_dwordx4 a[116:119], v1, s[16:19], s88 offen sc1\n"
      "v_mfma_f32_16x16x32_bf16 v[108:111], a[124:127], v[136:139], v[108:111]\n"
      "ds_read_b128 v[136:139], v2 offset:28672\n"
      "v_mfma_f32_16x16x32_bf16 v[112:115], a[120:123], v[140:143], v[112:115]\n"
      "v_mfma_f32_16x16x32_bf16 v[116:119], a[124:127], v[140:143], v[116:119]\n"
      "ds_read_b128 v[140:143], v2 offset:32768\n"
      "v_mfma_f32_16x16x32_bf16 v[120:123], a[120:123], v[144:147], v[120:123]\n"
      "s_add_u32 s31, s31, 4096\n"
      "buffer_load_dwordx4 a[240:243], v1, s[16:19], s31 offen sc1\n"
      "v_mfma_f32_16x16x32_bf16 v[124:127], a[124:127], v[144:147], v[124:127]\n"
      "ds_read_b128 v[144:147], v2 offset:36864\n"
      "s_waitcnt lgkmcnt(4)\n"
      "v_mfma_f32_16x16x32_bf16 v[88:91], a[128:131], v[128:131], v[88:91]\n"
      "v_mfma_f32_16x16x32_bf16 v[92:95], a[132:135], v[128:131], v[92:95]\n"
      "ds_read_b128 v[128:131], v2 offset:21504\n"
      "s_waitcnt lgkmcnt(4)\n"
      "v_mfma_f32_16x16x32_bf16 v[96:99], a[128:131], v[132:135], v[96:99]\n"
      "buffer_load_dwordx4 a[120:123], v1, s[16:19], s88 offen offset:1024 sc1\n"
      "v_mfma_f32_16x16x32_bf16 v[100:103], a[132:135], v[132:135], v[100:103]\n"
      "ds_read_b128 v[132:135], v2 offset:25600\n"
      "s_waitcnt lgkmcnt(4)\n"
      "v_mfma_f32_16x16x32_bf16 v[104:107], a[128:131], v[136:139], v[104:107]\n"
      "v_mfma_f32_16x16x32_bf16 v[108:111], a[132:135], v[136:139], v[108:111]\n"
      "ds_read_b128 v[136:139], v2 offset:29696\n"
      "s_waitcnt lgkmcnt(4)\n"
      "v_mfma_f32_16x16x32_bf16 v[112:115], a[128:131], v[140:143], v[112:115]\n"
      "buffer_load_dwordx4 a[244:247], v1, s[16:19], s31 offen offset:1024 sc1\n"
      "v_mfma_f32_16x16x32_bf16 v[116:119], a[132:135], v[140:143], v[116:119]\n"
      "ds_read_b128 v[140:143], v2 offset:33792\n"
      "s_waitcnt lgkmcnt(4)\n"
      "v_mfma_f32_16x16x32_bf16 v[120:123], a[128:131], v[144:147], v[120:123]\n"
      "v_mfma_f32_16x16x32_bf16 v[124:127], a[132:135], v[144:147], v[124:127]\n"
      "ds_read_b128 v[144:147], v2 offset:37888\n"
      "s_waitcnt lgkmcnt(4)\n"
      "v_mfma_f32_16x16x32_bf16 v[88:91], a[136:139], v[128:131], v[88:91]\n"
      "buffer_load_dwordx4 a[248:251], v1, s[16:19], s31 offen offset:2048 sc1\n"
      "v_mfma_f32_16x16x32_bf16 v[92:95], a[140:143], v[128:131], v[92:95]\n"
      "ds_read_b128 v[128:131], v2 offset:22528\n"
      "s_waitcnt lgkmcnt(4)\n"
      "v_mfma_f32_16x16x32_bf16 v[96:99], a[136:139], v[132:135], v[96:99]\n"
      "v_mfma_f32_16x16x32_bf16 v[100:103], a[140:143], v[132:135], v[100:103]\n"
      "ds_read_b128 v[132:135], v2 offset:26624\n"
      "s_waitcnt lgkmcnt(4)\n"
      "v_mfma_f32_16x16x32_bf16 v[104:107], a[136:139], v[136:139], v[104:107]\n"
      "buffer_load_dwordx4 a[252:255], v1, s[16:19], s31 offen offset:3072 sc1\n"
      "v_mfma_f32_16x16x32_bf16 v[108:111], a[140:143], v[136:139], v[108:111]\n"
      "s_waitcnt vmcnt(12)\n"
      "v_cmp_eq_u64 vcc, s[36:37], v[30:31]\n"
      "s_nop 3\n"
      "s_cmp_eq_u64 vcc, -1\n"
      "s_cbranch_scc1 Lpd_7\n"
      "Lps_8:\n"
      "s_sleep 1\n"
      "buffer_load_dwordx2 v[30:31], v19, s[16:19], s87 offen sc1\n"
      "s_waitcnt vmcnt(0)\n"
      "v_cmp_eq_u64 vcc, s[36:37], v[30:31]\n"
      "s_nop 3\n"
      "s_cmp_eq_u64 vcc, -1\n"
      "s_cbranch_scc1 Lpd_7\n"
      "s_add_u32 s42, s42, 1\n"
      "s_cmp_lt_u32 s42, 0x40000\n"
      "s_cbranch_scc1 Lps_8\n"
      "Lpd_7:\n"
      "ds_read_b128 v[136:139], v2 offset:30720\n"
      "s_waitcnt lgkmcnt(4)\n"
      "v_mfma_f32_16x16x32_bf16 v[112:115], a[136:139], v[140:143], v[112:115]\n"
      "v_mfma_f32_16x16x32_bf16 v[116:119], a[140:143], v[140:143], v[116:119]\n"
      "ds_read_b128 v[140:143], v2 offset:34816\n"
      "s_waitcnt lgkmcnt(4)\n"
      "v_mfma_f32_16x16x32_bf16 v[120:123], a[136:139], v[144:147], v[120:123]\n"
      "s_add_u32 s31, s31, 4096\n"
      "buffer_load_dwordx4 v[176:179], v1, s[16:19], s31 offen sc1\n"
      "v_mfma_f32_16x16x32_bf16 v[124:127], a[140:143], v[144:147], v[124:127]\n"
      "ds_read_b128 v[144:147], v2 offset:38912\n"
      "s_waitcnt lgkmcnt(4)\n"
      "v_mfma_f32_16x16x32_bf16 v[88:91], a[144:147], v[128:131], v[88:91]\n"
      "v_mfma_f32_16x16x32_bf16 v[92:95], a[148:151], v[128:131], v[92:95]\n"
      "ds_read_b128 v[128:131], v2 offset:23552\n"
      "s_waitcnt lgkmcnt(4)\n"
      "v_mfma_f32_16x16x32_bf16 v[96:99], a[144:147], v[132:135], v[96:99]\n"
      "s_add_u32 s99, s58, 10575872\n"
      "buffer_load_dwordx4 v[180:183], v1, s[16:19], s99 offen sc1\n"
      "v_mfma_f32_16x16x32_bf16 v[100:103], a[148:151], v[132:135], v[100:103]\n"
      "ds_read_b128 v[132:135], v2 offset:27648\n"
      "s_waitcnt lgkmcnt(4)\n"
      "v_mfma_f32_16x16x32_bf16 v[104:107], a[144:147], v[136:139], v[104:107]\n"
      "v_mfma_f32_16x16x32_bf16 v[108:111], a[148:151], v[136:139], v[108:111]\n"
      "ds_read_b128 v[136:139], v2 offset:31744\n"
      "s_waitcnt lgkmcnt(4)\n"
      "v_mfma_f32_16x16x32_bf16 v[112:115], a[144:147], v[140:143], v[112:115]\n"
      "buffer_load_dwordx4 v[184:187], v1, s[16:19], s99 offen offset:1024 sc1\n"
      "v_mfma_f32_16x16x32_bf16 v[116:119], a[148:151], v[140:143], v[116:119]\n"
      "ds_read_b128 v[140:143], v2 offset:35840\n"
      "s_waitcnt lgkmcnt(4)\n"
      "v_mfma_f32_16x16x32_bf16 v[120:123], a[144:147], v[144:147], v[120:123]\n"
      "v_mfma_f32_16x16x32_bf16 v[124:127], a[148:151], v[144:147], v[124:127]\n"
      "ds_read_b128 v[144:147], v2 offset:39936\n"
      "s_waitcnt lgkmcnt(4)\n"
      "v_mfma_f32_16x16x32_bf16 v[88:91], a[152:155], v[128:131], v[88:91]\n"
      "buffer_load_dwordx4 v[188:191], v1, s[16:19], s99 offen offset:2048 sc1\n"
      "v_mfma_f32_16x16x32_bf16 v[92:95], a[156:159], v[128:131], v[92:95]\n"
      "s_waitcnt lgkmcnt(3)\n"
      "v_mfma_f32_16x16x32_bf16 v[96:99], a[152:155], v[132:135], v[96:99]\n"
      "v_mfma_f32_16x16x32_bf16 v[100:103], a[156:159], v[132:135], v[100:103]\n"
      "s_waitcnt lgkmcnt(2)\n"
      "v_mfma_f32_16x16x32_bf16 v[104:107], a[152:155], v[136:139], v[104:107]\n"
      "buffer_load_dwordx4 v[192:195], v1, s[16:19], s99 offen offset:3072 sc1\n"
      "v_mfma_f32_16x16x32_bf16 v[108:111], a[156:159], v[136:139], v[108:111]\n"
      "s_waitcnt lgkmcnt(1)\n"
      "v_mfma_f32_16x16x32_bf16 v[112:115], a[152:155], v[140:143], v[112:115]\n"
      "v_mfma_f32_16x16x32_bf16 v[116:119], a[156:159], v[140:143], v[116:119]\n"
      "s_waitcnt lgkmcnt(0)\n"
      "v_mfma_f32_16x16x32_bf16 v[120:123], a[152:155], v[144:147], v[120:123]\n"
      "s_add_u32 s99, s99, 4096\n"
      "buffer_load_dwordx4 v[196:199], v1, s[16:19], s99 offen sc1\n"
      "v_mfma_f32_16x16x32_bf16 v[124:127], a[156:159], v[144:147], v[124:127]\n"
      "s_waitcnt lgkmcnt(0)\n"
      "s_barrier\n"
      "s_add_u32 s86, s58, 10596352\n"
      "buffer_load_dwordx4 v[200:203], v1, s[16:19], s86 offen sc1\n"
      "ds_read_b64 v[148:149], v25 offset:640\n"
      "ds_read_b64 v[150:151], v25 offset:768\n"
      "ds_read_b64 v[152:153], v25 offset:896\n"
      "ds_read_b64 v[154:155], v25 offset:1024\n"
      "ds_read_b64 v[156:157], v25 offset:1152\n"
      "buffer_load_dwordx4 v[204:207], v1, s[16:19], s86 offen offset:1024 sc1\n"
      "ds_read_b128 v[158:161], v24\n"
      "ds_read_b128 v[162:165], v24 offset:64\n"
      "ds_read_b32 v48, v25 offset:644\n"
      "ds_read_b32 v50, v25 offset:772\n"
      "ds_read_b32 v52, v25 offset:900\n"
      "buffer_load_dwordx4 v[208:211], v1, s[16:19], s86 offen offset:2048 sc1\n"
      "ds_read_b32 v54, v25 offset:1028\n"
      "ds_read_b32 v56, v25 offset:1156\n"
      "s_waitcnt lgkmcnt(4)\n"
      "v_pk_fma_f32 v[120:121], v[158:159], v[48:49], v[120:121] op_sel_hi:[1,0,1]\n"
      "s_waitcnt lgkmcnt(3)\n"
      "v_pk_fma_f32 v[88:89], v[158:159], v[50:51], v[88:89] op_sel_hi:[1,0,1]\n"
      "v_pk_fma_f32 v[90:91], v[160:161], v[50:51], v[90:91] op_sel_hi:[1,0,1]\n"
      "v_pk_fma_f32 v[92:93], v[162:163], v[50:51], v[92:93] op_sel_hi:[1,0,1]\n"
      "v_pk_mul_f32 v[88:89], v[88:89], v[150:151] op_sel_hi:[1,0]\n"
      "v_pk_fma_f32 v[94:95], v[164:165], v[50:51], v[94:95] op_sel_hi:[1,0,1]\n"
      "v_pk_mul_f32 v[90:91], v[90:91], v[150:151] op_sel_hi:[1,0]\n"
      "s_waitcnt lgkmcnt(2)\n"
      "v_pk_fma_f32 v[96:97], v[158:159], v[52:53], v[96:97] op_sel_hi:[1,0,1]\n"
      "buffer_load_dwordx4 v[212:215], v1, s[16:19], s86 offen offset:3072 sc1\n"
      "v_pk_mul_f32 v[92:93], v[92:93], v[150:151] op_sel_hi:[1,0]\n"
      "v_cvt_pk_bf16_f32 v88, v88, v89\n"
      "v_pk_fma_f32 v[98:99], v[160:161], v[52:53], v[98:99] op_sel_hi:[1,0,1]\n"
      "v_pk_mul_f32 v[94:95], v[94:95], v[150:151] op_sel_hi:[1,0]\n"
      "v_cvt_pk_bf16_f32 v89, v90, v91\n"
      "v_pk_fma_f32 v[100:101], v[162:163], v[52:53], v[100:101] op_sel_hi:[1,0,1]\n"
      "v_pk_mul_f32 v[96:97], v[96:97], v[152:153] op_sel_hi:[1,0]\n"
      "v_cvt_pk_bf16_f32 v92, v92, v93\n"
      "v_pk_fma_f32 v[102:103], v[164:165], v[52:53], v[102:103] op_sel_hi:[1,0,1]\n"
      "v_pk_mul_f32 v[98:99], v[98:99], v[152:153] op_sel_hi:[1,0]\n"
      "v_cvt_pk_bf16_f32 v93, v94, v95\n"
      "ds_write_b64 v3, v[88:89] offset:4480\n"
      "s_add_u32 s86, s86, 4096\n"
      "buffer_load_dwordx4 v[216:219], v1, s[16:19], s86 offen sc1\n"
      "s_waitcnt lgkmcnt(2)\n"
      "v_pk_fma_f32 v[104:105], v[158:159], v[54:55], v[104:105] op_sel_hi:[1,0,1]\n"
      "v_pk_mul_f32 v[100:101], v[100:101], v[152:153] op_sel_hi:[1,0]\n"
      "v_cvt_pk_bf16_f32 v96, v96, v97\n"
      "v_pk_fma_f32 v[106:107], v[160:161], v[54:55], v[106:107] op_sel_hi:[1,0,1]\n"
      "v_pk_mul_f32 v[102:103], v[102:103], v[152:153] op_sel_hi:[1,0]\n"
      "v_cvt_pk_bf16_f32 v97, v98, v99\n"
      "ds_write_b64 v3, v[92:93] offset:4512\n"
      "v_pk_fma_f32 v[108:109], v[162:163], v[54:55], v[108:109] op_sel_hi:[1,0,1]\n"
      "v_pk_mul_f32 v[104:105], v[104:105], v[154:155] op_sel_hi:[1,0]\n"
      "v_cvt_pk_bf16_f32 v100, v100, v101\n"
      "v_pk_fma_f32 v[110:111], v[164:165], v[54:55], v[110:111] op_sel_hi:[1,0,1]\n"
      "v_pk_mul_f32 v[106:107], v[106:107], v[154:155] op_sel_hi:[1,0]\n"
      "v_cvt_pk_bf16_f32 v101, v102, v103\n"
      "ds_write_b64 v3, v[96:97] offset:8960\n"
      "s_waitcnt lgkmcnt(3)\n"
      "v_pk_fma_f32 v[112:113], v[158:159], v[56:57], v[112:113] op_sel_hi:[1,0,1]\n"
      "v_pk_mul_f32 v[108:109], v[108:109], v[154:155] op_sel_hi:[1,0]\n"
      "v_cvt_pk_bf16_f32 v104, v104, v105\n"
      "v_pk_fma_f32 v[114:115], v[160:161], v[56:57], v[114:115] op_sel_hi:[1,0,1]\n"
      "v_pk_mul_f32 v[110:111], v[110:111], v[154:155] op_sel_hi:[1,0]\n"
      "v_cvt_pk_bf16_f32 v105, v106, v107\n"
      "ds_write_b64 v3, v[100:101] offset:8992\n"
      "v_pk_fma_f32 v[116:117], v[162:163], v[56:57], v[116:117] op_sel_hi:[1,0,1]\n"
      "v_pk_mul_f32 v[112:113], v[112:113], v[156:157] op_sel_hi:[1,0]\n"
      "v_cvt_pk_bf16_f32 v108, v108, v109\n"
      "v_pk_fma_f32 v[118:119], v[164:165], v[56:57], v[118:119] op_sel_hi:[1,0,1]\n"
      "v_pk_mul_f32 v[114:115], v[114:115], v[156:157] op_sel_hi:[1,0]\n"
      "v_cvt_pk_bf16_f32 v109, v110, v111\n"
      "ds_write_b64 v3, v[104:105] offset:13440\n"
      "v_pk_mul_f32 v[116:117], v[116:117], v[156:157] op_sel_hi:[1,0]\n"
      "v_cvt_pk_bf16_f32 v112, v112, v113\n"
      "v_pk_fma_f32 v[122:123], v[160:161], v[48:49], v[122:123] op_sel_hi:[1,0,1]\n"
      "v_pk_mul_f32 v[118:119], v[118:119], v[156:157] op_sel_hi:[1,0]\n"
      "v_cvt_pk_bf16_f32 v113, v114, v115\n"
      "ds_write_b64 v3, v[108:109] offset:13472\n"
      "v_pk_fma_f32 v[124:125], v[162:163], v[48:49], v[124:125] op_sel_hi:[1,0,1]\n"
      "v_pk_mul_f32 v[120:121], v[120:121], v[148:149] op_sel_hi:[1,0]\n"
      "v_cvt_pk_bf16_f32 v116, v116, v117\n"
      "v_pk_fma_f32 v[126:127], v[164:165], v[48:49], v[126:127] op_sel_hi:[1,0,1]\n"
      "v_pk_mul_f32 v[122:123], v[122:123], v[148:149] op_sel_hi:[1,0]\n"
      "v_cvt_pk_bf16_f32 v117, v118, v119\n"
      "ds_write_b64 v3, v[112:113] offset:17920\n"
      "v_pk_mul_f32 v[124:125], v[124:125], v[148:149] op_sel_hi:[1,0]\n"
      "v_cvt_pk_bf16_f32 v120, v120, v121\n"
      "v_pk_mul_f32 v[126:127], v[126:127], v[148:149] op_sel_hi:[1,0]\n"
      "v_cvt_pk_bf16_f32 v121, v122, v123\n"
      "ds_write_b64 v3, v[116:117] offset:17952\n"
      "v_cvt_pk_bf16_f32 v124, v124, v125\n"
      "v_cvt_pk_bf16_f32 v125, v126, v127\n"
      "ds_write_b64 v3, v[120:121]\n"
      "ds_write_b64 v3, v[124:125] offset:32\n"
      "s_waitcnt lgkmcnt(0)\n"
      "s_barrier\n"
      "ds_read_b64 v[96:97], v6 offset:160\n"
      "ds_read_b64 v[98:99], v6 offset:168\n"
      "ds_read_b64 v[88:89], v4\n"
      "ds_read_b64 v[90:91], v4 offset:8\n"
      "ds_read_b64 v[92:93], v5\n"
      "ds_read_b64 v[94:95], v5 offset:8\n"
      "ds_read_b64 v[116:117], v6 offset:200\n"
      "ds_read_b64 v[118:119], v6 offset:208\n"
      "ds_read_b64 v[108:109], v4 offset:40\n"
      "ds_read_b64 v[110:111], v4 offset:48\n"
      "ds_read_b64 v[112:113], v5 offset:40\n"
      "ds_read_b64 v[114:115], v5 offset:48\n"
      "s_mov_b32 s40, 0x3ea52b5c\n"
      "s_mov_b32 s41, 0x3ea52b5c\n"
      "s_waitcnt lgkmcnt(11)\n"
      "v_and_b32 v96, v96, v9\n"
      "v_and_b32 v97, v97, v9\n"
      "s_waitcnt lgkmcnt(10)\n"
      "v_and_b32 v98, v98, v10\n"
      "v_and_b32 v99, v99, v10\n"
      "s_waitcnt lgkmcnt(8)\n"
      "s_nop 0\n"
      "v_mfma_f32_16x16x32_bf16 v[100:103], v[88:91], v[96:99], 0\n"
      "s_waitcnt lgkmcnt(5)\n"
      "v_and_b32 v116, v116, v9\n"
      "v_and_b32 v117, v117, v9\n"
      "s_waitcnt lgkmcnt(4)\n"
      "v_and_b32 v118, v118, v10\n"
      "v_and_b32 v119, v119, v10\n"
      "v_mfma_f32_16x16x32_bf16 v[104:107], v[92:95], v[96:99], 0\n"
      "s_waitcnt lgkmcnt(2)\n"
      "v_mfma_f32_16x16x32_bf16 v[120:123], v[108:111], v[116:119], 0\n"
      "s_waitcnt lgkmcnt(0)\n"
      "v_mfma_f32_16x16x32_bf16 v[124:127], v[112:115], v[116:119], 0\n"
      "ds_read_b64_tr_b16 v[88:89], v7 offset:80\n"
      "v_pk_fma_f32 v[100:101], v[100:101], s[40:41], v[168:169]\n"
      "v_pk_fma_f32 v[102:103], v[102:103], s[40:41], v[170:171]\n"
      "ds_read_b64_tr_b16 v[90:91], v8 offset:80\n"
      "v_max3_f32 v148, v100, v101, v102\n"
      "ds_read_b64_tr_b16 v[92:93], v7 offset:112\n"
      "ds_read_b64_tr_b16 v[94:95], v8 offset:112\n"
      "v_pk_fma_f32 v[104:105], v[104:105], s[40:41], v[172:173]\n"
      "v_pk_fma_f32 v[106:107], v[106:107], s[40:41], v[174:175]\n"
      "v_pk_fma_f32 v[120:121], v[120:121], s[40:41], v[168:169]\n"
      "v_pk_fma_f32 v[122:123], v[122:123], s[40:41], v[170:171]\n"
      "v_pk_fma_f32 v[124:125], v[124:125], s[40:41], v[172:173]\n"
      "v_pk_fma_f32 v[126:127], v[126:127], s[40:41], v[174:175]\n"
      "v_max3_f32 v154, v120, v121, v122\n"
      "v_max3_f32 v150, v103, v104, v105\n"
      "v_max3_f32 v148, v148, v106, v107\n"
      "v_max3_f32 v156, v123, v124, v125\n"
      "v_max3_f32 v154, v154, v126, v127\n"
      "v_max_f32 v148, v148, v150\n"
      "v_max_f32 v154, v154, v156\n"
      "v_mov_b32 v150, v148\n"
      "v_mov_b32 v156, v154\n"
      "s_nop 0\n"
      "v_permlane16_swap_b32 v150, v148\n"
      "v_permlane16_swap_b32 v156, v154\n"
      "ds_read_b64_tr_b16 v[108:109], v7 offset:120\n"
      "v_max_f32 v148, v148, v150\n"
      "v_max_f32 v154, v154, v156\n"
      "v_mov_b32 v150, v148\n"
      "v_mov_b32 v156, v154\n"
      "s_nop 0\n"
      "v_permlane32_swap_b32 v150, v148\n"
      "v_permlane32_swap_b32 v156, v154\n"
      "ds_read_b64_tr_b16 v[110:111], v8 offset:120\n"
      "v_max_f32 v148, v148, v150\n"
      "v_max_f32 v154, v154, v156\n"
      "v_pk_add_f32 v[100:101], v[100:101], v[148:149] op_sel_hi:[1,0] neg_lo:[0,1] neg_hi:[0,1]\n"
      "v_pk_add_f32 v[102:103], v[102:103], v[148:149] op_sel_hi:[1,0] neg_lo:[0,1] neg_hi:[0,1]\n"
      "v_pk_add_f32 v[104:105], v[104:105], v[148:149] op_sel_hi:[1,0] neg_lo:[0,1] neg_hi:[0,1]\n"
      "v_pk_add_f32 v[106:107], v[106:107], v[148:149] op_sel_hi:[1,0] neg_lo:[0,1] neg_hi:[0,1]\n"
      "v_pk_add_f32 v[120:121], v[120:121], v[154:155] op_sel_hi:[1,0] neg_lo:[0,1] neg_hi:[0,1]\n"
      "v_pk_add_f32 v[122:123], v[122:123], v[154:155] op_sel_hi:[1,0] neg_lo:[0,1] neg_hi:[0,1]\n"
      "v_pk_add_f32 v[124:125], v[124:125], v[154:155] op_sel_hi:[1,0] neg_lo:[0,1] neg_hi:[0,1]\n"
      "v_pk_add_f32 v[126:127], v[126:127], v[154:155] op_sel_hi:[1,0] neg_lo:[0,1] neg_hi:[0,1]\n"
      "v_exp_f32 v100, v100\n"
      "v_exp_f32 v101, v101\n"
      "v_exp_f32 v102, v102\n"
      "v_exp_f32 v103, v103\n"
      "v_exp_f32 v104, v104\n"
      "v_exp_f32 v105, v105\n"
      "v_exp_f32 v106, v106\n"
      "v_exp_f32 v107, v107\n"
      "v_exp_f32 v120, v120\n"
      "v_exp_f32 v121, v121\n"
      "v_exp_f32 v122, v122\n"
      "v_exp_f32 v123, v123\n"
      "v_exp_f32 v124, v124\n"
      "v_exp_f32 v125, v125\n"
      "v_exp_f32 v126, v126\n"
      "v_exp_f32 v127, v127\n"
      "v_pk_add_f32 v[152:153], v[100:101], v[102:103]\n"
      "v_pk_add_f32 v[148:149], v[104:105], v[106:107]\n"
      "v_pk_add_f32 v[158:159], v[120:121], v[122:123]\n"
      "v_pk_add_f32 v[154:155], v[124:125], v[126:127]\n"
      "v_pk_add_f32 v[152:153], v[152:153], v[148:149]\n"
      "v_pk_add_f32 v[158:159], v[158:159], v[154:155]\n"
      "ds_read_b64_tr_b16 v[112:113], v7 offset:152\n"
      "ds_read_b64_tr_b16 v[114:115], v8 offset:152\n"
      "v_add_f32 v152, v152, v153\n"
      "v_add_f32 v158, v158, v159\n"
      "v_mov_b32 v150, v152\n"
      "v_mov_b32 v156, v158\n"
      "s_nop 0\n"
      "v_permlane16_swap_b32 v150, v152\n"
      "v_permlane16_swap_b32 v156, v158\n"
      "v_add_f32 v152, v152, v150\n"
      "v_add_f32 v158, v158, v156\n"
      "v_mov_b32 v150, v152\n"
      "v_mov_b32 v156, v158\n"
      "s_nop 0\n"
      "v_permlane32_swap_b32 v150, v152\n"
      "v_permlane32_swap_b32 v156, v158\n"
      "v_add_f32 v152, v152, v150\n"
      "v_add_f32 v158, v158, v156\n"
      "v_rcp_f32 v152, v152\n"
      "v_rcp_f32 v158, v158\n"
      "v_pk_mul_f32 v[100:101], v[100:101], v[152:153] op_sel_hi:[1,0]\n"
      "v_pk_mul_f32 v[102:103], v[102:103], v[152:153] op_sel_hi:[1,0]\n"
      "v_pk_mul_f32 v[104:105], v[104:105], v[152:153] op_sel_hi:[1,0]\n"
      "v_pk_mul_f32 v[106:107], v[106:107], v[152:153] op_sel_hi:[1,0]\n"
      "v_pk_mul_f32 v[120:121], v[120:121], v[158:159] op_sel_hi:[1,0]\n"
      "v_pk_mul_f32 v[122:123], v[122:123], v[158:159] op_sel_hi:[1,0]\n"
      "v_pk_mul_f32 v[124:125], v[124:125], v[158:159] op_sel_hi:[1,0]\n"
      "v_pk_mul_f32 v[126:127], v[126:127], v[158:159] op_sel_hi:[1,0]\n"
      "v_cvt_pk_bf16_f32 v96, v100, v101\n"
      "v_cvt_pk_bf16_f32 v97, v102, v103\n"
      "v_cvt_pk_bf16_f32 v98, v104, v105\n"
      "v_cvt_pk_bf16_f32 v99, v106, v107\n"
      "v_cvt_pk_bf16_f32 v116, v120, v121\n"
      "v_cvt_pk_bf16_f32 v117, v122, v123\n"
      "v_cvt_pk_bf16_f32 v118, v124, v125\n"
      "v_cvt_pk_bf16_f32 v119, v126, v127\n"
      "s_waitcnt lgkmcnt(6)\n"
      "v_mfma_f32_16x16x32_bf16 v[100:103], v[88:91], v[96:99], 0\n"
      "s_waitcnt lgkmcnt(4)\n"
      "v_mfma_f32_16x16x32_bf16 v[104:107], v[92:95], v[96:99], 0\n"
      "s_waitcnt lgkmcnt(2)\n"
      "v_mfma_f32_16x16x32_bf16 v[120:123], v[108:111], v[116:119], 0\n"
      "s_waitcnt lgkmcnt(0)\n"
      "v_mfma_f32_16x16x32_bf16 v[124:127], v[112:115], v[116:119], 0\n"
      "s_nop 1\n"
      "v_cvt_pk_bf16_f32 v160, v100, v101\n"
      "v_cvt_pk_bf16_f32 v161, v102, v103\n"
      "v_cvt_pk_bf16_f32 v164, v104, v105\n"
      "v_cvt_pk_bf16_f32 v165, v106, v107\n"
      "v_cvt_pk_bf16_f32 v162, v120, v121\n"
      "v_cvt_pk_bf16_f32 v163, v122, v123\n"
      "v_cvt_pk_bf16_f32 v166, v124, v125\n"
      "v_cvt_pk_bf16_f32 v167, v126, v127\n"
      "s_add_u32 s80, s56, 11886592\n"
      "buffer_store_dwordx4 v[160:163], v11, s[16:19], s80 offen sc1\n"
      "buffer_store_dwordx4 v[164:167], v12, s[16:19], s80 offen sc1\n"
      "; ==== O0\n"
      "s_waitcnt vmcnt(11)\n"
      "v_mfma_f32_16x16x32_bf16 v[48:51], a[160:163], v[180:183], 0\n"
      "v_mfma_f32_16x16x32_bf16 v[52:55], a[164:167], v[180:183], 0\n"
      "v_mfma_f32_16x16x32_bf16 v[56:59], a[168:171], v[180:183], 0\n"
      "v_mfma_f32_16x16x32_bf16 v[60:63], a[172:175], v[180:183], 0\n"
      "v_mfma_f32_16x16x32_bf16 v[64:67], a[176:179], v[180:183], 0\n"
      "s_waitcnt vmcnt(10)\n"
      "v_mfma_f32_16x16x32_bf16 v[48:51], a[180:183], v[184:187], v[48:51]\n"
      "v_mfma_f32_16x16x32_bf16 v[52:55], a[184:187], v[184:187], v[52:55]\n"
      "v_mfma_f32_16x16x32_bf16 v[56:59], a[188:191], v[184:187], v[56:59]\n"
      "v_mfma_f32_16x16x32_bf16 v[60:63], a[192:195], v[184:187], v[60:63]\n"
      "v_mfma_f32_16x16x32_bf16 v[64:67], a[196:199], v[184:187], v[64:67]\n"
      "s_waitcnt vmcnt(9)\n"
      "v_mfma_f32_16x16x32_bf16 v[48:51], a[200:203], v[188:191], v[48:51]\n"
      "v_mfma_f32_16x16x32_bf16 v[52:55], a[204:207], v[188:191], v[52:55]\n"
      "v_mfma_f32_16x16x32_bf16 v[56:59], a[208:211], v[188:191], v[56:59]\n"
      "v_mfma_f32_16x16x32_bf16 v[60:63], a[212:215], v[188:191], v[60:63]\n"
      "v_mfma_f32_16x16x32_bf16 v[64:67], a[216:219], v[188:191], v[64:67]\n"
      "s_waitcnt vmcnt(8)\n"
      "v_mfma_f32_16x16x32_bf16 v[48:51], a[220:223], v[192:195], v[48:51]\n"
      "v_mfma_f32_16x16x32_bf16 v[52:55], a[224:227], v[192:195], v[52:55]\n"
      "v_mfma_f32_16x16x32_bf16 v[56:59], a[228:231], v[192:195], v[56:59]\n"
      "v_mfma_f32_16x16x32_bf16 v[60:63], a[232:235], v[192:195], v[60:63]\n"
      "v_mfma_f32_16x16x32_bf16 v[64:67], a[236:239], v[192:195], v[64:67]\n"
      "s_waitcnt vmcnt(7)\n"
      "v_mfma_f32_16x16x32_bf16 v[48:51], a[240:243], v[196:199], v[48:51]\n"
      "v_mfma_f32_16x16x32_bf16 v[52:55], a[244:247], v[196:199], v[52:55]\n"
      "v_mfma_f32_16x16x32_bf16 v[56:59], a[248:251], v[196:199], v[56:59]\n"
      "v_mfma_f32_16x16x32_bf16 v[60:63], a[252:255], v[196:199], v[60:63]\n"
      "v_mfma_f32_16x16x32_bf16 v[64:67], v[176:179], v[196:199], v[64:67]\n"
      "s_waitcnt vmcnt(6)\n"
      "v_mfma_f32_16x16x32_bf16 v[68:71], a[160:163], v[200:203], 0\n"
      "s_barrier\n"
      "v_mfma_f32_16x16x32_bf16 v[72:75], a[164:167], v[200:203], 0\n"
      "v_mfma_f32_16x16x32_bf16 v[76:79], a[168:171], v[200:203], 0\n"
      "v_mfma_f32_16x16x32_bf16 v[80:83], a[172:175], v[200:203], 0\n"
      "ds_write_b128 v13, v[48:51]\n"
      "ds_write_b128 v13, v[52:55] offset:1152\n"
      "ds_write_b128 v13, v[56:59] offset:2304\n"
      "v_mfma_f32_16x16x32_bf16 v[84:87], a[176:179], v[200:203], 0\n"
      "ds_write_b128 v13, v[60:63] offset:3456\n"
      "ds_write_b128 v13, v[64:67] offset:4608\n"
      "s_waitcnt vmcnt(5)\n"
      "v_mfma_f32_16x16x32_bf16 v[68:71], a[180:183], v[204:207], v[68:71]\n"
      "v_mfma_f32_16x16x32_bf16 v[72:75], a[184:187], v[204:207], v[72:75]\n"
      "v_mfma_f32_16x16x32_bf16 v[76:79], a[188:191], v[204:207], v[76:79]\n"
      "v_mfma_f32_16x16x32_bf16 v[80:83], a[192:195], v[204:207], v[80:83]\n"
      "v_mfma_f32_16x16x32_bf16 v[84:87], a[196:199], v[204:207], v[84:87]\n"
      "s_waitcnt vmcnt(4)\n"
      "v_mfma_f32_16x16x32_bf16 v[68:71], a[200:203], v[208:211], v[68:71]\n"
      "v_mfma_f32_16x16x32_bf16 v[72:75], a[204:207], v[208:211], v[72:75]\n"
      "v_mfma_f32_16x16x32_bf16 v[76:79], a[208:211], v[208:211], v[76:79]\n"
      "v_mfma_f32_16x16x32_bf16 v[80:83], a[212:215], v[208:211], v[80:83]\n"
      "v_mfma_f32_16x16x32_bf16 v[84:87], a[216:219], v[208:211], v[84:87]\n"
      "s_waitcnt vmcnt(3)\n"
      "v_mfma_f32_16x16x32_bf16 v[68:71], a[220:223], v[212:215], v[68:71]\n"
      "v_mfma_f32_16x16x32_bf16 v[72:75], a[224:227], v[212:215], v[72:75]\n"
      "s_waitcnt vmcnt(0)\n"
      "s_barrier\n"
      "s_add_u32 s76, s67, 2756608\n"
      "buffer_store_dwordx2 v[28:29], v21, s[16:19], s76 offen sc1\n"
      "v_mfma_f32_16x16x32_bf16 v[76:79], a[228:231], v[212:215], v[76:79]\n"
      "v_mfma_f32_16x16x32_bf16 v[80:83], a[232:235], v[212:215], v[80:83]\n"
      "v_mfma_f32_16x16x32_bf16 v[84:87], a[236:239], v[212:215], v[84:87]\n"
      "v_mfma_f32_16x16x32_bf16 v[68:71], a[240:243], v[216:219], v[68:71]\n"
      "v_mfma_f32_16x16x32_bf16 v[72:75], a[244:247], v[216:219], v[72:75]\n"
      "v_mfma_f32_16x16x32_bf16 v[76:79], a[248:251], v[216:219], v[76:79]\n"
      "v_mfma_f32_16x16x32_bf16 v[80:83], a[252:255], v[216:219], v[80:83]\n"
      "v_mfma_f32_16x16x32_bf16 v[84:87], v[176:179], v[216:219], v[84:87]\n"
      "; ==== O1\n"
      "s_nop 3\n"
      "ds_write_b128 v13, v[68:71] offset:5760\n"
      "ds_write_b128 v13, v[72:75] offset:6912\n"
      "ds_write_b128 v13, v[76:79] offset:8064\n"
      "ds_write_b128 v13, v[80:83] offset:9216\n"
      "ds_write_b128 v13, v[84:87] offset:10368\n"
      "s_waitcnt lgkmcnt(0)\n"
      "s_barrier\n"
      "ds_read_b128 v[48:51], v14\n"
      "ds_read_b128 v[52:55], v14 offset:11520\n"
      "ds_read_b128 v[56:59], v14 offset:23040\n"
      "ds_read_b128 v[60:63], v14 offset:34560\n"
      "ds_read_b128 v[64:67], v14 offset:256\n"
      "ds_read_b128 v[68:71], v14 offset:11776\n"
      "ds_read_b128 v[72:75], v14 offset:23296\n"
      "ds_read_b128 v[76:79], v14 offset:34816\n"
      "ds_read_b128 v[88:91], v14 offset:512\n"
      "ds_read_b128 v[92:95], v14 offset:12032\n"
      "ds_read_b128 v[96:99], v14 offset:23552\n"
      "ds_read_b128 v[100:103], v14 offset:35072\n"
      "ds_read_b128 v[104:107], v14 offset:768\n"
      "ds_read_b128 v[108:111], v14 offset:12288\n"
      "ds_read_b128 v[112:115], v14 offset:23808\n"
      "ds_read_b128 v[116:119], v14 offset:35328\n"
      "v_lshlrev_b32 v148, 16, v32\n"
      "v_and_b32 v149, 0xffff0000, v32\n"
      "v_lshlrev_b32 v150, 16, v33\n"
      "v_and_b32 v151, 0xffff0000, v33\n"
      "v_mov_b32 v152, 0\n"
      "v_mov_b32 v153, 0\n"
      "v_mov_b32 v154, 0\n"
      "v_mov_b32 v155, 0\n"
      "s_add_u32 s80, s62, 13197312\n"
      "s_add_u32 s81, s63, 15818752\n"
      "s_waitcnt lgkmcnt(14)\n"
      "v_add_f32 v48, v48, v52\n"
      "v_add_f32 v49, v49, v53\n"
      "s_waitcnt lgkmcnt(12)\n"
      "v_add_f32 v56, v56, v60\n"
      "v_add_f32 v57, v57, v61\n"
      "v_add_f32 v50, v50, v54\n"
      "v_add_f32 v51, v51, v55\n"
      "v_add_f32 v58, v58, v62\n"
      "v_add_f32 v59, v59, v63\n"
      "v_add_f32 v128, v48, v56\n"
      "v_add_f32 v129, v49, v57\n"
      "v_add_f32 v130, v50, v58\n"
      "v_add_f32 v131, v51, v59\n"
      "s_waitcnt lgkmcnt(10)\n"
      "v_add_f32 v64, v64, v68\n"
      "v_add_f32 v65, v65, v69\n"
      "s_waitcnt lgkmcnt(8)\n"
      "v_add_f32 v72, v72, v76\n"
      "v_add_f32 v73, v73, v77\n"
      "v_add_f32 v66, v66, v70\n"
      "v_add_f32 v67, v67, v71\n"
      "v_add_f32 v74, v74, v78\n"
      "v_add_f32 v75, v75, v79\n"
      "v_add_f32 v128, v128, v148\n"
      "v_add_f32 v129, v129, v149\n"
      "v_add_f32 v130, v130, v150\n"
      "v_add_f32 v131, v131, v151\n"
      "v_add_f32 v132, v64, v72\n"
      "v_add_f32 v133, v65, v73\n"
      "v_add_f32 v134, v66, v74\n"
      "v_add_f32 v135, v67, v75\n"
      "v_cvt_pk_bf16_f32 v32, v128, v129\n"
      "v_cvt_pk_bf16_f32 v33, v130, v131\n"
      "v_lshlrev_b32 v148, 16, v34\n"
      "v_and_b32 v149, 0xffff0000, v34\n"
      "v_lshlrev_b32 v150, 16, v35\n"
      "v_and_b32 v151, 0xffff0000, v35\n"
      "s_waitcnt lgkmcnt(6)\n"
      "v_add_f32 v88, v88, v92\n"
      "v_add_f32 v89, v89, v93\n"
      "s_waitcnt lgkmcnt(4)\n"
      "v_add_f32 v96, v96, v100\n"
      "v_add_f32 v97, v97, v101\n"
      "v_add_f32 v90, v90, v94\n"
      "v_add_f32 v91, v91, v95\n"
      "v_add_f32 v98, v98, v102\n"
      "v_add_f32 v99, v99, v103\n"
      "v_add_f32 v132, v132, v148\n"
      "v_add_f32 v133, v133, v149\n"
      "v_add_f32 v134, v134, v150\n"
      "v_add_f32 v135, v135, v151\n"
      "v_dot2_f32_bf16 v152, v32, s78, v152\n"
      "v_dot2_f32_bf16 v153, v32, v32, v153\n"
      "v_dot2_f32_bf16 v154, v33, s78, v154\n"
      "v_dot2_f32_bf16 v155, v33, v33, v155\n"
      "v_add_f32 v136, v88, v96\n"
      "v_add_f32 v137, v89, v97\n"
      "v_add_f32 v138, v90, v98\n"
      "v_add_f32 v139, v91, v99\n"
      "v_cvt_pk_bf16_f32 v34, v132, v133\n"
      "v_cvt_pk_bf16_f32 v35, v134, v135\n"
      "v_lshlrev_b32 v148, 16, v36\n"
      "v_and_b32 v149, 0xffff0000, v36\n"
      "v_lshlrev_b32 v150, 16, v37\n"
      "v_and_b32 v151, 0xffff0000, v37\n"
      "s_waitcnt lgkmcnt(2)\n"
      "v_add_f32 v104, v104, v108\n"
      "v_add_f32 v105, v105, v109\n"
      "s_waitcnt lgkmcnt(0)\n"
      "v_add_f32 v112, v112, v116\n"
      "v_add_f32 v113, v113, v117\n"
      "v_add_f32 v106, v106, v110\n"
      "v_add_f32 v107, v107, v111\n"
      "v_add_f32 v114, v114, v118\n"
      "v_add_f32 v115, v115, v119\n"
      "v_add_f32 v136, v136, v148\n"
      "v_add_f32 v137, v137, v149\n"
      "v_add_f32 v138, v138, v150\n"
      "v_add_f32 v139, v139, v151\n"
      "v_dot2_f32_bf16 v152, v34, s78, v152\n"
      "v_dot2_f32_bf16 v153, v34, v34, v153\n"
      "v_dot2_f32_bf16 v154, v35, s78, v154\n"
      "v_dot2_f32_bf16 v155, v35, v35, v155\n"
      "v_add_f32 v140, v104, v112\n"
      "v_add_f32 v141, v105, v113\n"
      "v_add_f32 v142, v106, v114\n"
      "v_add_f32 v143, v107, v115\n"
      "v_cvt_pk_bf16_f32 v36, v136, v137\n"
      "v_cvt_pk_bf16_f32 v37, v138, v139\n"
      "v_lshlrev_b32 v148, 16, v38\n"
      "v_and_b32 v149, 0xffff0000, v38\n"
      "v_lshlrev_b32 v150, 16, v39\n"
      "v_and_b32 v151, 0xffff0000, v39\n"
      "v_add_f32 v140, v140, v148\n"
      "v_add_f32 v141, v141, v149\n"
      "v_add_f32 v142, v142, v150\n"
      "v_add_f32 v143, v143, v151\n"
      "v_dot2_f32_bf16 v152, v36, s78, v152\n"
      "v_dot2_f32_bf16 v153, v36, v36, v153\n"
      "v_dot2_f32_bf16 v154, v37, s78, v154\n"
      "v_dot2_f32_bf16 v155, v37, v37, v155\n"
      "v_cvt_pk_bf16_f32 v38, v140, v141\n"
      "v_cvt_pk_bf16_f32 v39, v142, v143\n"
      "v_dot2_f32_bf16 v152, v38, s78, v152\n"
      "v_dot2_f32_bf16 v153, v38, v38, v153\n"
      "v_dot2_f32_bf16 v154, v39, s78, v154\n"
      "v_dot2_f32_bf16 v155, v39, v39, v155\n"
      "buffer_store_dwordx4 v[32:35], v15, s[16:19], s80 offen sc1\n"
      "buffer_store_dwordx4 v[36:39], v15, s[16:19], s80 offen offset:256 sc1\n"
      "v_add_f32 v152, v152, v154\n"
      "v_add_f32 v153, v153, v155\n"
      "v_cndmask_b32 v152, 0, v152, s[82:83]\n"
      "v_cndmask_b32 v153, 0, v153, s[82:83]\n"
      "s_nop 0\n"
      "v_add_f32_dpp v152, v152, v152 row_ror:8 row_mask:0xf bank_mask:0xf\n"
      "v_add_f32_dpp v153, v153, v153 row_ror:8 row_mask:0xf bank_mask:0xf\n"
      "v_mov_b32 v156, v152\n"
      "v_mov_b32 v157, v153\n"
      "s_nop 0\n"
      "v_permlane16_swap_b32 v156, v152\n"
      "v_permlane16_swap_b32 v157, v153\n"
      "v_add_f32 v152, v152, v156\n"
      "v_add_f32 v153, v153, v157\n"
      "v_mov_b32 v156, v152\n"
      "v_mov_b32 v157, v153\n"
      "s_nop 0\n"
      "v_permlane32_swap_b32 v156, v152\n"
      "v_permlane32_swap_b32 v157, v153\n"
      "v_add_f32 v152, v152, v156\n"
      "v_add_f32 v153, v153, v157\n"
      "buffer_store_dwordx2 v[152:153], v16, s[16:19], s81 offen sc1\n"
      "v_add_f32_dpp v236, v236, v236 row_ror:8 row_mask:0xf bank_mask:0xf\n"
      "v_add_f32_dpp v237, v237, v237 row_ror:8 row_mask:0xf bank_mask:0xf\n"
      "v_add_f32_dpp v238, v238, v238 row_ror:8 row_mask:0xf bank_mask:0xf\n"
      "v_add_f32_dpp v239, v239, v239 row_ror:8 row_mask:0xf bank_mask:0xf\n"
      "v_add_f32_dpp v240, v240, v240 row_ror:8 row_mask:0xf bank_mask:0xf\n"
      "v_add_f32_dpp v241, v241, v241 row_ror:8 row_mask:0xf bank_mask:0xf\n"
      "v_add_f32_dpp v242, v242, v242 row_ror:8 row_mask:0xf bank_mask:0xf\n"
      "v_add_f32_dpp v243, v243, v243 row_ror:8 row_mask:0xf bank_mask:0xf\n"
      "v_add_f32_dpp v236, v236, v236 row_ror:4 row_mask:0xf bank_mask:0xf\n"
      "v_add_f32_dpp v237, v237, v237 row_ror:4 row_mask:0xf bank_mask:0xf\n"
      "v_add_f32_dpp v238, v238, v238 row_ror:4 row_mask:0xf bank_mask:0xf\n"
      "v_add_f32_dpp v239, v239, v239 row_ror:4 row_mask:0xf bank_mask:0xf\n"
      "v_add_f32_dpp v244, v244, v244 row_ror:8 row_mask:0xf bank_mask:0xf\n"
      "v_add_f32_dpp v245, v245, v245 row_ror:8 row_mask:0xf bank_mask:0xf\n"
      "v_add_f32_dpp v246, v246, v246 row_ror:8 row_mask:0xf bank_mask:0xf\n"
      "v_add_f32_dpp v247, v247, v247 row_ror:8 row_mask:0xf bank_mask:0xf\n"
      "v_add_f32_dpp v240, v240, v240 row_ror:4 row_mask:0xf bank_mask:0xf\n"
      "v_add_f32_dpp v241, v241, v241 row_ror:4 row_mask:0xf bank_mask:0xf\n"
      "v_add_f32_dpp v242, v242, v242 row_ror:4 row_mask:0xf bank_mask:0xf\n"
      "v_add_f32_dpp v243, v243, v243 row_ror:4 row_mask:0xf bank_mask:0xf\n"
      "v_add_f32_dpp v236, v236, v236 row_ror:2 row_mask:0xf bank_mask:0xf\n"
      "v_add_f32_dpp v237, v237, v237 row_ror:2 row_mask:0xf bank_mask:0xf\n"
      "v_add_f32_dpp v238, v238, v238 row_ror:2 row_mask:0xf bank_mask:0xf\n"
      "v_add_f32_dpp v239, v239, v239 row_ror:2 row_mask:0xf bank_mask:0xf\n"
      "v_add_f32_dpp v248, v248, v248 row_ror:8 row_mask:0xf bank_mask:0xf\n"
      "v_add_f32_dpp v249, v249, v249 row_ror:8 row_mask:0xf bank_mask:0xf\n"
      "v_add_f32_dpp v250, v250, v250 row_ror:8 row_mask:0xf bank_mask:0xf\n"
      "v_add_f32_dpp v251, v251, v251 row_ror:8 row_mask:0xf bank_mask:0xf\n"
      "v_add_f32_dpp v244, v244, v244 row_ror:4 row_mask:0xf bank_mask:0xf\n"
      "v_add_f32_dpp v245, v245, v245 row_ror:4 row_mask:0xf bank_mask:0xf\n"
      "v_add_f32_dpp v246, v246, v246 row_ror:4 row_mask:0xf bank_mask:0xf\n"
      "v_add_f32_dpp v247, v247, v247 row_ror:4 row_mask:0xf bank_mask:0xf\n"
      "v_add_f32_dpp v240, v240, v240 row_ror:2 row_mask:0xf bank_mask:0xf\n"
      "v_add_f32_dpp v241, v241, v241 row_ror:2 row_mask:0xf bank_mask:0xf\n"
      "v_add_f32_dpp v242, v242, v242 row_ror:2 row_mask:0xf bank_mask:0xf\n"
      "v_add_f32_dpp v243, v243, v243 row_ror:2 row_mask:0xf bank_mask:0xf\n"
      "v_add_f32_dpp v236, v236, v236 row_ror:1 row_mask:0xf bank_mask:0xf\n"
      "v_add_f32_dpp v237, v237, v237 row_ror:1 row_mask:0xf bank_mask:0xf\n"
      "v_add_f32_dpp v238, v238, v238 row_ror:1 row_mask:0xf bank_mask:0xf\n"
      "v_add_f32_dpp v239, v239, v239 row_ror:1 row_mask:0xf bank_mask:0xf\n"
      "v_add_f32_dpp v248, v248, v248 row_ror:4 row_mask:0xf bank_mask:0xf\n"
      "v_add_f32_dpp v249, v249, v249 row_ror:4 row_mask:0xf bank_mask:0xf\n"
      "v_add_f32_dpp v250, v250, v250 row_ror:4 row_mask:0xf bank_mask:0xf\n"
      "v_add_f32_dpp v251, v251, v251 row_ror:4 row_mask:0xf bank_mask:0xf\n"
      "v_add_f32_dpp v244, v244, v244 row_ror:2 row_mask:0xf bank_mask:0xf\n"
      "v_add_f32_dpp v245, v245, v245 row_ror:2 row_mask:0xf bank_mask:0xf\n"
      "v_add_f32_dpp v246, v246, v246 row_ror:2 row_mask:0xf bank_mask:0xf\n"
      "v_add_f32_dpp v247, v247, v247 row_ror:2 row_mask:0xf bank_mask:0xf\n"
      "v_add_f32_dpp v240, v240, v240 row_ror:1 row_mask:0xf bank_mask:0xf\n"
      "v_add_f32_dpp v241, v241, v241 row_ror:1 row_mask:0xf bank_mask:0xf\n"
      "v_add_f32_dpp v242, v242, v242 row_ror:1 row_mask:0xf bank_mask:0xf\n"
      "v_add_f32_dpp v243, v243, v243 row_ror:1 row_mask:0xf bank_mask:0xf\n"
      "ds_write_b128 v24, v[236:239] offset:128\n"
      "v_add_f32_dpp v248, v248, v248 row_ror:2 row_mask:0xf bank_mask:0xf\n"
      "v_add_f32_dpp v249, v249, v249 row_ror:2 row_mask:0xf bank_mask:0xf\n"
      "v_add_f32_dpp v250, v250, v250 row_ror:2 row_mask:0xf bank_mask:0xf\n"
      "v_add_f32_dpp v251, v251, v251 row_ror:2 row_mask:0xf bank_mask:0xf\n"
      "v_add_f32_dpp v244, v244, v244 row_ror:1 row_mask:0xf bank_mask:0xf\n"
      "v_add_f32_dpp v245, v245, v245 row_ror:1 row_mask:0xf bank_mask:0xf\n"
      "v_add_f32_dpp v246, v246, v246 row_ror:1 row_mask:0xf bank_mask:0xf\n"
      "v_add_f32_dpp v247, v247, v247 row_ror:1 row_mask:0xf bank_mask:0xf\n"
      "ds_write_b128 v24, v[240:243] offset:192\n"
      "v_add_f32_dpp v248, v248, v248 row_ror:1 row_mask:0xf bank_mask:0xf\n"
      "v_add_f32_dpp v249, v249, v249 row_ror:1 row_mask:0xf bank_mask:0xf\n"
      "v_add_f32_dpp v250, v250, v250 row_ror:1 row_mask:0xf bank_mask:0xf\n"
      "v_add_f32_dpp v251, v251, v251 row_ror:1 row_mask:0xf bank_mask:0xf\n"
      "ds_write_b128 v24, v[244:247] offset:256\n"
      "ds_write_b128 v24, v[248:251] offset:320\n"
      "s_add_u32 s87, s66, 2756608\n"
      "buffer_load_dwordx2 v[30:31], v19, s[16:19], s87 offen sc1\n"
      "s_waitcnt vmcnt(1)\n"
      "s_barrier\n"
      "s_add_u32 s76, s67, 2760704\n"
      "buffer_store_dwordx2 v[28:29], v21, s[16:19], s76 offen sc1\n"
      "s_waitcnt vmcnt(1)\n"
      "v_cmp_eq_u64 vcc, s[36:37], v[30:31]\n"
      "s_nop 3\n"
      "s_cmp_eq_u64 vcc, -1\n"
      "s_cbranch_scc1 Lpd_9\n"
      "Lps_10:\n"
      "s_sleep 1\n"
      "buffer_load_dwordx2 v[30:31], v19, s[16:19], s87 offen sc1\n"
      "s_waitcnt vmcnt(0)\n"
      "v_cmp_eq_u64 vcc, s[36:37], v[30:31]\n"
      "s_nop 3\n"
      "s_cmp_eq_u64 vcc, -1\n"
      "s_cbranch_scc1 Lpd_9\n"
      "s_add_u32 s42, s42, 1\n"
      "s_cmp_lt_u32 s42, 0x40000\n"
      "s_cbranch_scc1 Lps_10\n"
      "Lpd_9:\n"
      "s_add_u32 s99, s58, 11886592\n"
      "buffer_load_dwordx4 v[220:223], v1, s[16:19], s99 offen sc1\n"
      "buffer_load_dwordx4 v[224:227], v1, s[16:19], s99 offen offset:1024 sc1\n"
      "buffer_load_dwordx4 v[228:231], v1, s[16:19], s99 offen offset:2048 sc1\n"
      "buffer_load_dwordx4 v[232:235], v1, s[16:19], s99 offen offset:3072 sc1\n"
      "s_add_u32 s99, s99, 4096\n"
      "buffer_load_dwordx4 v[236:239], v1, s[16:19], s99 offen sc1\n"
      "s_add_u32 s86, s58, 11907072\n"
      "buffer_load_dwordx4 v[240:243], v1, s[16:19], s86 offen sc1\n"
      "buffer_load_dwordx4 v[244:247], v1, s[16:19], s86 offen offset:1024 sc1\n"
      "buffer_load_dwordx4 v[248:251], v1, s[16:19], s86 offen offset:2048 sc1\n"
      "buffer_load_dwordx4 v[252:255], v1, s[16:19], s86 offen offset:3072 sc1\n"
      "s_add_u32 s86, s86, 4096\n"
      "buffer_load_dwordx4 a[0:3], v1, s[16:19], s86 offen sc1\n"
      "s_waitcnt vmcnt(9)\n"
      "v_mfma_f32_16x16x32_bf16 v[88:91], a[160:163], v[220:223], 0\n"
      "s_add_u32 s87, s66, 2760704\n"
      "buffer_load_dwordx2 v[30:31], v19, s[16:19], s87 offen sc1\n"
      "v_mfma_f32_16x16x32_bf16 v[92:95], a[164:167], v[220:223], 0\n"
      "v_mfma_f32_16x16x32_bf16 v[96:99], a[168:171], v[220:223], 0\n"
      "v_mfma_f32_16x16x32_bf16 v[100:103], a[172:175], v[220:223], 0\n"
      "v_mfma_f32_16x16x32_bf16 v[104:107], a[176:179], v[220:223], 0\n"
      "s_waitcnt vmcnt(9)\n"
      "v_mfma_f32_16x16x32_bf16 v[88:91], a[180:183], v[224:227], v[88:91]\n"
      "v_mfma_f32_16x16x32_bf16 v[92:95], a[184:187], v[224:227], v[92:95]\n"
      "v_mfma_f32_16x16x32_bf16 v[96:99], a[188:191], v[224:227], v[96:99]\n"
      "v_mfma_f32_16x16x32_bf16 v[100:103], a[192:195], v[224:227], v[100:103]\n"
      "v_mfma_f32_16x16x32_bf16 v[104:107], a[196:199], v[224:227], v[104:107]\n"
      "s_waitcnt vmcnt(8)\n"
      "v_mfma_f32_16x16x32_bf16 v[88:91], a[200:203], v[228:231], v[88:91]\n"
      "v_mfma_f32_16x16x32_bf16 v[92:95], a[204:207], v[228:231], v[92:95]\n"
      "v_mfma_f32_16x16x32_bf16 v[96:99], a[208:211], v[228:231], v[96:99]\n"
      "v_mfma_f32_16x16x32_bf16 v[100:103], a[212:215], v[228:231], v[100:103]\n"
      "v_mfma_f32_16x16x32_bf16 v[104:107], a[216:219], v[228:231], v[104:107]\n"
      "s_waitcnt vmcnt(7)\n"
      "v_mfma_f32_16x16x32_bf16 v[88:91], a[220:223], v[232:235], v[88:91]\n"
      "v_mfma_f32_16x16x32_bf16 v[92:95], a[224:227], v[232:235], v[92:95]\n"
      "v_mfma_f32_16x16x32_bf16 v[96:99], a[228:231], v[232:235], v[96:99]\n"
      "v_mfma_f32_16x16x32_bf16 v[100:103], a[232:235], v[232:235], v[100:103]\n"
      "v_mfma_f32_16x16x32_bf16 v[104:107], a[236:239], v[232:235], v[104:107]\n"
      "s_waitcnt vmcnt(6)\n"
      "v_mfma_f32_16x16x32_bf16 v[88:91], a[240:243], v[236:239], v[88:91]\n"
      "v_mfma_f32_16x16x32_bf16 v[92:95], a[244:247], v[236:239], v[92:95]\n"
      "v_mfma_f32_16x16x32_bf16 v[96:99], a[248:251], v[236:239], v[96:99]\n"
      "v_mfma_f32_16x16x32_bf16 v[100:103], a[252:255], v[236:239], v[100:103]\n"
      "v_mfma_f32_16x16x32_bf16 v[104:107], v[176:179], v[236:239], v[104:107]\n"
      "s_waitcnt vmcnt(5)\n"
      "v_mfma_f32_16x16x32_bf16 v[108:111], a[160:163], v[240:243], 0\n"
      "v_mfma_f32_16x16x32_bf16 v[112:115], a[164:167], v[240:243], 0\n"
      "v_mfma_f32_16x16x32_bf16 v[116:119], a[168:171], v[240:243], 0\n"
      "v_mfma_f32_16x16x32_bf16 v[120:123], a[172:175], v[240:243], 0\n"
      "ds_write_b128 v13, v[88:91]\n"
      "ds_write_b128 v13, v[92:95] offset:1152\n"
      "v_mfma_f32_16x16x32_bf16 v[124:127], a[176:179], v[240:243], 0\n"
      "ds_write_b128 v13, v[96:99] offset:2304\n"
      "ds_write_b128 v13, v[100:103] offset:3456\n"
      "s_waitcnt vmcnt(4)\n"
      "v_mfma_f32_16x16x32_bf16 v[108:111], a[180:183], v[244:247], v[108:111]\n"
      "ds_write_b128 v13, v[104:107] offset:4608\n"
      "v_mfma_f32_16x16x32_bf16 v[112:115], a[184:187], v[244:247], v[112:115]\n"
      "v_mfma_f32_16x16x32_bf16 v[116:119], a[188:191], v[244:247], v[116:119]\n"
      "v_mfma_f32_16x16x32_bf16 v[120:123], a[192:195], v[244:247], v[120:123]\n"
      "v_mfma_f32_16x16x32_bf16 v[124:127], a[196:199], v[244:247], v[124:127]\n"
      "s_waitcnt vmcnt(3)\n"
      "v_mfma_f32_16x16x32_bf16 v[108:111], a[200:203], v[248:251], v[108:111]\n"
      "v_mfma_f32_16x16x32_bf16 v[112:115], a[204:207], v[248:251], v[112:115]\n"
      "v_mfma_f32_16x16x32_bf16 v[116:119], a[208:211], v[248:251], v[116:119]\n"
      "v_mfma_f32_16x16x32_bf16 v[120:123], a[212:215], v[248:251], v[120:123]\n"
      "v_mfma_f32_16x16x32_bf16 v[124:127], a[216:219], v[248:251], v[124:127]\n"
      "s_waitcnt vmcnt(2)\n"
      "v_mfma_f32_16x16x32_bf16 v[108:111], a[220:223], v[252:255], v[108:111]\n"
      "v_mfma_f32_16x16x32_bf16 v[112:115], a[224:227], v[252:255], v[112:115]\n"
      "v_mfma_f32_16x16x32_bf16 v[116:119], a[228:231], v[252:255], v[116:119]\n"
      "v_mfma_f32_16x16x32_bf16 v[120:123], a[232:235], v[252:255], v[120:123]\n"
      "v_mfma_f32_16x16x32_bf16 v[124:127], a[236:239], v[252:255], v[124:127]\n"
      "s_waitcnt vmcnt(1)\n"
      "v_mfma_f32_16x16x32_bf16 v[108:111], a[240:243], a[0:3], v[108:111]\n"
      "v_mfma_f32_16x16x32_bf16 v[112:115], a[244:247], a[0:3], v[112:115]\n"
      "v_mfma_f32_16x16x32_bf16 v[116:119], a[248:251], a[0:3], v[116:119]\n"
      "v_mfma_f32_16x16x32_bf16 v[120:123], a[252:255], a[0:3], v[120:123]\n"
      "v_mfma_f32_16x16x32_bf16 v[124:127], v[176:179], a[0:3], v[124:127]\n"
      "s_nop 3\n"
      "ds_write_b128 v13, v[108:111] offset:5760\n"
      "ds_write_b128 v13, v[112:115] offset:6912\n"
      "s_waitcnt vmcnt(0)\n"
      "v_cmp_eq_u64 vcc, s[36:37], v[30:31]\n"
      "s_nop 3\n"
      "s_cmp_eq_u64 vcc, -1\n"
      "s_cbranch_scc1 Lpd_11\n"
      "Lps_12:\n"
      "s_sleep 1\n"
      "buffer_load_dwordx2 v[30:31], v19, s[16:19], s87 offen sc1\n"
      "s_waitcnt vmcnt(0)\n"
      "v_cmp_eq_u64 vcc, s[36:37], v[30:31]\n"
      "s_nop 3\n"
      "s_cmp_eq_u64 vcc, -1\n"
      "s_cbranch_scc1 Lpd_11\n"
      "s_add_u32 s42, s42, 1\n"
      "s_cmp_lt_u32 s42, 0x40000\n"
      "s_cbranch_scc1 Lps_12\n"
      "Lpd_11:\n"
      "s_add_u32 s98, s56, 13197312\n"
      "s_add_u32 m0, s74, 0\n"
      "s_nop 0\n"
      "buffer_load_dwordx4 v1, s[16:19], s98 offen sc1 lds\n"
      "ds_write_b128 v13, v[116:119] offset:8064\n"
      "ds_write_b128 v13, v[120:123] offset:9216\n"
      "buffer_load_dwordx4 v1, s[16:19], s98 offen offset:1024 sc1 lds\n"
      "ds_write_b128 v13, v[124:127] offset:10368\n"
      "s_waitcnt lgkmcnt(0)\n"
      "s_barrier\n"
      "buffer_load_dwordx4 v1, s[16:19], s98 offen offset:2048 sc1 lds\n"
      "ds_read_b128 v[148:151], v14\n"
      "ds_read_b128 v[152:155], v14 offset:11520\n"
      "ds_read_b128 v[156:159], v14 offset:23040\n"
      "ds_read_b128 v[160:163], v14 offset:34560\n"
      "s_add_u32 s80, s62, 14508032\n"
      "s_add_u32 s81, s63, 15884288\n"
      "buffer_load_dwordx4 v1, s[16:19], s98 offen offset:3072 sc1 lds\n"
      "s_waitcnt lgkmcnt(2)\n"
      "v_pk_add_f32 v[148:149], v[148:149], v[152:153]\n"
      "v_pk_add_f32 v[150:151], v[150:151], v[154:155]\n"
      "s_waitcnt lgkmcnt(0)\n"
      "v_pk_add_f32 v[156:157], v[156:157], v[160:161]\n"
      "v_pk_add_f32 v[158:159], v[158:159], v[162:163]\n"
      "v_pk_add_f32 v[112:113], v[148:149], v[156:157]\n"
      "v_pk_add_f32 v[114:115], v[150:151], v[158:159]\n"
      "ds_read_b128 v[148:151], v14 offset:256\n"
      "ds_read_b128 v[152:155], v14 offset:11776\n"
      "ds_read_b128 v[156:159], v14 offset:23296\n"
      "s_add_u32 s98, s56, 13201408\n"
      "s_add_u32 m0, s74, 20480\n"
      "s_nop 0\n"
      "buffer_load_dwordx4 v1, s[16:19], s98 offen sc1 lds\n"
      "ds_read_b128 v[160:163], v14 offset:34816\n"
      "s_waitcnt lgkmcnt(2)\n"
      "v_pk_add_f32 v[148:149], v[148:149], v[152:153]\n"
      "v_pk_add_f32 v[150:151], v[150:151], v[154:155]\n"
      "s_waitcnt lgkmcnt(0)\n"
      "v_pk_add_f32 v[156:157], v[156:157], v[160:161]\n"
      "v_pk_add_f32 v[158:159], v[158:159], v[162:163]\n"
      "v_pk_add_f32 v[116:117], v[148:149], v[156:157]\n"
      "v_pk_add_f32 v[118:119], v[150:151], v[158:159]\n"
      "ds_read_b128 v[148:151], v14 offset:512\n"
      "ds_read_b128 v[152:155], v14 offset:12032\n"
      "buffer_load_dwordx4 v1, s[16:19], s98 offen offset:1024 sc1 lds\n"
      "ds_read_b128 v[156:159], v14 offset:23552\n"
      "ds_read_b128 v[160:163], v14 offset:35072\n"
      "s_waitcnt lgkmcnt(2)\n"
      "v_pk_add_f32 v[148:149], v[148:149], v[152:153]\n"
      "v_pk_add_f32 v[150:151], v[150:151], v[154:155]\n"
      "s_waitcnt lgkmcnt(0)\n"
      "v_pk_add_f32 v[156:157], v[156:157], v[160:161]\n"
      "v_pk_add_f32 v[158:159], v[158:159], v[162:163]\n"
      "v_pk_add_f32 v[120:121], v[148:149], v[156:157]\n"
      "v_pk_add_f32 v[122:123], v[150:151], v[158:159]\n"
      "ds_read_b128 v[148:151], v14 offset:768\n"
      "buffer_load_dwordx4 v1, s[16:19], s98 offen offset:2048 sc1 lds\n"
      "ds_read_b128 v[152:155], v14 offset:12288\n"
      "ds_read_b128 v[156:159], v14 offset:23808\n"
      "ds_read_b128 v[160:163], v14 offset:35328\n"
      "s_waitcnt lgkmcnt(2)\n"
      "v_pk_add_f32 v[148:149], v[148:149], v[152:153]\n"
      "v_pk_add_f32 v[150:151], v[150:151], v[154:155]\n"
      "s_waitcnt lgkmcnt(0)\n"
      "v_pk_add_f32 v[156:157], v[156:157], v[160:161]\n"
      "v_pk_add_f32 v[158:159], v[158:159], v[162:163]\n"
      "v_pk_add_f32 v[124:125], v[148:149], v[156:157]\n"
      "buffer_load_dwordx4 v1, s[16:19], s98 offen offset:3072 sc1 lds\n"
      "v_pk_add_f32 v[126:127], v[150:151], v[158:159]\n"
      "v_lshlrev_b32 v148, 16, v40\n"
      "v_and_b32 v149, 0xffff0000, v40\n"
      "v_lshlrev_b32 v150, 16, v41\n"
      "v_and_b32 v151, 0xffff0000, v41\n"
      "v_pk_add_f32 v[112:113], v[112:113], v[148:149]\n"
      "v_pk_add_f32 v[114:115], v[114:115], v[150:151]\n"
      "v_cvt_pk_bf16_f32 v40, v112, v113\n"
      "v_cvt_pk_bf16_f32 v41, v114, v115\n"
      "v_lshlrev_b32 v148, 16, v42\n"
      "v_and_b32 v149, 0xffff0000, v42\n"
      "v_lshlrev_b32 v150, 16, v43\n"
      "v_and_b32 v151, 0xffff0000, v43\n"
      "s_add_u32 s98, s56, 13205504\n"
      "s_add_u32 m0, s74, 40960\n"
      "s_nop 0\n"
      "buffer_load_dwordx4 v1, s[16:19], s98 offen sc1 lds\n"
      "v_mov_b32 v152, 0\n"
      "v_mov_b32 v153, 0\n"
      "v_mov_b32 v154, 0\n"
      "v_mov_b32 v155, 0\n"
      "v_pk_add_f32 v[116:117], v[116:117], v[148:149]\n"
      "v_pk_add_f32 v[118:119], v[118:119], v[150:151]\n"
      "v_dot2_f32_bf16 v152, v40, s78, v152\n"
      "v_dot2_f32_bf16 v153, v40, v40, v153\n"
      "v_dot2_f32_bf16 v154, v41, s78, v154\n"
      "v_dot2_f32_bf16 v155, v41, v41, v155\n"
      "v_cvt_pk_bf16_f32 v42, v116, v117\n"
      "v_cvt_pk_bf16_f32 v43, v118, v119\n"
      "v_lshlrev_b32 v148, 16, v44\n"
      "buffer_load_dwordx4 v1, s[16:19], s98 offen offset:1024 sc1 lds\n"
      "v_and_b32 v149, 0xffff0000, v44\n"
      "v_lshlrev_b32 v150, 16, v45\n"
      "v_and_b32 v151, 0xffff0000, v45\n"
      "v_pk_add_f32 v[120:121], v[120:121], v[148:149]\n"
      "v_pk_add_f32 v[122:123], v[122:123], v[150:151]\n"
      "v_dot2_f32_bf16 v152, v42, s78, v152\n"
      "v_dot2_f32_bf16 v153, v42, v42, v153\n"
      "v_dot2_f32_bf16 v154, v43, s78, v154\n"
      "v_dot2_f32_bf16 v155, v43, v43, v155\n"
      "v_cvt_pk_bf16_f32 v44, v120, v121\n"
      "v_cvt_pk_bf16_f32 v45, v122, v123\n"
      "v_lshlrev_b32 v148, 16, v46\n"
      "v_and_b32 v149, 0xffff0000, v46\n"
      "buffer_load_dwordx4 v1, s[16:19], s98 offen offset:2048 sc1 lds\n"
      "v_lshlrev_b32 v150, 16, v47\n"
      "v_and_b32 v151, 0xffff0000, v47\n"
      "v_pk_add_f32 v[124:125], v[124:125], v[148:149]\n"
      "v_pk_add_f32 v[126:127], v[126:127], v[150:151]\n"
      "v_dot2_f32_bf16 v152, v44, s78, v152\n"
      "v_dot2_f32_bf16 v153, v44, v44, v153\n"
      "v_dot2_f32_bf16 v154, v45, s78, v154\n"
      "v_dot2_f32_bf16 v155, v45, v45, v155\n"
      "v_cvt_pk_bf16_f32 v46, v124, v125\n"
      "v_cvt_pk_bf16_f32 v47, v126, v127\n"
      "v_dot2_f32_bf16 v152, v46, s78, v152\n"
      "v_dot2_f32_bf16 v153, v46, v46, v153\n"
      "v_dot2_f32_bf16 v154, v47, s78, v154\n"
      "buffer_load_dwordx4 v1, s[16:19], s98 offen offset:3072 sc1 lds\n"
      "v_dot2_f32_bf16 v155, v47, v47, v155\n"
      "buffer_store_dwordx4 v[40:43], v15, s[16:19], s80 offen sc1\n"
      "buffer_store_dwordx4 v[44:47], v15, s[16:19], s80 offen offset:256 sc1\n"
      "s_nop 0\n"
      "v_pk_add_f32 v[152:153], v[152:153], v[154:155]\n"
      "v_cndmask_b32 v152, 0, v152, s[82:83]\n"
      "v_cndmask_b32 v153, 0, v153, s[82:83]\n"
      "s_nop 0\n"
      "v_add_f32_dpp v152, v152, v152 row_ror:8 row_mask:0xf bank_mask:0xf\n"
      "v_add_f32_dpp v153, v153, v153 row_ror:8 row_mask:0xf bank_mask:0xf\n"
      "v_mov_b32 v156, v152\n"
      "v_mov_b32 v157, v153\n"
      "s_nop 0\n"
      "v_permlane16_swap_b32 v156, v152\n"
      "v_permlane16_swap_b32 v157, v153\n"
      "v_add_f32 v152, v152, v156\n"
      "s_add_u32 s98, s56, 13209600\n"
      "s_add_u32 m0, s74, 61440\n"
      "s_nop 0\n"
      "buffer_load_dwordx4 v1, s[16:19], s98 offen sc1 lds\n"
      "v_add_f32 v153, v153, v157\n"
      "v_mov_b32 v156, v152\n"
      "v_mov_b32 v157, v153\n"
      "s_nop 0\n"
      "v_permlane32_swap_b32 v156, v152\n"
      "v_permlane32_swap_b32 v157, v153\n"
      "v_add_f32 v152, v152, v156\n"
      "v_add_f32 v153, v153, v157\n"
      "buffer_store_dwordx2 v[152:153], v16, s[16:19], s81 offen sc1\n"
      "; ==== G0\n"
      "s_waitcnt lgkmcnt(0)\n"
      "s_waitcnt vmcnt(12)\n"
      "s_barrier\n"
      "ds_read_b128 v[128:131], v1\n"
      "ds_read_b128 v[132:135], v1 offset:4096\n"
      "ds_read_b128 v[136:139], v1 offset:8192\n"
      "ds_read_b128 v[140:143], v1 offset:12288\n"
      "s_waitcnt lgkmcnt(3)\n"
      "v_mfma_f32_16x16x32_bf16 v[48:51], a[4:7], v[128:131], 0\n"
      "buffer_load_dwordx4 a[124:127], v1, s[16:19], s88 offen offset:2048 sc1\n"
      "v_mfma_f32_16x16x32_bf16 v[52:55], a[8:11], v[128:131], 0\n"
      "v_mfma_f32_16x16x32_bf16 v[56:59], a[12:15], v[128:131], 0\n"
      "v_mfma_f32_16x16x32_bf16 v[60:63], a[16:19], v[128:131], 0\n"
      "ds_read_b128 v[128:131], v1 offset:1024\n"
      "s_waitcnt lgkmcnt(3)\n"
      "v_mfma_f32_16x16x32_bf16 v[64:67], a[4:7], v[132:135], 0\n"
      "buffer_load_dwordx4 a[128:131], v1, s[16:19], s88 offen offset:3072 sc1\n"
      "v_mfma_f32_16x16x32_bf16 v[68:71], a[8:11], v[132:135], 0\n"
      "v_mfma_f32_16x16x32_bf16 v[72:75], a[12:15], v[132:135], 0\n"
      "v_mfma_f32_16x16x32_bf16 v[76:79], a[16:19], v[132:135], 0\n"
      "ds_read_b128 v[132:135], v1 offset:5120\n"
      "s_waitcnt lgkmcnt(3)\n"
      "v_mfma_f32_16x16x32_bf16 v[80:83], a[4:7], v[136:139], 0\n"
      "s_add_u32 s88, s88, 4096\n"
      "buffer_load_dwordx4 a[132:135], v1, s[16:19], s88 offen sc1\n"
      "v_mfma_f32_16x16x32_bf16 v[84:87], a[8:11], v[136:139], 0\n"
      "v_mfma_f32_16x16x32_bf16 v[88:91], a[12:15], v[136:139], 0\n"
      "v_mfma_f32_16x16x32_bf16 v[92:95], a[16:19], v[136:139], 0\n"
      "ds_read_b128 v[136:139], v1 offset:9216\n"
      "s_waitcnt lgkmcnt(3)\n"
      "v_mfma_f32_16x16x32_bf16 v[96:99], a[4:7], v[140:143], 0\n"
      "buffer_load_dwordx4 a[136:139], v1, s[16:19], s88 offen offset:1024 sc1\n"
      "v_mfma_f32_16x16x32_bf16 v[100:103], a[8:11], v[140:143], 0\n"
      "v_mfma_f32_16x16x32_bf16 v[104:107], a[12:15], v[140:143], 0\n"
      "v_mfma_f32_16x16x32_bf16 v[108:111], a[16:19], v[140:143], 0\n"
      "ds_read_b128 v[140:143], v1 offset:13312\n"
      "s_waitcnt lgkmcnt(3)\n"
      "v_mfma_f32_16x16x32_bf16 v[48:51], a[20:23], v[128:131], v[48:51]\n"
      "buffer_load_dwordx4 a[140:143], v1, s[16:19], s88 offen offset:2048 sc1\n"
      "v_mfma_f32_16x16x32_bf16 v[52:55], a[24:27], v[128:131], v[52:55]\n"
      "v_mfma_f32_16x16x32_bf16 v[56:59], a[28:31], v[128:131], v[56:59]\n"
      "v_mfma_f32_16x16x32_bf16 v[60:63], a[32:35], v[128:131], v[60:63]\n"
      "s_waitcnt vmcnt(5)\n"
      "s_barrier\n"
      "s_add_u32 s76, s67, 2764800\n"
      "buffer_store_dwordx2 v[28:29], v21, s[16:19], s76 offen sc1\n"
      "ds_read_b128 v[128:131], v1 offset:2048\n"
      "s_waitcnt lgkmcnt(3)\n"
      "v_mfma_f32_16x16x32_bf16 v[64:67], a[20:23], v[132:135], v[64:67]\n"
      "v_mfma_f32_16x16x32_bf16 v[68:71], a[24:27], v[132:135], v[68:71]\n"
      "v_mfma_f32_16x16x32_bf16 v[72:75], a[28:31], v[132:135], v[72:75]\n"
      "v_mfma_f32_16x16x32_bf16 v[76:79], a[32:35], v[132:135], v[76:79]\n"
      "buffer_load_dwordx4 a[144:147], v1, s[16:19], s88 offen offset:3072 sc1\n"
      "ds_read_b128 v[132:135], v1 offset:6144\n"
      "s_waitcnt lgkmcnt(3)\n"
      "v_mfma_f32_16x16x32_bf16 v[80:83], a[20:23], v[136:139], v[80:83]\n"
      "v_mfma_f32_16x16x32_bf16 v[84:87], a[24:27], v[136:139], v[84:87]\n"
      "v_mfma_f32_16x16x32_bf16 v[88:91], a[28:31], v[136:139], v[88:91]\n"
      "v_mfma_f32_16x16x32_bf16 v[92:95], a[32:35], v[136:139], v[92:95]\n"
      "s_add_u32 s88, s88, 4096\n"
      "buffer_load_dwordx4 a[148:151], v1, s[16:19], s88 offen sc1\n"
      "ds_read_b128 v[136:139], v1 offset:10240\n"
      "s_waitcnt lgkmcnt(3)\n"
      "v_mfma_f32_16x16x32_bf16 v[96:99], a[20:23], v[140:143], v[96:99]\n"
      "v_mfma_f32_16x16x32_bf16 v[100:103], a[24:27], v[140:143], v[100:103]\n"
      "v_mfma_f32_16x16x32_bf16 v[104:107], a[28:31], v[140:143], v[104:107]\n"
      "v_mfma_f32_16x16x32_bf16 v[108:111], a[32:35], v[140:143], v[108:111]\n"
      "buffer_load_dwordx4 a[152:155], v1, s[16:19], s88 offen offset:1024 sc1\n"
      "ds_read_b128 v[140:143], v1 offset:14336\n"
      "s_waitcnt lgkmcnt(3)\n"
      "v_mfma_f32_16x16x32_bf16 v[48:51], a[36:39], v[128:131], v[48:51]\n"
      "v_mfma_f32_16x16x32_bf16 v[52:55], a[40:43], v[128:131], v[52:55]\n"
      "v_mfma_f32_16x16x32_bf16 v[56:59], a[44:47], v[128:131], v[56:59]\n"
      "v_mfma_f32_16x16x32_bf16 v[60:63], a[48:51], v[128:131], v[60:63]\n"
      "buffer_load_dwordx4 a[156:159], v1, s[16:19], s88 offen offset:2048 sc1\n"
      "ds_read_b128 v[128:131], v1 offset:3072\n"
      "s_waitcnt lgkmcnt(3)\n"
      "v_mfma_f32_16x16x32_bf16 v[64:67], a[36:39], v[132:135], v[64:67]\n"
      "v_mfma_f32_16x16x32_bf16 v[68:71], a[40:43], v[132:135], v[68:71]\n"
      "v_mfma_f32_16x16x32_bf16 v[72:75], a[44:47], v[132:135], v[72:75]\n"
      "v_mfma_f32_16x16x32_bf16 v[76:79], a[48:51], v[132:135], v[76:79]\n"
      "buffer_load_dwordx4 a[160:163], v1, s[16:19], s88 offen offset:3072 sc1\n"
      "ds_read_b128 v[132:135], v1 offset:7168\n"
      "s_waitcnt lgkmcnt(3)\n"
      "v_mfma_f32_16x16x32_bf16 v[80:83], a[36:39], v[136:139], v[80:83]\n"
      "v_mfma_f32_16x16x32_bf16 v[84:87], a[40:43], v[136:139], v[84:87]\n"
      "v_mfma_f32_16x16x32_bf16 v[88:91], a[44:47], v[136:139], v[88:91]\n"
      "v_mfma_f32_16x16x32_bf16 v[92:95], a[48:51], v[136:139], v[92:95]\n"
      "s_add_u32 s88, s88, 4096\n"
      "buffer_load_dwordx4 a[164:167], v1, s[16:19], s88 offen sc1\n"
      "ds_read_b128 v[136:139], v1 offset:11264\n"
      "s_waitcnt lgkmcnt(3)\n"
      "v_mfma_f32_16x16x32_bf16 v[96:99], a[36:39], v[140:143], v[96:99]\n"
      "v_mfma_f32_16x16x32_bf16 v[100:103], a[40:43], v[140:143], v[100:103]\n"
      "v_mfma_f32_16x16x32_bf16 v[104:107], a[44:47], v[140:143], v[104:107]\n"
      "v_mfma_f32_16x16x32_bf16 v[108:111], a[48:51], v[140:143], v[108:111]\n"
      "buffer_load_dwordx4 a[168:171], v1, s[16:19], s88 offen offset:1024 sc1\n"
      "ds_read_b128 v[140:143], v1 offset:15360\n"
      "s_waitcnt lgkmcnt(3)\n"
      "v_mfma_f32_16x16x32_bf16 v[48:51], a[52:55], v[128:131], v[48:51]\n"
      "v_mfma_f32_16x16x32_bf16 v[52:55], a[56:59], v[128:131], v[52:55]\n"
      "v_mfma_f32_16x16x32_bf16 v[56:59], a[60:63], v[128:131], v[56:59]\n"
      "v_mfma_f32_16x16x32_bf16 v[60:63], a[64:67], v[128:131], v[60:63]\n"
      "s_add_u32 s77, s64, 15818752\n"
      "buffer_load_dwordx2 v[144:145], v17, s[16:19], s77 offen sc1\n"
      "buffer_load_dwordx2 v[146:147], v17, s[16:19], s77 offen offset:128 sc1\n"
      "s_waitcnt lgkmcnt(2)\n"
      "v_mfma_f32_16x16x32_bf16 v[64:67], a[52:55], v[132:135], v[64:67]\n"
      "v_mfma_f32_16x16x32_bf16 v[68:71], a[56:59], v[132:135], v[68:71]\n"
      "v_mfma_f32_16x16x32_bf16 v[72:75], a[60:63], v[132:135], v[72:75]\n"
      "v_mfma_f32_16x16x32_bf16 v[76:79], a[64:67], v[132:135], v[76:79]\n"
      "buffer_load_dwordx4 a[172:175], v1, s[16:19], s88 offen offset:2048 sc1\n"
      "s_waitcnt lgkmcnt(0)\n"
      "s_barrier\n"
      "ds_read_b128 v[128:131], v1 offset:20480\n"
      "ds_read_b128 v[132:135], v1 offset:24576\n"
      "v_mfma_f32_16x16x32_bf16 v[80:83], a[52:55], v[136:139], v[80:83]\n"
      "buffer_load_dwordx4 a[176:179], v1, s[16:19], s88 offen offset:3072 sc1\n"
      "v_mfma_f32_16x16x32_bf16 v[84:87], a[56:59], v[136:139], v[84:87]\n"
      "v_mfma_f32_16x16x32_bf16 v[88:91], a[60:63], v[136:139], v[88:91]\n"
      "v_mfma_f32_16x16x32_bf16 v[92:95], a[64:67], v[136:139], v[92:95]\n"
      "ds_read_b128 v[136:139], v1 offset:28672\n"
      "v_mfma_f32_16x16x32_bf16 v[96:99], a[52:55], v[140:143], v[96:99]\n"
      "s_add_u32 s88, s88, 4096\n"
      "buffer_load_dwordx4 a[180:183], v1, s[16:19], s88 offen sc1\n"
      "v_mfma_f32_16x16x32_bf16 v[100:103], a[56:59], v[140:143], v[100:103]\n"
      "v_mfma_f32_16x16x32_bf16 v[104:107], a[60:63], v[140:143], v[104:107]\n"
      "v_mfma_f32_16x16x32_bf16 v[108:111], a[64:67], v[140:143], v[108:111]\n"
      "ds_read_b128 v[140:143], v1 offset:32768\n"
      "s_waitcnt lgkmcnt(3)\n"
      "v_mfma_f32_16x16x32_bf16 v[48:51], a[68:71], v[128:131], v[48:51]\n"
      "buffer_load_dwordx4 a[184:187], v1, s[16:19], s88 offen offset:1024 sc1\n"
      "v_mfma_f32_16x16x32_bf16 v[52:55], a[72:75], v[128:131], v[52:55]\n"
      "v_mfma_f32_16x16x32_bf16 v[56:59], a[76:79], v[128:131], v[56:59]\n"
      "v_mfma_f32_16x16x32_bf16 v[60:63], a[80:83], v[128:131], v[60:63]\n"
      "ds_read_b128 v[128:131], v1 offset:21504\n"
      "s_waitcnt lgkmcnt(3)\n"
      "v_mfma_f32_16x16x32_bf16 v[64:67], a[68:71], v[132:135], v[64:67]\n"
      "buffer_load_dwordx4 a[188:191], v1, s[16:19], s88 offen offset:2048 sc1\n"
      "v_mfma_f32_16x16x32_bf16 v[68:71], a[72:75], v[132:135], v[68:71]\n"
      "v_mfma_f32_16x16x32_bf16 v[72:75], a[76:79], v[132:135], v[72:75]\n"
      "v_mfma_f32_16x16x32_bf16 v[76:79], a[80:83], v[132:135], v[76:79]\n"
      "ds_read_b128 v[132:135], v1 offset:25600\n"
      "s_waitcnt lgkmcnt(3)\n"
      "v_mfma_f32_16x16x32_bf16 v[80:83], a[68:71], v[136:139], v[80:83]\n"
      "buffer_load_dwordx4 a[192:195], v1, s[16:19], s88 offen offset:3072 sc1\n"
      "v_mfma_f32_16x16x32_bf16 v[84:87], a[72:75], v[136:139], v[84:87]\n"
      "v_mfma_f32_16x16x32_bf16 v[88:91], a[76:79], v[136:139], v[88:91]\n"
      "v_mfma_f32_16x16x32_bf16 v[92:95], a[80:83], v[136:139], v[92:95]\n"
      "ds_read_b128 v[136:139], v1 offset:29696\n"
      "s_waitcnt lgkmcnt(3)\n"
      "v_mfma_f32_16x16x32_bf16 v[96:99], a[68:71], v[140:143], v[96:99]\n"
      "s_add_u32 s88, s88, 4096\n"
      "buffer_load_dwordx4 a[196:199], v1, s[16:19], s88 offen sc1\n"
      "v_mfma_f32_16x16x32_bf16 v[100:103], a[72:75], v[140:143], v[100:103]\n"
      "v_mfma_f32_16x16x32_bf16 v[104:107], a[76:79], v[140:143], v[104:107]\n"
      "v_mfma_f32_16x16x32_bf16 v[108:111], a[80:83], v[140:143], v[108:111]\n"
      "ds_read_b128 v[140:143], v1 offset:33792\n"
      "s_waitcnt lgkmcnt(3)\n"
      "v_mfma_f32_16x16x32_bf16 v[48:51], a[84:87], v[128:131], v[48:51]\n"
      "buffer_load_dwordx4 a[200:203], v1, s[16:19], s88 offen offset:1024 sc1\n"
      "v_mfma_f32_16x16x32_bf16 v[52:55], a[88:91], v[128:131], v[52:55]\n"
      "v_mfma_f32_16x16x32_bf16 v[56:59], a[92:95], v[128:131], v[56:59]\n"
      "v_mfma_f32_16x16x32_bf16 v[60:63], a[96:99], v[128:131], v[60:63]\n"
      "ds_read_b128 v[128:131], v1 offset:22528\n"
      "s_waitcnt lgkmcnt(3)\n"
      "v_mfma_f32_16x16x32_bf16 v[64:67], a[84:87], v[132:135], v[64:67]\n"
      "buffer_load_dwordx4 a[204:207], v1, s[16:19], s88 offen offset:2048 sc1\n"
      "v_mfma_f32_16x16x32_bf16 v[68:71], a[88:91], v[132:135], v[68:71]\n"
      "v_mfma_f32_16x16x32_bf16 v[72:75], a[92:95], v[132:135], v[72:75]\n"
      "v_mfma_f32_16x16x32_bf16 v[76:79], a[96:99], v[132:135], v[76:79]\n"
      "ds_read_b128 v[132:135], v1 offset:26624\n"
      "s_waitcnt lgkmcnt(3)\n"
      "v_mfma_f32_16x16x32_bf16 v[80:83], a[84:87], v[136:139], v[80:83]\n"
      "buffer_load_dwordx4 a[208:211], v1, s[16:19], s88 offen offset:3072 sc1\n"
      "v_mfma_f32_16x16x32_bf16 v[84:87], a[88:91], v[136:139], v[84:87]\n"
      "v_mfma_f32_16x16x32_bf16 v[88:91], a[92:95], v[136:139], v[88:91]\n"
      "v_mfma_f32_16x16x32_bf16 v[92:95], a[96:99], v[136:139], v[92:95]\n"
      "ds_read_b128 v[136:139], v1 offset:30720\n"
      "s_waitcnt lgkmcnt(3)\n"
      "v_mfma_f32_16x16x32_bf16 v[96:99], a[84:87], v[140:143], v[96:99]\n"
      "s_add_u32 s88, s88, 4096\n"
      "buffer_load_dwordx4 a[212:215], v1, s[16:19], s88 offen sc1\n"
      "v_mfma_f32_16x16x32_bf16 v[100:103], a[88:91], v[140:143], v[100:103]\n"
      "v_mfma_f32_16x16x32_bf16 v[104:107], a[92:95], v[140:143], v[104:107]\n"
      "v_mfma_f32_16x16x32_bf16 v[108:111], a[96:99], v[140:143], v[108:111]\n"
      "ds_read_b128 v[140:143], v1 offset:34816\n"
      "s_waitcnt lgkmcnt(3)\n"
      "v_mfma_f32_16x16x32_bf16 v[48:51], a[100:103], v[128:131], v[48:51]\n"
      "buffer_load_dwordx4 a[216:219], v1, s[16:19], s88 offen offset:1024 sc1\n"
      "v_mfma_f32_16x16x32_bf16 v[52:55], a[104:107], v[128:131], v[52:55]\n"
      "v_mfma_f32_16x16x32_bf16 v[56:59], a[108:111], v[128:131], v[56:59]\n"
      "v_mfma_f32_16x16x32_bf16 v[60:63], a[112:115], v[128:131], v[60:63]\n"
      "ds_read_b128 v[128:131], v1 offset:23552\n"
      "s_waitcnt lgkmcnt(3)\n"
      "v_mfma_f32_16x16x32_bf16 v[64:67], a[100:103], v[132:135], v[64:67]\n"
      "buffer_load_dwordx4 a[220:223], v1, s[16:19], s88 offen offset:2048 sc1\n"
      "v_mfma_f32_16x16x32_bf16 v[68:71], a[104:107], v[132:135], v[68:71]\n"
      "v_mfma_f32_16x16x32_bf16 v[72:75], a[108:111], v[132:135], v[72:75]\n"
      "v_mfma_f32_16x16x32_bf16 v[76:79], a[112:115], v[132:135], v[76:79]\n"
      "ds_read_b128 v[132:135], v1 offset:27648\n"
      "s_waitcnt lgkmcnt(3)\n"
      "v_mfma_f32_16x16x32_bf16 v[80:83], a[100:103], v[136:139], v[80:83]\n"
      "buffer_load_dwordx4 a[224:227], v1, s[16:19], s88 offen offset:3072 sc1\n"
      "v_mfma_f32_16x16x32_bf16 v[84:87], a[104:107], v[136:139], v[84:87]\n"
      "v_mfma_f32_16x16x32_bf16 v[88:91], a[108:111], v[136:139], v[88:91]\n"
      "v_mfma_f32_16x16x32_bf16 v[92:95], a[112:115], v[136:139], v[92:95]\n"
      "ds_read_b128 v[136:139], v1 offset:31744\n"
      "s_waitcnt lgkmcnt(3)\n"
      "v_mfma_f32_16x16x32_bf16 v[96:99], a[100:103], v[140:143], v[96:99]\n"
      "s_add_u32 s88, s88, 4096\n"
      "buffer_load_dwordx4 a[228:231], v1, s[16:19], s88 offen sc1\n"
      "v_mfma_f32_16x16x32_bf16 v[100:103], a[104:107], v[140:143], v[100:103]\n"
      "v_mfma_f32_16x16x32_bf16 v[104:107], a[108:111], v[140:143], v[104:107]\n"
      "s_waitcnt vmcnt(15)\n"
      "v_add_f32 v144, v144, v146\n"
      "v_add_f32 v145, v145, v147\n"
      "v_mfma_f32_16x16x32_bf16 v[108:111], a[112:115], v[140:143], v[108:111]\n"
      "v_mov_b32 v156, v144\n"
      "v_mov_b32 v157, v145\n"
      "ds_read_b128 v[140:143], v1 offset:35840\n"
      "s_waitcnt lgkmcnt(3)\n"
      "v_mfma_f32_16x16x32_bf16 v[48:51], a[116:119], v[128:131], v[48:51]\n"
      "buffer_load_dwordx4 a[232:235], v1, s[16:19], s88 offen offset:1024 sc1\n"
      "v_permlane16_swap_b32 v156, v144\n"
      "v_permlane16_swap_b32 v157, v145\n"
      "v_mfma_f32_16x16x32_bf16 v[52:55], a[120:123], v[128:131], v[52:55]\n"
      "v_add_f32 v144, v144, v156\n"
      "v_add_f32 v145, v145, v157\n"
      "v_mfma_f32_16x16x32_bf16 v[56:59], a[124:127], v[128:131], v[56:59]\n"
      "v_mov_b32 v156, v144\n"
      "v_mov_b32 v157, v145\n"
      "v_mfma_f32_16x16x32_bf16 v[60:63], a[128:131], v[128:131], v[60:63]\n"
      "v_permlane32_swap_b32 v156, v144\n"
      "v_permlane32_swap_b32 v157, v145\n"
      "s_waitcnt lgkmcnt(2)\n"
      "v_mfma_f32_16x16x32_bf16 v[64:67], a[116:119], v[132:135], v[64:67]\n"
      "buffer_load_dwordx4 a[236:239], v1, s[16:19], s88 offen offset:2048 sc1\n"
      "v_add_f32 v144, v144, v156\n"
      "v_add_f32 v145, v145, v157\n"
      "v_mfma_f32_16x16x32_bf16 v[68:71], a[120:123], v[132:135], v[68:71]\n"
      "v_mul_f32 v146, 0x3acccccd, v144\n"
      "v_mul_f32 v145, 0x3acccccd, v145\n"
      "v_mfma_f32_16x16x32_bf16 v[72:75], a[124:127], v[132:135], v[72:75]\n"
      "v_sub_f32 v159, 0, v146\n"
      "v_fma_f32 v145, -v146, v146, v145\n"
      "v_mfma_f32_16x16x32_bf16 v[76:79], a[128:131], v[132:135], v[76:79]\n"
      "v_max_f32 v145, 0, v145\n"
      "v_add_f32 v145, 0x3727c5ac, v145\n"
      "s_waitcnt lgkmcnt(0)\n"
      "s_barrier\n"
      "ds_read_b128 v[128:131], v1 offset:40960\n"
      "ds_read_b128 v[132:135], v1 offset:45056\n"
      "v_mfma_f32_16x16x32_bf16 v[80:83], a[116:119], v[136:139], v[80:83]\n"
      "buffer_load_dwordx4 a[240:243], v1, s[16:19], s88 offen offset:3072 sc1\n"
      "v_rcp_f32 v158, v145\n"
      "v_mfma_f32_16x16x32_bf16 v[84:87], a[120:123], v[136:139], v[84:87]\n"
      "ds_write_b64 v26, v[158:159]\n"
      "v_mfma_f32_16x16x32_bf16 v[88:91], a[124:127], v[136:139], v[88:91]\n"
      "v_mfma_f32_16x16x32_bf16 v[92:95], a[128:131], v[136:139], v[92:95]\n"
      "ds_read_b128 v[136:139], v1 offset:49152\n"
      "v_mfma_f32_16x16x32_bf16 v[96:99], a[116:119], v[140:143], v[96:99]\n"
      "s_add_u32 s88, s88, 4096\n"
      "buffer_load_dwordx4 a[244:247], v1, s[16:19], s88 offen sc1\n"
      "v_mfma_f32_16x16x32_bf16 v[100:103], a[120:123], v[140:143], v[100:103]\n"
      "v_mfma_f32_16x16x32_bf16 v[104:107], a[124:127], v[140:143], v[104:107]\n"
      "v_mfma_f32_16x16x32_bf16 v[108:111], a[128:131], v[140:143], v[108:111]\n"
      "ds_read_b128 v[140:143], v1 offset:53248\n"
      "s_waitcnt lgkmcnt(4)\n"
      "v_mfma_f32_16x16x32_bf16 v[48:51], a[132:135], v[128:131], v[48:51]\n"
      "buffer_load_dwordx4 a[248:251], v1, s[16:19], s88 offen offset:1024 sc1\n"
      "v_mfma_f32_16x16x32_bf16 v[52:55], a[136:139], v[128:131], v[52:55]\n"
      "v_mfma_f32_16x16x32_bf16 v[56:59], a[140:143], v[128:131], v[56:59]\n"
      "v_mfma_f32_16x16x32_bf16 v[60:63], a[144:147], v[128:131], v[60:63]\n"
      "ds_read_b128 v[128:131], v1 offset:41984\n"
      "s_waitcnt lgkmcnt(4)\n"
      "v_mfma_f32_16x16x32_bf16 v[64:67], a[132:135], v[132:135], v[64:67]\n"
      "buffer_load_dwordx4 v1, s[16:19], s98 offen offset:1024 sc1 lds\n"
      "v_mfma_f32_16x16x32_bf16 v[68:71], a[136:139], v[132:135], v[68:71]\n"
      "v_mfma_f32_16x16x32_bf16 v[72:75], a[140:143], v[132:135], v[72:75]\n"
      "v_mfma_f32_16x16x32_bf16 v[76:79], a[144:147], v[132:135], v[76:79]\n"
      "ds_read_b128 v[132:135], v1 offset:46080\n"
      "s_waitcnt lgkmcnt(3)\n"
      "v_mfma_f32_16x16x32_bf16 v[80:83], a[132:135], v[136:139], v[80:83]\n"
      "buffer_load_dwordx4 v1, s[16:19], s98 offen offset:2048 sc1 lds\n"
      "v_mfma_f32_16x16x32_bf16 v[84:87], a[136:139], v[136:139], v[84:87]\n"
      "v_mfma_f32_16x16x32_bf16 v[88:91], a[140:143], v[136:139], v[88:91]\n"
      "v_mfma_f32_16x16x32_bf16 v[92:95], a[144:147], v[136:139], v[92:95]\n"
      "ds_read_b128 v[136:139], v1 offset:50176\n"
      "s_waitcnt lgkmcnt(3)\n"
      "v_mfma_f32_16x16x32_bf16 v[96:99], a[132:135], v[140:143], v[96:99]\n"
      "buffer_load_dwordx4 v1, s[16:19], s98 offen offset:3072 sc1 lds\n"
      "v_mfma_f32_16x16x32_bf16 v[100:103], a[136:139], v[140:143], v[100:103]\n"
      "v_mfma_f32_16x16x32_bf16 v[104:107], a[140:143], v[140:143], v[104:107]\n"
      "v_mfma_f32_16x16x32_bf16 v[108:111], a[144:147], v[140:143], v[108:111]\n"
      "ds_read_b128 v[140:143], v1 offset:54272\n"
      "s_waitcnt lgkmcnt(3)\n"
      "v_mfma_f32_16x16x32_bf16 v[48:51], a[148:151], v[128:131], v[48:51]\n"
      "buffer_load_dwordx4 a[252:255], v1, s[16:19], s88 offen offset:2048 sc1\n"
      "v_mfma_f32_16x16x32_bf16 v[52:55], a[152:155], v[128:131], v[52:55]\n"
      "v_mfma_f32_16x16x32_bf16 v[56:59], a[156:159], v[128:131], v[56:59]\n"
      "v_mfma_f32_16x16x32_bf16 v[60:63], a[160:163], v[128:131], v[60:63]\n"
      "ds_read_b128 v[128:131], v1 offset:43008\n"
      "s_waitcnt lgkmcnt(3)\n"
      "v_mfma_f32_16x16x32_bf16 v[64:67], a[148:151], v[132:135], v[64:67]\n"
      "buffer_load_dwordx4 v[176:179], v1, s[16:19], s88 offen offset:3072 sc1\n"
      "v_mfma_f32_16x16x32_bf16 v[68:71], a[152:155], v[132:135], v[68:71]\n"
      "v_mfma_f32_16x16x32_bf16 v[72:75], a[156:159], v[132:135], v[72:75]\n"
      "v_mfma_f32_16x16x32_bf16 v[76:79], a[160:163], v[132:135], v[76:79]\n"
      "ds_read_b128 v[132:135], v1 offset:47104\n"
      "s_waitcnt lgkmcnt(3)\n"
      "v_mfma_f32_16x16x32_bf16 v[80:83], a[148:151], v[136:139], v[80:83]\n"
      "s_add_u32 s88, s88, 4096\n"
      "buffer_load_dwordx4 v[180:183], v1, s[16:19], s88 offen sc1\n"
      "v_mfma_f32_16x16x32_bf16 v[84:87], a[152:155], v[136:139], v[84:87]\n"
      "v_mfma_f32_16x16x32_bf16 v[88:91], a[156:159], v[136:139], v[88:91]\n"
      "v_mfma_f32_16x16x32_bf16 v[92:95], a[160:163], v[136:139], v[92:95]\n"
      "ds_read_b128 v[136:139], v1 offset:51200\n"
      "s_waitcnt lgkmcnt(3)\n"
      "v_mfma_f32_16x16x32_bf16 v[96:99], a[148:151], v[140:143], v[96:99]\n"
      "buffer_load_dwordx4 v[184:187], v1, s[16:19], s88 offen offset:1024 sc1\n"
      "v_mfma_f32_16x16x32_bf16 v[100:103], a[152:155], v[140:143], v[100:103]\n"
      "v_mfma_f32_16x16x32_bf16 v[104:107], a[156:159], v[140:143], v[104:107]\n"
      "v_mfma_f32_16x16x32_bf16 v[108:111], a[160:163], v[140:143], v[108:111]\n"
      "ds_read_b128 v[140:143], v1 offset:55296\n"
      "s_waitcnt lgkmcnt(3)\n"
      "v_mfma_f32_16x16x32_bf16 v[48:51], a[164:167], v[128:131], v[48:51]\n"
      "buffer_load_dwordx4 v[188:191], v1, s[16:19], s88 offen offset:2048 sc1\n"
      "v_mfma_f32_16x16x32_bf16 v[52:55], a[168:171], v[128:131], v[52:55]\n"
      "s_waitcnt vmcnt(27)\n"
      "v_mfma_f32_16x16x32_bf16 v[56:59], a[172:175], v[128:131], v[56:59]\n"
      "s_waitcnt vmcnt(26)\n"
      "v_mfma_f32_16x16x32_bf16 v[60:63], a[176:179], v[128:131], v[60:63]\n"
      "ds_read_b128 v[128:131], v1 offset:44032\n"
      "s_waitcnt lgkmcnt(3)\n"
      "v_mfma_f32_16x16x32_bf16 v[64:67], a[164:167], v[132:135], v[64:67]\n"
      "buffer_load_dwordx4 v[192:195], v1, s[16:19], s88 offen offset:3072 sc1\n"
      "v_mfma_f32_16x16x32_bf16 v[68:71], a[168:171], v[132:135], v[68:71]\n"
      "v_mfma_f32_16x16x32_bf16 v[72:75], a[172:175], v[132:135], v[72:75]\n"
      "v_mfma_f32_16x16x32_bf16 v[76:79], a[176:179], v[132:135], v[76:79]\n"
      "ds_read_b128 v[132:135], v1 offset:48128\n"
      "s_waitcnt lgkmcnt(3)\n"
      "v_mfma_f32_16x16x32_bf16 v[80:83], a[164:167], v[136:139], v[80:83]\n"
      "s_add_u32 s88, s88, 4096\n"
      "buffer_load_dwordx4 v[196:199], v1, s[16:19], s88 offen sc1\n"
      "v_mfma_f32_16x16x32_bf16 v[84:87], a[168:171], v[136:139], v[84:87]\n"
      "v_mfma_f32_16x16x32_bf16 v[88:91], a[172:175], v[136:139], v[88:91]\n"
      "v_mfma_f32_16x16x32_bf16 v[92:95], a[176:179], v[136:139], v[92:95]\n"
      "ds_read_b128 v[136:139], v1 offset:52224\n"
      "s_waitcnt lgkmcnt(3)\n"
      "v_mfma_f32_16x16x32_bf16 v[96:99], a[164:167], v[140:143], v[96:99]\n"
      "buffer_load_dwordx4 v[200:203], v1, s[16:19], s88 offen offset:1024 sc1\n"
      "v_mfma_f32_16x16x32_bf16 v[100:103], a[168:171], v[140:143], v[100:103]\n"
      "v_mfma_f32_16x16x32_bf16 v[104:107], a[172:175], v[140:143], v[104:107]\n"
      "v_mfma_f32_16x16x32_bf16 v[108:111], a[176:179], v[140:143], v[108:111]\n"
      "ds_read_b128 v[140:143], v1 offset:56320\n"
      "s_waitcnt vmcnt(28) lgkmcnt(3)\n"
      "v_mfma_f32_16x16x32_bf16 v[48:51], a[180:183], v[128:131], v[48:51]\n"
      "buffer_load_dwordx4 v[204:207], v1, s[16:19], s88 offen offset:2048 sc1\n"
      "s_waitcnt vmcnt(28)\n"
      "v_mfma_f32_16x16x32_bf16 v[52:55], a[184:187], v[128:131], v[52:55]\n"
      "s_waitcnt vmcnt(27)\n"
      "v_mfma_f32_16x16x32_bf16 v[56:59], a[188:191], v[128:131], v[56:59]\n"
      "s_waitcnt vmcnt(26)\n"
      "v_mfma_f32_16x16x32_bf16 v[60:63], a[192:195], v[128:131], v[60:63]\n"
      "s_waitcnt lgkmcnt(2)\n"
      "v_mfma_f32_16x16x32_bf16 v[64:67], a[180:183], v[132:135], v[64:67]\n"
      "buffer_load_dwordx4 v[208:211], v1, s[16:19], s88 offen offset:3072 sc1\n"
      "v_mfma_f32_16x16x32_bf16 v[68:71], a[184:187], v[132:135], v[68:71]\n"
      "v_mfma_f32_16x16x32_bf16 v[72:75], a[188:191], v[132:135], v[72:75]\n"
      "v_mfma_f32_16x16x32_bf16 v[76:79], a[192:195], v[132:135], v[76:79]\n"
      "s_waitcnt lgkmcnt(0)\n"
      "s_waitcnt vmcnt(10)\n"
      "s_barrier\n"
      "ds_read_b128 v[128:131], v2\n"
      "ds_read_b128 v[132:135], v2 offset:4096\n"
      "v_mfma_f32_16x16x32_bf16 v[80:83], a[180:183], v[136:139], v[80:83]\n"
      "s_add_u32 s87, s66, 2764800\n"
      "buffer_load_dwordx2 v[30:31], v19, s[16:19], s87 offen sc1\n"
      "v_mfma_f32_16x16x32_bf16 v[84:87], a[184:187], v[136:139], v[84:87]\n"
      "v_mfma_f32_16x16x32_bf16 v[88:91], a[188:191], v[136:139], v[88:91]\n"
      "v_mfma_f32_16x16x32_bf16 v[92:95], a[192:195], v[136:139], v[92:95]\n"
      "ds_read_b128 v[136:139], v2 offset:8192\n"
      "v_mfma_f32_16x16x32_bf16 v[96:99], a[180:183], v[140:143], v[96:99]\n"
      "s_add_u32 s88, s88, 4096\n"
      "buffer_load_dwordx4 v[212:215], v1, s[16:19], s88 offen sc1\n"
      "v_mfma_f32_16x16x32_bf16 v[100:103], a[184:187], v[140:143], v[100:103]\n"
      "v_mfma_f32_16x16x32_bf16 v[104:107], a[188:191], v[140:143], v[104:107]\n"
      "v_mfma_f32_16x16x32_bf16 v[108:111], a[192:195], v[140:143], v[108:111]\n"
      "ds_read_b128 v[140:143], v2 offset:12288\n"
      "s_waitcnt lgkmcnt(3)\n"
      "v_mfma_f32_16x16x32_bf16 v[48:51], a[196:199], v[128:131], v[48:51]\n"
      "s_add_u32 s98, s56, 13213696\n"
      "s_add_u32 m0, s74, 81920\n"
      "s_nop 0\n"
      "buffer_load_dwordx4 v1, s[16:19], s98 offen sc1 lds\n"
      "v_mfma_f32_16x16x32_bf16 v[52:55], a[200:203], v[128:131], v[52:55]\n"
      "v_mfma_f32_16x16x32_bf16 v[56:59], a[204:207], v[128:131], v[56:59]\n"
      "v_mfma_f32_16x16x32_bf16 v[60:63], a[208:211], v[128:131], v[60:63]\n"
      "ds_read_b128 v[128:131], v2 offset:1024\n"
      "s_waitcnt lgkmcnt(3)\n"
      "v_mfma_f32_16x16x32_bf16 v[64:67], a[196:199], v[132:135], v[64:67]\n"
      "buffer_load_dwordx4 v1, s[16:19], s98 offen offset:1024 sc1 lds\n"
      "v_mfma_f32_16x16x32_bf16 v[68:71], a[200:203], v[132:135], v[68:71]\n"
      "v_mfma_f32_16x16x32_bf16 v[72:75], a[204:207], v[132:135], v[72:75]\n"
      "v_mfma_f32_16x16x32_bf16 v[76:79], a[208:211], v[132:135], v[76:79]\n"
      "ds_read_b128 v[132:135], v2 offset:5120\n"
      "s_waitcnt lgkmcnt(3)\n"
      "v_mfma_f32_16x16x32_bf16 v[80:83], a[196:199], v[136:139], v[80:83]\n"
      "buffer_load_dwordx4 v1, s[16:19], s98 offen offset:2048 sc1 lds\n"
      "v_mfma_f32_16x16x32_bf16 v[84:87], a[200:203], v[136:139], v[84:87]\n"
      "v_mfma_f32_16x16x32_bf16 v[88:91], a[204:207], v[136:139], v[88:91]\n"
      "v_mfma_f32_16x16x32_bf16 v[92:95], a[208:211], v[136:139], v[92:95]\n"
      "ds_read_b128 v[136:139], v2 offset:9216\n"
      "s_waitcnt lgkmcnt(3)\n"
      "v_mfma_f32_16x16x32_bf16 v[96:99], a[196:199], v[140:143], v[96:99]\n"
      "buffer_load_dwordx4 v1, s[16:19], s98 offen offset:3072 sc1 lds\n"
      "v_mfma_f32_16x16x32_bf16 v[100:103], a[200:203], v[140:143], v[100:103]\n"
      "v_mfma_f32_16x16x32_bf16 v[104:107], a[204:207], v[140:143], v[104:107]\n"
      "v_mfma_f32_16x16x32_bf16 v[108:111], a[208:211], v[140:143], v[108:111]\n"
      "ds_read_b128 v[140:143], v2 offset:13312\n"
      "s_waitcnt lgkmcnt(3)\n"
      "v_mfma_f32_16x16x32_bf16 v[48:51], a[212:215], v[128:131], v[48:51]\n"
      "buffer_load_dwordx4 v[216:219], v1, s[16:19], s88 offen offset:1024 sc1\n"
      "v_mfma_f32_16x16x32_bf16 v[52:55], a[216:219], v[128:131], v[52:55]\n"
      "v_mfma_f32_16x16x32_bf16 v[56:59], a[220:223], v[128:131], v[56:59]\n"
      "v_mfma_f32_16x16x32_bf16 v[60:63], a[224:227], v[128:131], v[60:63]\n"
      "ds_read_b128 v[128:131], v2 offset:2048\n"
      "s_waitcnt lgkmcnt(3)\n"
      "v_mfma_f32_16x16x32_bf16 v[64:67], a[212:215], v[132:135], v[64:67]\n"
      "buffer_load_dwordx4 v[220:223], v1, s[16:19], s88 offen offset:2048 sc1\n"
      "v_mfma_f32_16x16x32_bf16 v[68:71], a[216:219], v[132:135], v[68:71]\n"
      "v_mfma_f32_16x16x32_bf16 v[72:75], a[220:223], v[132:135], v[72:75]\n"
      "v_mfma_f32_16x16x32_bf16 v[76:79], a[224:227], v[132:135], v[76:79]\n"
      "ds_read_b128 v[132:135], v2 offset:6144\n"
      "s_waitcnt lgkmcnt(3)\n"
      "v_mfma_f32_16x16x32_bf16 v[80:83], a[212:215], v[136:139], v[80:83]\n"
      "buffer_load_dwordx4 v[224:227], v1, s[16:19], s88 offen offset:3072 sc1\n"
      "v_mfma_f32_16x16x32_bf16 v[84:87], a[216:219], v[136:139], v[84:87]\n"
      "v_mfma_f32_16x16x32_bf16 v[88:91], a[220:223], v[136:139], v[88:91]\n"
      "v_mfma_f32_16x16x32_bf16 v[92:95], a[224:227], v[136:139], v[92:95]\n"
      "ds_read_b128 v[136:139], v2 offset:10240\n"
      "s_waitcnt lgkmcnt(3)\n"
      "v_mfma_f32_16x16x32_bf16 v[96:99], a[212:215], v[140:143], v[96:99]\n"
      "s_add_u32 s88, s88, 4096\n"
      "buffer_load_dwordx4 v[228:231], v1, s[16:19], s88 offen sc1\n"
      "v_mfma_f32_16x16x32_bf16 v[100:103], a[216:219], v[140:143], v[100:103]\n"
      "v_mfma_f32_16x16x32_bf16 v[104:107], a[220:223], v[140:143], v[104:107]\n"
      "v_mfma_f32_16x16x32_bf16 v[108:111], a[224:227], v[140:143], v[108:111]\n"
      "ds_read_b128 v[140:143], v2 offset:14336\n"
      "s_waitcnt lgkmcnt(3)\n"
      "v_mfma_f32_16x16x32_bf16 v[48:51], a[228:231], v[128:131], v[48:51]\n"
      "buffer_load_dwordx4 v[232:235], v1, s[16:19], s88 offen offset:1024 sc1\n"
      "v_mfma_f32_16x16x32_bf16 v[52:55], a[232:235], v[128:131], v[52:55]\n"
      "v_mfma_f32_16x16x32_bf16 v[56:59], a[236:239], v[128:131], v[56:59]\n"
      "v_mfma_f32_16x16x32_bf16 v[60:63], a[240:243], v[128:131], v[60:63]\n"
      "ds_read_b128 v[128:131], v2 offset:3072\n"
      "s_waitcnt lgkmcnt(3)\n"
      "v_mfma_f32_16x16x32_bf16 v[64:67], a[228:231], v[132:135], v[64:67]\n"
      "buffer_load_dwordx4 v[236:239], v1, s[16:19], s88 offen offset:2048 sc1\n"
      "v_mfma_f32_16x16x32_bf16 v[68:71], a[232:235], v[132:135], v[68:71]\n"
      "v_mfma_f32_16x16x32_bf16 v[72:75], a[236:239], v[132:135], v[72:75]\n"
      "v_mfma_f32_16x16x32_bf16 v[76:79], a[240:243], v[132:135], v[76:79]\n"
      "ds_read_b128 v[132:135], v2 offset:7168\n"
      "s_waitcnt lgkmcnt(3)\n"
      "v_mfma_f32_16x16x32_bf16 v[80:83], a[228:231], v[136:139], v[80:83]\n"
      "buffer_load_dwordx4 v[240:243], v1, s[16:19], s88 offen offset:3072 sc1\n"
      "v_mfma_f32_16x16x32_bf16 v[84:87], a[232:235], v[136:139], v[84:87]\n"
      "v_mfma_f32_16x16x32_bf16 v[88:91], a[236:239], v[136:139], v[88:91]\n"
      "v_mfma_f32_16x16x32_bf16 v[92:95], a[240:243], v[136:139], v[92:95]\n"
      "ds_read_b128 v[136:139], v2 offset:11264\n"
      "s_waitcnt lgkmcnt(3)\n"
      "v_mfma_f32_16x16x32_bf16 v[96:99], a[228:231], v[140:143], v[96:99]\n"
      "s_mov_b32 s89, s71\n"
      "buffer_load_dwordx4 v[244:247], v1, s[16:19], s89 offen sc1\n"
      "v_mfma_f32_16x16x32_bf16 v[100:103], a[232:235], v[140:143], v[100:103]\n"
      "s_waitcnt vmcnt(13)\n"
      "v_cmp_eq_u64 vcc, s[36:37], v[30:31]\n"
      "s_nop 3\n"
      "s_cmp_eq_u64 vcc, -1\n"
      "s_cbranch_scc1 Lpd_13\n"
      "Lps_14:\n"
      "s_sleep 1\n"
      "buffer_load_dwordx2 v[30:31], v19, s[16:19], s87 offen sc1\n"
      "s_waitcnt vmcnt(0)\n"
      "v_cmp_eq_u64 vcc, s[36:37], v[30:31]\n"
      "s_nop 3\n"
      "s_cmp_eq_u64 vcc, -1\n"
      "s_cbranch_scc1 Lpd_13\n"
      "s_add_u32 s42, s42, 1\n"
      "s_cmp_lt_u32 s42, 0x40000\n"
      "s_cbranch_scc1 Lps_14\n"
      "Lpd_13:\n"
      "v_mfma_f32_16x16x32_bf16 v[104:107], a[236:239], v[140:143], v[104:107]\n"
      "v_mfma_f32_16x16x32_bf16 v[108:111], a[240:243], v[140:143], v[108:111]\n"
      "ds_read_b128 v[140:143], v2 offset:15360\n"
      "s_waitcnt lgkmcnt(3)\n"
      "v_mfma_f32_16x16x32_bf16 v[48:51], a[244:247], v[128:131], v[48:51]\n"
      "s_add_u32 s98, s56, 14508032\n"
      "s_add_u32 m0, s74, 0\n"
      "s_nop 0\n"
      "buffer_load_dwordx4 v1, s[16:19], s98 offen sc1 lds\n"
      "v_mfma_f32_16x16x32_bf16 v[52:55], a[248:251], v[128:131], v[52:55]\n"
      "v_mfma_f32_16x16x32_bf16 v[56:59], a[252:255], v[128:131], v[56:59]\n"
      "v_mfma_f32_16x16x32_bf16 v[60:63], v[176:179], v[128:131], v[60:63]\n"
      "s_waitcnt lgkmcnt(2)\n"
      "v_mfma_f32_16x16x32_bf16 v[64:67], a[244:247], v[132:135], v[64:67]\n"
      "buffer_load_dwordx4 v1, s[16:19], s98 offen offset:1024 sc1 lds\n"
      "v_mfma_f32_16x16x32_bf16 v[68:71], a[248:251], v[132:135], v[68:71]\n"
      "v_mfma_f32_16x16x32_bf16 v[72:75], a[252:255], v[132:135], v[72:75]\n"
      "v_mfma_f32_16x16x32_bf16 v[76:79], v[176:179], v[132:135], v[76:79]\n"
      "s_waitcnt lgkmcnt(0)\n"
      "s_waitcnt vmcnt(10)\n"
      "s_barrier\n"
      "ds_read_b128 v[128:131], v2 offset:20480\n"
      "ds_read_b128 v[132:135], v2 offset:24576\n"
      "v_mfma_f32_16x16x32_bf16 v[80:83], a[244:247], v[136:139], v[80:83]\n"
      "buffer_load_dwordx4 v1, s[16:19], s98 offen offset:2048 sc1 lds\n"
      "v_mfma_f32_16x16x32_bf16 v[84:87], a[248:251], v[136:139], v[84:87]\n"
      "v_mfma_f32_16x16x32_bf16 v[88:91], a[252:255], v[136:139], v[88:91]\n"
      "v_mfma_f32_16x16x32_bf16 v[92:95], v[176:179], v[136:139], v[92:95]\n"
      "ds_read_b128 v[136:139], v2 offset:28672\n"
      "v_mfma_f32_16x16x32_bf16 v[96:99], a[244:247], v[140:143], v[96:99]\n"
      "buffer_load_dwordx4 v1, s[16:19], s98 offen offset:3072 sc1 lds\n"
      "v_mfma_f32_16x16x32_bf16 v[100:103], a[248:251], v[140:143], v[100:103]\n"
      "v_mfma_f32_16x16x32_bf16 v[104:107], a[252:255], v[140:143], v[104:107]\n"
      "v_mfma_f32_16x16x32_bf16 v[108:111], v[176:179], v[140:143], v[108:111]\n"
      "ds_read_b128 v[140:143], v2 offset:32768\n"
      "s_waitcnt lgkmcnt(3)\n"
      "v_mfma_f32_16x16x32_bf16 v[48:51], v[180:183], v[128:131], v[48:51]\n"
      "s_add_u32 s98, s56, 14512128\n"
      "s_add_u32 m0, s74, 20480\n"
      "s_nop 0\n"
      "buffer_load_dwordx4 v1, s[16:19], s98 offen sc1 lds\n"
      "v_mfma_f32_16x16x32_bf16 v[52:55], v[184:187], v[128:131], v[52:55]\n"
      "v_mfma_f32_16x16x32_bf16 v[56:59], v[188:191], v[128:131], v[56:59]\n"
      "v_mfma_f32_16x16x32_bf16 v[60:63], v[192:195], v[128:131], v[60:63]\n"
      "ds_read_b128 v[128:131], v2 offset:21504\n"
      "s_waitcnt lgkmcnt(3)\n"
      "v_mfma_f32_16x16x32_bf16 v[64:67], v[180:183], v[132:135], v[64:67]\n"
      "buffer_load_dwordx4 v1, s[16:19], s98 offen offset:1024 sc1 lds\n"
      "v_mfma_f32_16x16x32_bf16 v[68:71], v[184:187], v[132:135], v[68:71]\n"
      "v_mfma_f32_16x16x32_bf16 v[72:75], v[188:191], v[132:135], v[72:75]\n"
      "v_mfma_f32_16x16x32_bf16 v[76:79], v[192:195], v[132:135], v[76:79]\n"
      "ds_read_b128 v[132:135], v2 offset:25600\n"
      "s_waitcnt lgkmcnt(3)\n"
      "v_mfma_f32_16x16x32_bf16 v[80:83], v[180:183], v[136:139], v[80:83]\n"
      "buffer_load_dwordx4 v1, s[16:19], s98 offen offset:2048 sc1 lds\n"
      "v_mfma_f32_16x16x32_bf16 v[84:87], v[184:187], v[136:139], v[84:87]\n"
      "v_mfma_f32_16x16x32_bf16 v[88:91], v[188:191], v[136:139], v[88:91]\n"
      "v_mfma_f32_16x16x32_bf16 v[92:95], v[192:195], v[136:139], v[92:95]\n"
      "ds_read_b128 v[136:139], v2 offset:29696\n"
      "s_waitcnt lgkmcnt(3)\n"
      "v_mfma_f32_16x16x32_bf16 v[96:99], v[180:183], v[140:143], v[96:99]\n"
      "buffer_load_dwordx4 v1, s[16:19], s98 offen offset:3072 sc1 lds\n"
      "v_mfma_f32_16x16x32_bf16 v[100:103], v[184:187], v[140:143], v[100:103]\n"
      "v_mfma_f32_16x16x32_bf16 v[104:107], v[188:191], v[140:143], v[104:107]\n"
      "v_mfma_f32_16x16x32_bf16 v[108:111], v[192:195], v[140:143], v[108:111]\n"
      "ds_read_b128 v[140:143], v2 offset:33792\n"
      "s_waitcnt lgkmcnt(3)\n"
      "v_mfma_f32_16x16x32_bf16 v[48:51], v[196:199], v[128:131], v[48:51]\n"
      "buffer_load_dwordx4 v[248:251], v1, s[16:19], s89 offen offset:1024 sc1\n"
      "v_mfma_f32_16x16x32_bf16 v[52:55], v[200:203], v[128:131], v[52:55]\n"
      "v_mfma_f32_16x16x32_bf16 v[56:59], v[204:207], v[128:131], v[56:59]\n"
      "v_mfma_f32_16x16x32_bf16 v[60:63], v[208:211], v[128:131], v[60:63]\n"
      "ds_read_b128 v[128:131], v2 offset:22528\n"
      "s_waitcnt lgkmcnt(3)\n"
      "v_mfma_f32_16x16x32_bf16 v[64:67], v[196:199], v[132:135], v[64:67]\n"
      "buffer_load_dwordx4 v[252:255], v1, s[16:19], s89 offen offset:2048 sc1\n"
      "v_mfma_f32_16x16x32_bf16 v[68:71], v[200:203], v[132:135], v[68:71]\n"
      "v_mfma_f32_16x16x32_bf16 v[72:75], v[204:207], v[132:135], v[72:75]\n"
      "v_mfma_f32_16x16x32_bf16 v[76:79], v[208:211], v[132:135], v[76:79]\n"
      "ds_read_b128 v[132:135], v2 offset:26624\n"
      "s_waitcnt lgkmcnt(3)\n"
      "v_mfma_f32_16x16x32_bf16 v[80:83], v[196:199], v[136:139], v[80:83]\n"
      "buffer_load_dwordx4 a[0:3], v1, s[16:19], s89 offen offset:3072 sc1\n"
      "v_mfma_f32_16x16x32_bf16 v[84:87], v[200:203], v[136:139], v[84:87]\n"
      "v_mfma_f32_16x16x32_bf16 v[88:91], v[204:207], v[136:139], v[88:91]\n"
      "v_mfma_f32_16x16x32_bf16 v[92:95], v[208:211], v[136:139], v[92:95]\n"
      "ds_read_b128 v[136:139], v2 offset:30720\n"
      "s_waitcnt lgkmcnt(3)\n"
      "v_mfma_f32_16x16x32_bf16 v[96:99], v[196:199], v[140:143], v[96:99]\n"
      "s_add_u32 s98, s56, 14516224\n"
      "s_add_u32 m0, s74, 40960\n"
      "s_nop 0\n"
      "buffer_load_dwordx4 v1, s[16:19], s98 offen sc1 lds\n"
      "v_mfma_f32_16x16x32_bf16 v[100:103], v[200:203], v[140:143], v[100:103]\n"
      "v_mfma_f32_16x16x32_bf16 v[104:107], v[204:207], v[140:143], v[104:107]\n"
      "v_mfma_f32_16x16x32_bf16 v[108:111], v[208:211], v[140:143], v[108:111]\n"
      "ds_read_b128 v[140:143], v2 offset:34816\n"
      "s_waitcnt lgkmcnt(3)\n"
      "v_mfma_f32_16x16x32_bf16 v[48:51], v[212:215], v[128:131], v[48:51]\n"
      "buffer_load_dwordx4 v1, s[16:19], s98 offen offset:1024 sc1 lds\n"
      "s_waitcnt vmcnt(20)\n"
      "v_mfma_f32_16x16x32_bf16 v[52:55], v[216:219], v[128:131], v[52:55]\n"
      "s_waitcnt vmcnt(19)\n"
      "v_mfma_f32_16x16x32_bf16 v[56:59], v[220:223], v[128:131], v[56:59]\n"
      "s_waitcnt vmcnt(18)\n"
      "v_mfma_f32_16x16x32_bf16 v[60:63], v[224:227], v[128:131], v[60:63]\n"
      "ds_read_b128 v[128:131], v2 offset:23552\n"
      "s_waitcnt lgkmcnt(3)\n"
      "v_mfma_f32_16x16x32_bf16 v[64:67], v[212:215], v[132:135], v[64:67]\n"
      "buffer_load_dwordx4 v1, s[16:19], s98 offen offset:2048 sc1 lds\n"
      "v_mfma_f32_16x16x32_bf16 v[68:71], v[216:219], v[132:135], v[68:71]\n"
      "v_mfma_f32_16x16x32_bf16 v[72:75], v[220:223], v[132:135], v[72:75]\n"
      "v_mfma_f32_16x16x32_bf16 v[76:79], v[224:227], v[132:135], v[76:79]\n"
      "ds_read_b128 v[132:135], v2 offset:27648\n"
      "s_waitcnt lgkmcnt(3)\n"
      "v_mfma_f32_16x16x32_bf16 v[80:83], v[212:215], v[136:139], v[80:83]\n"
      "buffer_load_dwordx4 v1, s[16:19], s98 offen offset:3072 sc1 lds\n"
      "v_mfma_f32_16x16x32_bf16 v[84:87], v[216:219], v[136:139], v[84:87]\n"
      "v_mfma_f32_16x16x32_bf16 v[88:91], v[220:223], v[136:139], v[88:91]\n"
      "v_mfma_f32_16x16x32_bf16 v[92:95], v[224:227], v[136:139], v[92:95]\n"
      "ds_read_b128 v[136:139], v2 offset:31744\n"
      "s_waitcnt lgkmcnt(3)\n"
      "v_mfma_f32_16x16x32_bf16 v[96:99], v[212:215], v[140:143], v[96:99]\n"
      "s_add_u32 s98, s56, 14520320\n"
      "s_add_u32 m0, s74, 61440\n"
      "s_nop 0\n"
      "buffer_load_dwordx4 v1, s[16:19], s98 offen sc1 lds\n"
      "v_mfma_f32_16x16x32_bf16 v[100:103], v[216:219], v[140:143], v[100:103]\n"
      "v_mfma_f32_16x16x32_bf16 v[104:107], v[220:223], v[140:143], v[104:107]\n"
      "v_mfma_f32_16x16x32_bf16 v[108:111], v[224:227], v[140:143], v[108:111]\n"
      "ds_read_b128 v[140:143], v2 offset:35840\n"
      "s_waitcnt vmcnt(20) lgkmcnt(3)\n"
      "v_mfma_f32_16x16x32_bf16 v[48:51], v[228:231], v[128:131], v[48:51]\n"
      "buffer_load_dwordx4 v1, s[16:19], s98 offen offset:1024 sc1 lds\n"
      "s_waitcnt vmcnt(20)\n"
      "v_mfma_f32_16x16x32_bf16 v[52:55], v[232:235], v[128:131], v[52:55]\n"
      "s_waitcnt vmcnt(19)\n"
      "v_mfma_f32_16x16x32_bf16 v[56:59], v[236:239], v[128:131], v[56:59]\n"
      "s_waitcnt vmcnt(18)\n"
      "v_mfma_f32_16x16x32_bf16 v[60:63], v[240:243], v[128:131], v[60:63]\n"
      "s_waitcnt lgkmcnt(2)\n"
      "v_mfma_f32_16x16x32_bf16 v[64:67], v[228:231], v[132:135], v[64:67]\n"
      "buffer_load_dwordx4 v1, s[16:19], s98 offen offset:2048 sc1 lds\n"
      "v_mfma_f32_16x16x32_bf16 v[68:71], v[232:235], v[132:135], v[68:71]\n"
      "v_mfma_f32_16x16x32_bf16 v[72:75], v[236:239], v[132:135], v[72:75]\n"
      "v_mfma_f32_16x16x32_bf16 v[76:79], v[240:243], v[132:135], v[76:79]\n"
      "s_waitcnt lgkmcnt(1)\n"
      "v_mfma_f32_16x16x32_bf16 v[80:83], v[228:231], v[136:139], v[80:83]\n"
      "buffer_load_dwordx4 v1, s[16:19], s98 offen offset:3072 sc1 lds\n"
      "v_mfma_f32_16x16x32_bf16 v[84:87], v[232:235], v[136:139], v[84:87]\n"
      "v_mfma_f32_16x16x32_bf16 v[88:91], v[236:239], v[136:139], v[88:91]\n"
      "v_mfma_f32_16x16x32_bf16 v[92:95], v[240:243], v[136:139], v[92:95]\n"
      "s_waitcnt lgkmcnt(0)\n"
      "v_mfma_f32_16x16x32_bf16 v[96:99], v[228:231], v[140:143], v[96:99]\n"
      "v_mfma_f32_16x16x32_bf16 v[100:103], v[232:235], v[140:143], v[100:103]\n"
      "v_mfma_f32_16x16x32_bf16 v[104:107], v[236:239], v[140:143], v[104:107]\n"
      "v_mfma_f32_16x16x32_bf16 v[108:111], v[240:243], v[140:143], v[108:111]\n"
      "s_waitcnt lgkmcnt(0)\n"
      "s_barrier\n"
      "s_add_u32 s98, s56, 14524416\n"
      "s_add_u32 m0, s74, 81920\n"
      "s_nop 0\n"
      "buffer_load_dwordx4 v1, s[16:19], s98 offen sc1 lds\n"
      "ds_read_b64 v[120:121], v25 offset:128\n"
      "ds_read_b32 v148, v25 offset:132\n"
      "ds_read_b64 v[122:123], v25 offset:256\n"
      "ds_read_b32 v150, v25 offset:260\n"
      "ds_read_b64 v[124:125], v25 offset:384\n"
      "buffer_load_dwordx4 v1, s[16:19], s98 offen offset:1024 sc1 lds\n"
      "ds_read_b32 v152, v25 offset:388\n"
      "ds_read_b64 v[126:127], v25 offset:512\n"
      "ds_read_b32 v154, v25 offset:516\n"
      "ds_read_b128 v[112:115], v24 offset:128\n"
      "ds_read_b128 v[116:119], v24 offset:192\n"
      "buffer_load_dwordx4 v1, s[16:19], s98 offen offset:2048 sc1 lds\n"
      "s_add_u32 s80, s61, 15949824\n"
      "s_add_u32 s81, s61, 16005120\n"
      "s_waitcnt lgkmcnt(1)\n"
      "v_pk_fma_f32 v[48:49], v[112:113], v[148:149], v[48:49] op_sel_hi:[1,0,1]\n"
      "v_pk_fma_f32 v[50:51], v[114:115], v[148:149], v[50:51] op_sel_hi:[1,0,1]\n"
      "v_pk_fma_f32 v[64:65], v[112:113], v[150:151], v[64:65] op_sel_hi:[1,0,1]\n"
      "v_pk_fma_f32 v[66:67], v[114:115], v[150:151], v[66:67] op_sel_hi:[1,0,1]\n"
      "v_pk_fma_f32 v[80:81], v[112:113], v[152:153], v[80:81] op_sel_hi:[1,0,1]\n"
      "v_pk_fma_f32 v[82:83], v[114:115], v[152:153], v[82:83] op_sel_hi:[1,0,1]\n"
      "v_pk_fma_f32 v[96:97], v[112:113], v[154:155], v[96:97] op_sel_hi:[1,0,1]\n"
      "v_pk_fma_f32 v[98:99], v[114:115], v[154:155], v[98:99] op_sel_hi:[1,0,1]\n"
      "s_waitcnt lgkmcnt(0)\n"
      "v_pk_fma_f32 v[52:53], v[116:117], v[148:149], v[52:53] op_sel_hi:[1,0,1]\n"
      "v_pk_fma_f32 v[54:55], v[118:119], v[148:149], v[54:55] op_sel_hi:[1,0,1]\n"
      "v_pk_fma_f32 v[68:69], v[116:117], v[150:151], v[68:69] op_sel_hi:[1,0,1]\n"
      "v_pk_fma_f32 v[70:71], v[118:119], v[150:151], v[70:71] op_sel_hi:[1,0,1]\n"
      "buffer_load_dwordx4 v1, s[16:19], s98 offen offset:3072 sc1 lds\n"
      "v_pk_fma_f32 v[84:85], v[116:117], v[152:153], v[84:85] op_sel_hi:[1,0,1]\n"
      "v_pk_fma_f32 v[86:87], v[118:119], v[152:153], v[86:87] op_sel_hi:[1,0,1]\n"
      "v_pk_fma_f32 v[100:101], v[116:117], v[154:155], v[100:101] op_sel_hi:[1,0,1]\n"
      "v_pk_fma_f32 v[102:103], v[118:119], v[154:155], v[102:103] op_sel_hi:[1,0,1]\n"
      "ds_read_b128 v[112:115], v24 offset:256\n"
      "ds_read_b128 v[116:119], v24 offset:320\n"
      "v_pk_mul_f32 v[48:49], v[48:49], v[52:53]\n"
      "v_pk_mul_f32 v[50:51], v[50:51], v[54:55]\n"
      "v_pk_mul_f32 v[64:65], v[64:65], v[68:69]\n"
      "v_pk_mul_f32 v[66:67], v[66:67], v[70:71]\n"
      "v_pk_mul_f32 v[48:49], v[48:49], v[120:121] op_sel_hi:[1,0]\n"
      "v_pk_mul_f32 v[50:51], v[50:51], v[120:121] op_sel_hi:[1,0]\n"
      "v_pk_mul_f32 v[80:81], v[80:81], v[84:85]\n"
      "v_pk_mul_f32 v[82:83], v[82:83], v[86:87]\n"
      "v_pk_mul_f32 v[64:65], v[64:65], v[122:123] op_sel_hi:[1,0]\n"
      "v_pk_mul_f32 v[66:67], v[66:67], v[122:123] op_sel_hi:[1,0]\n"
      "v_cvt_pk_bf16_f32 v52, v48, v49\n"
      "v_cvt_pk_bf16_f32 v53, v50, v51\n"
      "v_pk_mul_f32 v[96:97], v[96:97], v[100:101]\n"
      "v_pk_mul_f32 v[98:99], v[98:99], v[102:103]\n"
      "v_pk_mul_f32 v[80:81], v[80:81], v[124:125] op_sel_hi:[1,0]\n"
      "v_pk_mul_f32 v[82:83], v[82:83], v[124:125] op_sel_hi:[1,0]\n"
      "v_cvt_pk_bf16_f32 v68, v64, v65\n"
      "v_cvt_pk_bf16_f32 v69, v66, v67\n"
      "v_pk_mul_f32 v[96:97], v[96:97], v[126:127] op_sel_hi:[1,0]\n"
      "v_pk_mul_f32 v[98:99], v[98:99], v[126:127] op_sel_hi:[1,0]\n"
      "v_cvt_pk_bf16_f32 v84, v80, v81\n"
      "v_cvt_pk_bf16_f32 v85, v82, v83\n"
      "v_cvt_pk_bf16_f32 v100, v96, v97\n"
      "v_cvt_pk_bf16_f32 v101, v98, v99\n"
      "s_waitcnt lgkmcnt(1)\n"
      "v_pk_fma_f32 v[56:57], v[112:113], v[148:149], v[56:57] op_sel_hi:[1,0,1]\n"
      "v_pk_fma_f32 v[58:59], v[114:115], v[148:149], v[58:59] op_sel_hi:[1,0,1]\n"
      "s_waitcnt lgkmcnt(0)\n"
      "v_pk_fma_f32 v[60:61], v[116:117], v[148:149], v[60:61] op_sel_hi:[1,0,1]\n"
      "v_pk_fma_f32 v[62:63], v[118:119], v[148:149], v[62:63] op_sel_hi:[1,0,1]\n"
      "v_pk_fma_f32 v[72:73], v[112:113], v[150:151], v[72:73] op_sel_hi:[1,0,1]\n"
      "v_pk_fma_f32 v[74:75], v[114:115], v[150:151], v[74:75] op_sel_hi:[1,0,1]\n"
      "v_pk_fma_f32 v[76:77], v[116:117], v[150:151], v[76:77] op_sel_hi:[1,0,1]\n"
      "v_pk_fma_f32 v[78:79], v[118:119], v[150:151], v[78:79] op_sel_hi:[1,0,1]\n"
      "v_pk_mul_f32 v[56:57], v[56:57], v[60:61]\n"
      "v_pk_mul_f32 v[58:59], v[58:59], v[62:63]\n"
      "v_pk_fma_f32 v[88:89], v[112:113], v[152:153], v[88:89] op_sel_hi:[1,0,1]\n"
      "v_pk_fma_f32 v[90:91], v[114:115], v[152:153], v[90:91] op_sel_hi:[1,0,1]\n"
      "v_pk_fma_f32 v[92:93], v[116:117], v[152:153], v[92:93] op_sel_hi:[1,0,1]\n"
      "v_pk_fma_f32 v[94:95], v[118:119], v[152:153], v[94:95] op_sel_hi:[1,0,1]\n"
      "v_pk_mul_f32 v[72:73], v[72:73], v[76:77]\n"
      "v_pk_mul_f32 v[74:75], v[74:75], v[78:79]\n"
      "v_pk_mul_f32 v[56:57], v[56:57], v[120:121] op_sel_hi:[1,0]\n"
      "v_pk_mul_f32 v[58:59], v[58:59], v[120:121] op_sel_hi:[1,0]\n"
      "v_pk_fma_f32 v[104:105], v[112:113], v[154:155], v[104:105] op_sel_hi:[1,0,1]\n"
      "v_pk_fma_f32 v[106:107], v[114:115], v[154:155], v[106:107] op_sel_hi:[1,0,1]\n"
      "v_pk_fma_f32 v[108:109], v[116:117], v[154:155], v[108:109] op_sel_hi:[1,0,1]\n"
      "v_pk_fma_f32 v[110:111], v[118:119], v[154:155], v[110:111] op_sel_hi:[1,0,1]\n"
      "v_pk_mul_f32 v[88:89], v[88:89], v[92:93]\n"
      "v_pk_mul_f32 v[90:91], v[90:91], v[94:95]\n"
      "v_pk_mul_f32 v[72:73], v[72:73], v[122:123] op_sel_hi:[1,0]\n"
      "v_pk_mul_f32 v[74:75], v[74:75], v[122:123] op_sel_hi:[1,0]\n"
      "v_cvt_pk_bf16_f32 v54, v56, v57\n"
      "v_cvt_pk_bf16_f32 v55, v58, v59\n"
      "v_pk_mul_f32 v[104:105], v[104:105], v[108:109]\n"
      "v_pk_mul_f32 v[106:107], v[106:107], v[110:111]\n"
      "v_pk_mul_f32 v[88:89], v[88:89], v[124:125] op_sel_hi:[1,0]\n"
      "v_pk_mul_f32 v[90:91], v[90:91], v[124:125] op_sel_hi:[1,0]\n"
      "v_cvt_pk_bf16_f32 v70, v72, v73\n"
      "v_cvt_pk_bf16_f32 v71, v74, v75\n"
      "buffer_store_dwordx4 v[52:55], v1, s[16:19], s80 offen sc1\n"
      "v_pk_mul_f32 v[104:105], v[104:105], v[126:127] op_sel_hi:[1,0]\n"
      "v_pk_mul_f32 v[106:107], v[106:107], v[126:127] op_sel_hi:[1,0]\n"
      "v_cvt_pk_bf16_f32 v86, v88, v89\n"
      "v_cvt_pk_bf16_f32 v87, v90, v91\n"
      "buffer_store_dwordx4 v[68:71], v1, s[16:19], s81 offen sc1\n"
      "s_add_u32 s80, s61, 16060416\n"
      "v_cvt_pk_bf16_f32 v102, v104, v105\n"
      "v_cvt_pk_bf16_f32 v103, v106, v107\n"
      "buffer_store_dwordx4 v[84:87], v1, s[16:19], s80 offen sc1\n"
      "s_add_u32 s81, s61, 16115712\n"
      "buffer_store_dwordx4 v[100:103], v1, s[16:19], s81 offen sc1\n"
      "; ==== G1\n"
      "s_waitcnt lgkmcnt(0)\n"
      "s_waitcnt vmcnt(23)\n"
      "s_barrier\n"
      "ds_read_b128 v[128:131], v1\n"
      "ds_read_b128 v[132:135], v1 offset:4096\n"
      "ds_read_b128 v[136:139], v1 offset:8192\n"
      "ds_read_b128 v[140:143], v1 offset:12288\n"
      "s_waitcnt lgkmcnt(3)\n"
      "v_mfma_f32_16x16x32_bf16 v[48:51], a[4:7], v[128:131], 0\n"
      "s_add_u32 s77, s64, 15884288\n"
      "buffer_load_dwordx2 v[144:145], v17, s[16:19], s77 offen sc1\n"
      "buffer_load_dwordx2 v[146:147], v17, s[16:19], s77 offen offset:128 sc1\n"
      "v_mfma_f32_16x16x32_bf16 v[52:55], a[8:11], v[128:131], 0\n"
      "v_mfma_f32_16x16x32_bf16 v[56:59], a[12:15], v[128:131], 0\n"
      "v_mfma_f32_16x16x32_bf16 v[60:63], a[16:19], v[128:131], 0\n"
      "ds_read_b128 v[128:131], v1 offset:1024\n"
      "s_waitcnt lgkmcnt(3)\n"
      "v_mfma_f32_16x16x32_bf16 v[64:67], a[4:7], v[132:135], 0\n"
      "v_mfma_f32_16x16x32_bf16 v[68:71], a[8:11], v[132:135], 0\n"
      "v_mfma_f32_16x16x32_bf16 v[72:75], a[12:15], v[132:135], 0\n"
      "v_mfma_f32_16x16x32_bf16 v[76:79], a[16:19], v[132:135], 0\n"
      "ds_read_b128 v[132:135], v1 offset:5120\n"
      "s_waitcnt lgkmcnt(3)\n"
      "v_mfma_f32_16x16x32_bf16 v[80:83], a[4:7], v[136:139], 0\n"
      "v_mfma_f32_16x16x32_bf16 v[84:87], a[8:11], v[136:139], 0\n"
      "v_mfma_f32_16x16x32_bf16 v[88:91], a[12:15], v[136:139], 0\n"
      "v_mfma_f32_16x16x32_bf16 v[92:95], a[16:19], v[136:139], 0\n"
      "ds_read_b128 v[136:139], v1 offset:9216\n"
      "s_waitcnt lgkmcnt(3)\n"
      "v_mfma_f32_16x16x32_bf16 v[96:99], a[4:7], v[140:143], 0\n"
      "v_mfma_f32_16x16x32_bf16 v[100:103], a[8:11], v[140:143], 0\n"
      "s_add_u32 s89, s89, 4096\n"
      "buffer_load_dwordx4 a[4:7], v1, s[16:19], s89 offen sc1\n"
      "v_mfma_f32_16x16x32_bf16 v[104:107], a[12:15], v[140:143], 0\n"
      "v_mfma_f32_16x16x32_bf16 v[108:111], a[16:19], v[140:143], 0\n"
      "ds_read_b128 v[140:143], v1 offset:13312\n"
      "s_waitcnt lgkmcnt(3)\n"
      "v_mfma_f32_16x16x32_bf16 v[48:51], a[20:23], v[128:131], v[48:51]\n"
      "v_mfma_f32_16x16x32_bf16 v[52:55], a[24:27], v[128:131], v[52:55]\n"
      "buffer_load_dwordx4 a[8:11], v1, s[16:19], s89 offen offset:1024 sc1\n"
      "v_mfma_f32_16x16x32_bf16 v[56:59], a[28:31], v[128:131], v[56:59]\n"
      "v_mfma_f32_16x16x32_bf16 v[60:63], a[32:35], v[128:131], v[60:63]\n"
      "ds_read_b128 v[128:131], v1 offset:2048\n"
      "s_waitcnt lgkmcnt(3)\n"
      "v_mfma_f32_16x16x32_bf16 v[64:67], a[20:23], v[132:135], v[64:67]\n"
      "v_mfma_f32_16x16x32_bf16 v[68:71], a[24:27], v[132:135], v[68:71]\n"
      "buffer_load_dwordx4 a[12:15], v1, s[16:19], s89 offen offset:2048 sc1\n"
      "v_mfma_f32_16x16x32_bf16 v[72:75], a[28:31], v[132:135], v[72:75]\n"
      "v_mfma_f32_16x16x32_bf16 v[76:79], a[32:35], v[132:135], v[76:79]\n"
      "ds_read_b128 v[132:135], v1 offset:6144\n"
      "s_waitcnt lgkmcnt(3)\n"
      "v_mfma_f32_16x16x32_bf16 v[80:83], a[20:23], v[136:139], v[80:83]\n"
      "v_mfma_f32_16x16x32_bf16 v[84:87], a[24:27], v[136:139], v[84:87]\n"
      "buffer_load_dwordx4 a[16:19], v1, s[16:19], s89 offen offset:3072 sc1\n"
      "v_mfma_f32_16x16x32_bf16 v[88:91], a[28:31], v[136:139], v[88:91]\n"
      "v_mfma_f32_16x16x32_bf16 v[92:95], a[32:35], v[136:139], v[92:95]\n"
      "ds_read_b128 v[136:139], v1 offset:10240\n"
      "s_waitcnt lgkmcnt(3)\n"
      "v_mfma_f32_16x16x32_bf16 v[96:99], a[20:23], v[140:143], v[96:99]\n"
      "v_mfma_f32_16x16x32_bf16 v[100:103], a[24:27], v[140:143], v[100:103]\n"
      "s_add_u32 s89, s89, 4096\n"
      "buffer_load_dwordx4 a[20:23], v1, s[16:19], s89 offen sc1\n"
      "v_mfma_f32_16x16x32_bf16 v[104:107], a[28:31], v[140:143], v[104:107]\n"
      "v_mfma_f32_16x16x32_bf16 v[108:111], a[32:35], v[140:143], v[108:111]\n"
      "ds_read_b128 v[140:143], v1 offset:14336\n"
      "s_waitcnt lgkmcnt(3)\n"
      "v_mfma_f32_16x16x32_bf16 v[48:51], a[36:39], v[128:131], v[48:51]\n"
      "v_mfma_f32_16x16x32_bf16 v[52:55], a[40:43], v[128:131], v[52:55]\n"
      "buffer_load_dwordx4 a[24:27], v1, s[16:19], s89 offen offset:1024 sc1\n"
      "v_mfma_f32_16x16x32_bf16 v[56:59], a[44:47], v[128:131], v[56:59]\n"
      "v_mfma_f32_16x16x32_bf16 v[60:63], a[48:51], v[128:131], v[60:63]\n"
      "ds_read_b128 v[128:131], v1 offset:3072\n"
      "s_waitcnt lgkmcnt(3)\n"
      "v_mfma_f32_16x16x32_bf16 v[64:67], a[36:39], v[132:135], v[64:67]\n"
      "v_mfma_f32_16x16x32_bf16 v[68:71], a[40:43], v[132:135], v[68:71]\n"
      "buffer_load_dwordx4 a[28:31], v1, s[16:19], s89 offen offset:2048 sc1\n"
      "v_mfma_f32_16x16x32_bf16 v[72:75], a[44:47], v[132:135], v[72:75]\n"
      "v_mfma_f32_16x16x32_bf16 v[76:79], a[48:51], v[132:135], v[76:79]\n"
      "ds_read_b128 v[132:135], v1 offset:7168\n"
      "s_waitcnt lgkmcnt(3)\n"
      "v_mfma_f32_16x16x32_bf16 v[80:83], a[36:39], v[136:139], v[80:83]\n"
      "v_mfma_f32_16x16x32_bf16 v[84:87], a[40:43], v[136:139], v[84:87]\n"
      "buffer_load_dwordx4 a[32:35], v1, s[16:19], s89 offen offset:3072 sc1\n"
      "v_mfma_f32_16x16x32_bf16 v[88:91], a[44:47], v[136:139], v[88:91]\n"
      "v_mfma_f32_16x16x32_bf16 v[92:95], a[48:51], v[136:139], v[92:95]\n"
      "s_waitcnt vmcnt(10)\n"
      "s_barrier\n"
      "s_add_u32 s76, s67, 2768896\n"
      "buffer_store_dwordx2 v[28:29], v21, s[16:19], s76 offen sc1\n"
      "ds_read_b128 v[136:139], v1 offset:11264\n"
      "s_waitcnt lgkmcnt(3)\n"
      "v_mfma_f32_16x16x32_bf16 v[96:99], a[36:39], v[140:143], v[96:99]\n"
      "v_mfma_f32_16x16x32_bf16 v[100:103], a[40:43], v[140:143], v[100:103]\n"
      "v_mfma_f32_16x16x32_bf16 v[104:107], a[44:47], v[140:143], v[104:107]\n"
      "v_mfma_f32_16x16x32_bf16 v[108:111], a[48:51], v[140:143], v[108:111]\n"
      "s_add_u32 s89, s89, 4096\n"
      "buffer_load_dwordx4 a[36:39], v1, s[16:19], s89 offen sc1\n"
      "ds_read_b128 v[140:143], v1 offset:15360\n"
      "s_waitcnt lgkmcnt(3)\n"
      "v_mfma_f32_16x16x32_bf16 v[48:51], a[52:55], v[128:131], v[48:51]\n"
      "v_mfma_f32_16x16x32_bf16 v[52:55], a[56:59], v[128:131], v[52:55]\n"
      "v_mfma_f32_16x16x32_bf16 v[56:59], a[60:63], v[128:131], v[56:59]\n"
      "v_mfma_f32_16x16x32_bf16 v[60:63], a[64:67], v[128:131], v[60:63]\n"
      "buffer_load_dwordx4 a[40:43], v1, s[16:19], s89 offen offset:1024 sc1\n"
      "s_waitcnt lgkmcnt(2)\n"
      "v_mfma_f32_16x16x32_bf16 v[64:67], a[52:55], v[132:135], v[64:67]\n"
      "v_mfma_f32_16x16x32_bf16 v[68:71], a[56:59], v[132:135], v[68:71]\n"
      "v_mfma_f32_16x16x32_bf16 v[72:75], a[60:63], v[132:135], v[72:75]\n"
      "v_mfma_f32_16x16x32_bf16 v[76:79], a[64:67], v[132:135], v[76:79]\n"
      "buffer_load_dwordx4 a[44:47], v1, s[16:19], s89 offen offset:2048 sc1\n"
      "s_waitcnt lgkmcnt(0)\n"
      "s_barrier\n"
      "ds_read_b128 v[128:131], v1 offset:20480\n"
      "ds_read_b128 v[132:135], v1 offset:24576\n"
      "v_mfma_f32_16x16x32_bf16 v[80:83], a[52:55], v[136:139], v[80:83]\n"
      "buffer_load_dwordx4 a[48:51], v1, s[16:19], s89 offen offset:3072 sc1\n"
      "v_mfma_f32_16x16x32_bf16 v[84:87], a[56:59], v[136:139], v[84:87]\n"
      "v_mfma_f32_16x16x32_bf16 v[88:91], a[60:63], v[136:139], v[88:91]\n"
      "v_mfma_f32_16x16x32_bf16 v[92:95], a[64:67], v[136:139], v[92:95]\n"
      "ds_read_b128 v[136:139], v1 offset:28672\n"
      "v_mfma_f32_16x16x32_bf16 v[96:99], a[52:55], v[140:143], v[96:99]\n"
      "s_waitcnt vmcnt(13)\n"
      "v_add_f32 v144, v144, v146\n"
      "v_add_f32 v145, v145, v147\n"
      "v_mfma_f32_16x16x32_bf16 v[100:103], a[56:59], v[140:143], v[100:103]\n"
      "s_add_u32 s89, s89, 4096\n"
      "buffer_load_dwordx4 a[52:55], v1, s[16:19], s89 offen sc1\n"
      "v_mov_b32 v156, v144\n"
      "v_mov_b32 v157, v145\n"
      "v_mfma_f32_16x16x32_bf16 v[104:107], a[60:63], v[140:143], v[104:107]\n"
      "v_permlane16_swap_b32 v156, v144\n"
      "v_permlane16_swap_b32 v157, v145\n"
      "v_mfma_f32_16x16x32_bf16 v[108:111], a[64:67], v[140:143], v[108:111]\n"
      "v_add_f32 v144, v144, v156\n"
      "v_add_f32 v145, v145, v157\n"
      "ds_read_b128 v[140:143], v1 offset:32768\n"
      "s_waitcnt lgkmcnt(3)\n"
      "v_mfma_f32_16x16x32_bf16 v[48:51], a[68:71], v[128:131], v[48:51]\n"
      "v_mov_b32 v156, v144\n"
      "v_mov_b32 v157, v145\n"
      "v_mfma_f32_16x16x32_bf16 v[52:55], a[72:75], v[128:131], v[52:55]\n"
      "buffer_load_dwordx4 a[56:59], v1, s[16:19], s89 offen offset:1024 sc1\n"
      "v_permlane32_swap_b32 v156, v144\n"
      "v_permlane32_swap_b32 v157, v145\n"
      "v_mfma_f32_16x16x32_bf16 v[56:59], a[76:79], v[128:131], v[56:59]\n"
      "v_add_f32 v144, v144, v156\n"
      "v_add_f32 v145, v145, v157\n"
      "v_mfma_f32_16x16x32_bf16 v[60:63], a[80:83], v[128:131], v[60:63]\n"
      "v_mul_f32 v146, 0x3acccccd, v144\n"
      "v_mul_f32 v145, 0x3acccccd, v145\n"
      "ds_read_b128 v[128:131], v1 offset:21504\n"
      "s_waitcnt lgkmcnt(3)\n"
      "v_mfma_f32_16x16x32_bf16 v[64:67], a[68:71], v[132:135], v[64:67]\n"
      "v_sub_f32 v159, 0, v146\n"
      "v_fma_f32 v145, -v146, v146, v145\n"
      "v_mfma_f32_16x16x32_bf16 v[68:71], a[72:75], v[132:135], v[68:71]\n"
      "buffer_load_dwordx4 a[60:63], v1, s[16:19], s89 offen offset:2048 sc1\n"
      "v_max_f32 v145, 0, v145\n"
      "v_add_f32 v145, 0x3727c5ac, v145\n"
      "v_mfma_f32_16x16x32_bf16 v[72:75], a[76:79], v[132:135], v[72:75]\n"
      "v_rcp_f32 v158, v145\n"
      "v_mfma_f32_16x16x32_bf16 v[76:79], a[80:83], v[132:135], v[76:79]\n"
      "ds_write_b64 v26, v[158:159] offset:640\n"
      "ds_read_b128 v[132:135], v1 offset:25600\n"
      "s_waitcnt lgkmcnt(4)\n"
      "v_mfma_f32_16x16x32_bf16 v[80:83], a[68:71], v[136:139], v[80:83]\n"
      "v_mfma_f32_16x16x32_bf16 v[84:87], a[72:75], v[136:139], v[84:87]\n"
      "buffer_load_dwordx4 a[64:67], v1, s[16:19], s89 offen offset:3072 sc1\n"
      "v_mfma_f32_16x16x32_bf16 v[88:91], a[76:79], v[136:139], v[88:91]\n"
      "v_mfma_f32_16x16x32_bf16 v[92:95], a[80:83], v[136:139], v[92:95]\n"
      "ds_read_b128 v[136:139], v1 offset:29696\n"
      "s_waitcnt lgkmcnt(4)\n"
      "v_mfma_f32_16x16x32_bf16 v[96:99], a[68:71], v[140:143], v[96:99]\n"
      "v_mfma_f32_16x16x32_bf16 v[100:103], a[72:75], v[140:143], v[100:103]\n"
      "s_add_u32 s89, s89, 4096\n"
      "buffer_load_dwordx4 a[68:71], v1, s[16:19], s89 offen sc1\n"
      "v_mfma_f32_16x16x32_bf16 v[104:107], a[76:79], v[140:143], v[104:107]\n"
      "v_mfma_f32_16x16x32_bf16 v[108:111], a[80:83], v[140:143], v[108:111]\n"
      "ds_read_b128 v[140:143], v1 offset:33792\n"
      "s_waitcnt lgkmcnt(4)\n"
      "v_mfma_f32_16x16x32_bf16 v[48:51], a[84:87], v[128:131], v[48:51]\n"
      "v_mfma_f32_16x16x32_bf16 v[52:55], a[88:91], v[128:131], v[52:55]\n"
      "buffer_load_dwordx4 a[72:75], v1, s[16:19], s89 offen offset:1024 sc1\n"
      "v_mfma_f32_16x16x32_bf16 v[56:59], a[92:95], v[128:131], v[56:59]\n"
      "v_mfma_f32_16x16x32_bf16 v[60:63], a[96:99], v[128:131], v[60:63]\n"
      "ds_read_b128 v[128:131], v1 offset:22528\n"
      "s_waitcnt lgkmcnt(3)\n"
      "v_mfma_f32_16x16x32_bf16 v[64:67], a[84:87], v[132:135], v[64:67]\n"
      "v_mfma_f32_16x16x32_bf16 v[68:71], a[88:91], v[132:135], v[68:71]\n"
      "buffer_load_dwordx4 a[76:79], v1, s[16:19], s89 offen offset:2048 sc1\n"
      "v_mfma_f32_16x16x32_bf16 v[72:75], a[92:95], v[132:135], v[72:75]\n"
      "v_mfma_f32_16x16x32_bf16 v[76:79], a[96:99], v[132:135], v[76:79]\n"
      "ds_read_b128 v[132:135], v1 offset:26624\n"
      "s_waitcnt lgkmcnt(3)\n"
      "v_mfma_f32_16x16x32_bf16 v[80:83], a[84:87], v[136:139], v[80:83]\n"
      "v_mfma_f32_16x16x32_bf16 v[84:87], a[88:91], v[136:139], v[84:87]\n"
      "buffer_load_dwordx4 a[80:83], v1, s[16:19], s89 offen offset:3072 sc1\n"
      "v_mfma_f32_16x16x32_bf16 v[88:91], a[92:95], v[136:139], v[88:91]\n"
      "v_mfma_f32_16x16x32_bf16 v[92:95], a[96:99], v[136:139], v[92:95]\n"
      "ds_read_b128 v[136:139], v1 offset:30720\n"
      "s_waitcnt lgkmcnt(3)\n"
      "v_mfma_f32_16x16x32_bf16 v[96:99], a[84:87], v[140:143], v[96:99]\n"
      "v_mfma_f32_16x16x32_bf16 v[100:103], a[88:91], v[140:143], v[100:103]\n"
      "s_add_u32 s89, s89, 4096\n"
      "buffer_load_dwordx4 a[84:87], v1, s[16:19], s89 offen sc1\n"
      "v_mfma_f32_16x16x32_bf16 v[104:107], a[92:95], v[140:143], v[104:107]\n"
      "v_mfma_f32_16x16x32_bf16 v[108:111], a[96:99], v[140:143], v[108:111]\n"
      "ds_read_b128 v[140:143], v1 offset:34816\n"
      "s_waitcnt lgkmcnt(3)\n"
      "v_mfma_f32_16x16x32_bf16 v[48:51], a[100:103], v[128:131], v[48:51]\n"
      "v_mfma_f32_16x16x32_bf16 v[52:55], a[104:107], v[128:131], v[52:55]\n"
      "buffer_load_dwordx4 a[88:91], v1, s[16:19], s89 offen offset:1024 sc1\n"
      "v_mfma_f32_16x16x32_bf16 v[56:59], a[108:111], v[128:131], v[56:59]\n"
      "v_mfma_f32_16x16x32_bf16 v[60:63], a[112:115], v[128:131], v[60:63]\n"
      "ds_read_b128 v[128:131], v1 offset:23552\n"
      "s_waitcnt lgkmcnt(3)\n"
      "v_mfma_f32_16x16x32_bf16 v[64:67], a[100:103], v[132:135], v[64:67]\n"
      "v_mfma_f32_16x16x32_bf16 v[68:71], a[104:107], v[132:135], v[68:71]\n"
      "buffer_load_dwordx4 a[92:95], v1, s[16:19], s89 offen offset:2048 sc1\n"
      "v_mfma_f32_16x16x32_bf16 v[72:75], a[108:111], v[132:135], v[72:75]\n"
      "v_mfma_f32_16x16x32_bf16 v[76:79], a[112:115], v[132:135], v[76:79]\n"
      "ds_read_b128 v[132:135], v1 offset:27648\n"
      "s_waitcnt lgkmcnt(3)\n"
      "v_mfma_f32_16x16x32_bf16 v[80:83], a[100:103], v[136:139], v[80:83]\n"
      "v_mfma_f32_16x16x32_bf16 v[84:87], a[104:107], v[136:139], v[84:87]\n"
      "buffer_load_dwordx4 a[96:99], v1, s[16:19], s89 offen offset:3072 sc1\n"
      "v_mfma_f32_16x16x32_bf16 v[88:91], a[108:111], v[136:139], v[88:91]\n"
      "v_mfma_f32_16x16x32_bf16 v[92:95], a[112:115], v[136:139], v[92:95]\n"
      "ds_read_b128 v[136:139], v1 offset:31744\n"
      "s_waitcnt lgkmcnt(3)\n"
      "v_mfma_f32_16x16x32_bf16 v[96:99], a[100:103], v[140:143], v[96:99]\n"
      "v_mfma_f32_16x16x32_bf16 v[100:103], a[104:107], v[140:143], v[100:103]\n"
      "s_add_u32 s89, s89, 4096\n"
      "buffer_load_dwordx4 a[100:103], v1, s[16:19], s89 offen sc1\n"
      "v_mfma_f32_16x16x32_bf16 v[104:107], a[108:111], v[140:143], v[104:107]\n"
      "v_mfma_f32_16x16x32_bf16 v[108:111], a[112:115], v[140:143], v[108:111]\n"
      "ds_read_b128 v[140:143], v1 offset:35840\n"
      "s_waitcnt lgkmcnt(3)\n"
      "v_mfma_f32_16x16x32_bf16 v[48:51], a[116:119], v[128:131], v[48:51]\n"
      "v_mfma_f32_16x16x32_bf16 v[52:55], a[120:123], v[128:131], v[52:55]\n"
      "buffer_load_dwordx4 a[104:107], v1, s[16:19], s89 offen offset:1024 sc1\n"
      "v_mfma_f32_16x16x32_bf16 v[56:59], a[124:127], v[128:131], v[56:59]\n"
      "v_mfma_f32_16x16x32_bf16 v[60:63], a[128:131], v[128:131], v[60:63]\n"
      "s_waitcnt lgkmcnt(2)\n"
      "v_mfma_f32_16x16x32_bf16 v[64:67], a[116:119], v[132:135], v[64:67]\n"
      "v_mfma_f32_16x16x32_bf16 v[68:71], a[120:123], v[132:135], v[68:71]\n"
      "buffer_load_dwordx4 a[108:111], v1, s[16:19], s89 offen offset:2048 sc1\n"
      "v_mfma_f32_16x16x32_bf16 v[72:75], a[124:127], v[132:135], v[72:75]\n"
      "v_mfma_f32_16x16x32_bf16 v[76:79], a[128:131], v[132:135], v[76:79]\n"
      "s_waitcnt lgkmcnt(0)\n"
      "s_barrier\n"
      "ds_read_b128 v[128:131], v1 offset:40960\n"
      "ds_read_b128 v[132:135], v1 offset:45056\n"
      "v_mfma_f32_16x16x32_bf16 v[80:83], a[116:119], v[136:139], v[80:83]\n"
      "buffer_load_dwordx4 a[112:115], v1, s[16:19], s89 offen offset:3072 sc1\n"
      "v_mfma_f32_16x16x32_bf16 v[84:87], a[120:123], v[136:139], v[84:87]\n"
      "v_mfma_f32_16x16x32_bf16 v[88:91], a[124:127], v[136:139], v[88:91]\n"
      "v_mfma_f32_16x16x32_bf16 v[92:95], a[128:131], v[136:139], v[92:95]\n"
      "ds_read_b128 v[136:139], v1 offset:49152\n"
      "v_mfma_f32_16x16x32_bf16 v[96:99], a[116:119], v[140:143], v[96:99]\n"
      "v_mfma_f32_16x16x32_bf16 v[100:103], a[120:123], v[140:143], v[100:103]\n"
      "s_add_u32 s89, s89, 4096\n"
      "buffer_load_dwordx4 a[116:119], v1, s[16:19], s89 offen sc1\n"
      "v_mfma_f32_16x16x32_bf16 v[104:107], a[124:127], v[140:143], v[104:107]\n"
      "v_mfma_f32_16x16x32_bf16 v[108:111], a[128:131], v[140:143], v[108:111]\n"
      "s_add_u32 s87, s66, 2768896\n"
      "buffer_load_dwordx2 v[30:31], v19, s[16:19], s87 offen sc1\n"
      "ds_read_b128 v[140:143], v1 offset:53248\n"
      "s_waitcnt lgkmcnt(3)\n"
      "v_mfma_f32_16x16x32_bf16 v[48:51], a[132:135], v[128:131], v[48:51]\n"
      "v_mfma_f32_16x16x32_bf16 v[52:55], a[136:139], v[128:131], v[52:55]\n"
      "v_mfma_f32_16x16x32_bf16 v[56:59], a[140:143], v[128:131], v[56:59]\n"
      "v_mfma_f32_16x16x32_bf16 v[60:63], a[144:147], v[128:131], v[60:63]\n"
      "buffer_load_dwordx4 a[120:123], v1, s[16:19], s89 offen offset:1024 sc1\n"
      "ds_read_b128 v[128:131], v1 offset:41984\n"
      "s_waitcnt lgkmcnt(3)\n"
      "v_mfma_f32_16x16x32_bf16 v[64:67], a[132:135], v[132:135], v[64:67]\n"
      "v_mfma_f32_16x16x32_bf16 v[68:71], a[136:139], v[132:135], v[68:71]\n"
      "v_mfma_f32_16x16x32_bf16 v[72:75], a[140:143], v[132:135], v[72:75]\n"
      "v_mfma_f32_16x16x32_bf16 v[76:79], a[144:147], v[132:135], v[76:79]\n"
      "buffer_load_dwordx4 a[124:127], v1, s[16:19], s89 offen offset:2048 sc1\n"
      "ds_read_b128 v[132:135], v1 offset:46080\n"
      "s_waitcnt lgkmcnt(3)\n"
      "v_mfma_f32_16x16x32_bf16 v[80:83], a[132:135], v[136:139], v[80:83]\n"
      "v_mfma_f32_16x16x32_bf16 v[84:87], a[136:139], v[136:139], v[84:87]\n"
      "v_mfma_f32_16x16x32_bf16 v[88:91], a[140:143], v[136:139], v[88:91]\n"
      "v_mfma_f32_16x16x32_bf16 v[92:95], a[144:147], v[136:139], v[92:95]\n"
      "buffer_load_dwordx4 a[128:131], v1, s[16:19], s89 offen offset:3072 sc1\n"
      "ds_read_b128 v[136:139], v1 offset:50176\n"
      "s_waitcnt lgkmcnt(3)\n"
      "v_mfma_f32_16x16x32_bf16 v[96:99], a[132:135], v[140:143], v[96:99]\n"
      "v_mfma_f32_16x16x32_bf16 v[100:103], a[136:139], v[140:143], v[100:103]\n"
      "v_mfma_f32_16x16x32_bf16 v[104:107], a[140:143], v[140:143], v[104:107]\n"
      "v_mfma_f32_16x16x32_bf16 v[108:111], a[144:147], v[140:143], v[108:111]\n"
      "s_add_u32 s89, s89, 4096\n"
      "buffer_load_dwordx4 a[132:135], v1, s[16:19], s89 offen sc1\n"
      "ds_read_b128 v[140:143], v1 offset:54272\n"
      "s_waitcnt lgkmcnt(3)\n"
      "v_mfma_f32_16x16x32_bf16 v[48:51], a[148:151], v[128:131], v[48:51]\n"
      "v_mfma_f32_16x16x32_bf16 v[52:55], a[152:155], v[128:131], v[52:55]\n"
      "v_mfma_f32_16x16x32_bf16 v[56:59], a[156:159], v[128:131], v[56:59]\n"
      "v_mfma_f32_16x16x32_bf16 v[60:63], a[160:163], v[128:131], v[60:63]\n"
      "buffer_load_dwordx4 a[136:139], v1, s[16:19], s89 offen offset:1024 sc1\n"
      "ds_read_b128 v[128:131], v1 offset:43008\n"
      "s_waitcnt lgkmcnt(3)\n"
      "v_mfma_f32_16x16x32_bf16 v[64:67], a[148:151], v[132:135], v[64:67]\n"
      "v_mfma_f32_16x16x32_bf16 v[68:71], a[152:155], v[132:135], v[68:71]\n"
      "v_mfma_f32_16x16x32_bf16 v[72:75], a[156:159], v[132:135], v[72:75]\n"
      "v_mfma_f32_16x16x32_bf16 v[76:79], a[160:163], v[132:135], v[76:79]\n"
      "buffer_load_dwordx4 a[140:143], v1, s[16:19], s89 offen offset:2048 sc1\n"
      "ds_read_b128 v[132:135], v1 offset:47104\n"
      "s_waitcnt lgkmcnt(3)\n"
      "v_mfma_f32_16x16x32_bf16 v[80:83], a[148:151], v[136:139], v[80:83]\n"
      "v_mfma_f32_16x16x32_bf16 v[84:87], a[152:155], v[136:139], v[84:87]\n"
      "v_mfma_f32_16x16x32_bf16 v[88:91], a[156:159], v[136:139], v[88:91]\n"
      "v_mfma_f32_16x16x32_bf16 v[92:95], a[160:163], v[136:139], v[92:95]\n"
      "buffer_load_dwordx4 a[144:147], v1, s[16:19], s89 offen offset:3072 sc1\n"
      "ds_read_b128 v[136:139], v1 offset:51200\n"
      "s_waitcnt lgkmcnt(3)\n"
      "v_mfma_f32_16x16x32_bf16 v[96:99], a[148:151], v[140:143], v[96:99]\n"
      "v_mfma_f32_16x16x32_bf16 v[100:103], a[152:155], v[140:143], v[100:103]\n"
      "v_mfma_f32_16x16x32_bf16 v[104:107], a[156:159], v[140:143], v[104:107]\n"
      "v_mfma_f32_16x16x32_bf16 v[108:111], a[160:163], v[140:143], v[108:111]\n"
      "s_add_u32 s89, s89, 4096\n"
      "buffer_load_dwordx4 a[148:151], v1, s[16:19], s89 offen sc1\n"
      "ds_read_b128 v[140:143], v1 offset:55296\n"
      "s_waitcnt lgkmcnt(3)\n"
      "v_mfma_f32_16x16x32_bf16 v[48:51], a[164:167], v[128:131], v[48:51]\n"
      "v_mfma_f32_16x16x32_bf16 v[52:55], a[168:171], v[128:131], v[52:55]\n"
      "v_mfma_f32_16x16x32_bf16 v[56:59], a[172:175], v[128:131], v[56:59]\n"
      "v_mfma_f32_16x16x32_bf16 v[60:63], a[176:179], v[128:131], v[60:63]\n"
      "buffer_load_dwordx4 a[152:155], v1, s[16:19], s89 offen offset:1024 sc1\n"
      "ds_read_b128 v[128:131], v1 offset:44032\n"
      "s_waitcnt lgkmcnt(3)\n"
      "v_mfma_f32_16x16x32_bf16 v[64:67], a[164:167], v[132:135], v[64:67]\n"
      "v_mfma_f32_16x16x32_bf16 v[68:71], a[168:171], v[132:135], v[68:71]\n"
      "v_mfma_f32_16x16x32_bf16 v[72:75], a[172:175], v[132:135], v[72:75]\n"
      "v_mfma_f32_16x16x32_bf16 v[76:79], a[176:179], v[132:135], v[76:79]\n"
      "buffer_load_dwordx4 a[156:159], v1, s[16:19], s89 offen offset:2048 sc1\n"
      "ds_read_b128 v[132:135], v1 offset:48128\n"
      "s_waitcnt lgkmcnt(3)\n"
      "v_mfma_f32_16x16x32_bf16 v[80:83], a[164:167], v[136:139], v[80:83]\n"
      "v_mfma_f32_16x16x32_bf16 v[84:87], a[168:171], v[136:139], v[84:87]\n"
      "v_mfma_f32_16x16x32_bf16 v[88:91], a[172:175], v[136:139], v[88:91]\n"
      "v_mfma_f32_16x16x32_bf16 v[92:95], a[176:179], v[136:139], v[92:95]\n"
      "buffer_load_dwordx4 a[160:163], v1, s[16:19], s89 offen offset:3072 sc1\n"
      "ds_read_b128 v[136:139], v1 offset:52224\n"
      "s_waitcnt lgkmcnt(3)\n"
      "v_mfma_f32_16x16x32_bf16 v[96:99], a[164:167], v[140:143], v[96:99]\n"
      "v_mfma_f32_16x16x32_bf16 v[100:103], a[168:171], v[140:143], v[100:103]\n"
      "v_mfma_f32_16x16x32_bf16 v[104:107], a[172:175], v[140:143], v[104:107]\n"
      "v_mfma_f32_16x16x32_bf16 v[108:111], a[176:179], v[140:143], v[108:111]\n"
      "s_add_u32 s89, s89, 4096\n"
      "buffer_load_dwordx4 a[164:167], v1, s[16:19], s89 offen sc1\n"
      "ds_read_b128 v[140:143], v1 offset:56320\n"
      "s_waitcnt lgkmcnt(3)\n"
      "v_mfma_f32_16x16x32_bf16 v[48:51], a[180:183], v[128:131], v[48:51]\n"
      "v_mfma_f32_16x16x32_bf16 v[52:55], a[184:187], v[128:131], v[52:55]\n"
      "v_mfma_f32_16x16x32_bf16 v[56:59], a[188:191], v[128:131], v[56:59]\n"
      "v_mfma_f32_16x16x32_bf16 v[60:63], a[192:195], v[128:131], v[60:63]\n"
      "buffer_load_dwordx4 a[168:171], v1, s[16:19], s89 offen offset:1024 sc1\n"
      "s_waitcnt lgkmcnt(2)\n"
      "v_mfma_f32_16x16x32_bf16 v[64:67], a[180:183], v[132:135], v[64:67]\n"
      "s_waitcnt vmcnt(13)\n"
      "v_cmp_eq_u64 vcc, s[36:37], v[30:31]\n"
      "s_nop 3\n"
      "s_cmp_eq_u64 vcc, -1\n"
      "s_cbranch_scc1 Lpd_15\n"
      "Lps_16:\n"
      "s_sleep 1\n"
      "buffer_load_dwordx2 v[30:31], v19, s[16:19], s87 offen sc1\n"
      "s_waitcnt vmcnt(0)\n"
      "v_cmp_eq_u64 vcc, s[36:37], v[30:31]\n"
      "s_nop 3\n"
      "s_cmp_eq_u64 vcc, -1\n"
      "s_cbranch_scc1 Lpd_15\n"
      "s_add_u32 s42, s42, 1\n"
      "s_cmp_lt_u32 s42, 0x40000\n"
      "s_cbranch_scc1 Lps_16\n"
      "Lpd_15:\n"
      "v_mfma_f32_16x16x32_bf16 v[68:71], a[184:187], v[132:135], v[68:71]\n"
      "v_mfma_f32_16x16x32_bf16 v[72:75], a[188:191], v[132:135], v[72:75]\n"
      "v_mfma_f32_16x16x32_bf16 v[76:79], a[192:195], v[132:135], v[76:79]\n"
      "buffer_load_dwordx4 a[172:175], v1, s[16:19], s89 offen offset:2048 sc1\n"
      "s_waitcnt lgkmcnt(0)\n"
      "s_barrier\n"
      "ds_read_b128 v[128:131], v2\n"
      "ds_read_b128 v[132:135], v2 offset:4096\n"
      "v_mfma_f32_16x16x32_bf16 v[80:83], a[180:183], v[136:139], v[80:83]\n"
      "buffer_load_dwordx4 a[176:179], v1, s[16:19], s89 offen offset:3072 sc1\n"
      "v_mfma_f32_16x16x32_bf16 v[84:87], a[184:187], v[136:139], v[84:87]\n"
      "v_mfma_f32_16x16x32_bf16 v[88:91], a[188:191], v[136:139], v[88:91]\n"
      "v_mfma_f32_16x16x32_bf16 v[92:95], a[192:195], v[136:139], v[92:95]\n"
      "ds_read_b128 v[136:139], v2 offset:8192\n"
      "v_mfma_f32_16x16x32_bf16 v[96:99], a[180:183], v[140:143], v[96:99]\n"
      "v_mfma_f32_16x16x32_bf16 v[100:103], a[184:187], v[140:143], v[100:103]\n"
      "s_add_u32 s89, s89, 4096\n"
      "buffer_load_dwordx4 a[180:183], v1, s[16:19], s89 offen sc1\n"
      "v_mfma_f32_16x16x32_bf16 v[104:107], a[188:191], v[140:143], v[104:107]\n"
      "v_mfma_f32_16x16x32_bf16 v[108:111], a[192:195], v[140:143], v[108:111]\n"
      "ds_read_b128 v[140:143], v2 offset:12288\n"
      "s_waitcnt lgkmcnt(3)\n"
      "v_mfma_f32_16x16x32_bf16 v[48:51], a[196:199], v[128:131], v[48:51]\n"
      "v_mfma_f32_16x16x32_bf16 v[52:55], a[200:203], v[128:131], v[52:55]\n"
      "buffer_load_dwordx4 a[184:187], v1, s[16:19], s89 offen offset:1024 sc1\n"
      "v_mfma_f32_16x16x32_bf16 v[56:59], a[204:207], v[128:131], v[56:59]\n"
      "v_mfma_f32_16x16x32_bf16 v[60:63], a[208:211], v[128:131], v[60:63]\n"
      "ds_read_b128 v[128:131], v2 offset:1024\n"
      "s_waitcnt lgkmcnt(3)\n"
      "v_mfma_f32_16x16x32_bf16 v[64:67], a[196:199], v[132:135], v[64:67]\n"
      "v_mfma_f32_16x16x32_bf16 v[68:71], a[200:203], v[132:135], v[68:71]\n"
      "buffer_load_dwordx4 a[188:191], v1, s[16:19], s89 offen offset:2048 sc1\n"
      "v_mfma_f32_16x16x32_bf16 v[72:75], a[204:207], v[132:135], v[72:75]\n"
      "v_mfma_f32_16x16x32_bf16 v[76:79], a[208:211], v[132:135], v[76:79]\n"
      "ds_read_b128 v[132:135], v2 offset:5120\n"
      "s_waitcnt lgkmcnt(3)\n"
      "v_mfma_f32_16x16x32_bf16 v[80:83], a[196:199], v[136:139], v[80:83]\n"
      "v_mfma_f32_16x16x32_bf16 v[84:87], a[200:203], v[136:139], v[84:87]\n"
      "buffer_load_dwordx4 a[192:195], v1, s[16:19], s89 offen offset:3072 sc1\n"
      "v_mfma_f32_16x16x32_bf16 v[88:91], a[204:207], v[136:139], v[88:91]\n"
      "v_mfma_f32_16x16x32_bf16 v[92:95], a[208:211], v[136:139], v[92:95]\n"
      "ds_read_b128 v[136:139], v2 offset:9216\n"
      "s_waitcnt lgkmcnt(3)\n"
      "v_mfma_f32_16x16x32_bf16 v[96:99], a[196:199], v[140:143], v[96:99]\n"
      "v_mfma_f32_16x16x32_bf16 v[100:103], a[200:203], v[140:143], v[100:103]\n"
      "s_add_u32 s89, s89, 4096\n"
      "buffer_load_dwordx4 a[196:199], v1, s[16:19], s89 offen sc1\n"
      "v_mfma_f32_16x16x32_bf16 v[104:107], a[204:207], v[140:143], v[104:107]\n"
      "v_mfma_f32_16x16x32_bf16 v[108:111], a[208:211], v[140:143], v[108:111]\n"
      "ds_read_b128 v[140:143], v2 offset:13312\n"
      "s_waitcnt lgkmcnt(3)\n"
      "v_mfma_f32_16x16x32_bf16 v[48:51], a[212:215], v[128:131], v[48:51]\n"
      "v_mfma_f32_16x16x32_bf16 v[52:55], a[216:219], v[128:131], v[52:55]\n"
      "buffer_load_dwordx4 a[200:203], v1, s[16:19], s89 offen offset:1024 sc1\n"
      "v_mfma_f32_16x16x32_bf16 v[56:59], a[220:223], v[128:131], v[56:59]\n"
      "v_mfma_f32_16x16x32_bf16 v[60:63], a[224:227], v[128:131], v[60:63]\n"
      "ds_read_b128 v[128:131], v2 offset:2048\n"
      "s_waitcnt lgkmcnt(3)\n"
      "v_mfma_f32_16x16x32_bf16 v[64:67], a[212:215], v[132:135], v[64:67]\n"
      "v_mfma_f32_16x16x32_bf16 v[68:71], a[216:219], v[132:135], v[68:71]\n"
      "buffer_load_dwordx4 a[204:207], v1, s[16:19], s89 offen offset:2048 sc1\n"
      "v_mfma_f32_16x16x32_bf16 v[72:75], a[220:223], v[132:135], v[72:75]\n"
      "v_mfma_f32_16x16x32_bf16 v[76:79], a[224:227], v[132:135], v[76:79]\n"
      "ds_read_b128 v[132:135], v2 offset:6144\n"
      "s_waitcnt lgkmcnt(3)\n"
      "v_mfma_f32_16x16x32_bf16 v[80:83], a[212:215], v[136:139], v[80:83]\n"
      "v_mfma_f32_16x16x32_bf16 v[84:87], a[216:219], v[136:139], v[84:87]\n"
      "buffer_load_dwordx4 a[208:211], v1, s[16:19], s89 offen offset:3072 sc1\n"
      "v_mfma_f32_16x16x32_bf16 v[88:91], a[220:223], v[136:139], v[88:91]\n"
      "v_mfma_f32_16x16x32_bf16 v[92:95], a[224:227], v[136:139], v[92:95]\n"
      "ds_read_b128 v[136:139], v2 offset:10240\n"
      "s_waitcnt lgkmcnt(3)\n"
      "v_mfma_f32_16x16x32_bf16 v[96:99], a[212:215], v[140:143], v[96:99]\n"
      "v_mfma_f32_16x16x32_bf16 v[100:103], a[216:219], v[140:143], v[100:103]\n"
      "s_add_u32 s89, s89, 4096\n"
      "buffer_load_dwordx4 a[212:215], v1, s[16:19], s89 offen sc1\n"
      "v_mfma_f32_16x16x32_bf16 v[104:107], a[220:223], v[140:143], v[104:107]\n"
      "v_mfma_f32_16x16x32_bf16 v[108:111], a[224:227], v[140:143], v[108:111]\n"
      "ds_read_b128 v[140:143], v2 offset:14336\n"
      "s_waitcnt lgkmcnt(3)\n"
      "v_mfma_f32_16x16x32_bf16 v[48:51], a[228:231], v[128:131], v[48:51]\n"
      "v_mfma_f32_16x16x32_bf16 v[52:55], a[232:235], v[128:131], v[52:55]\n"
      "buffer_load_dwordx4 a[216:219], v1, s[16:19], s89 offen offset:1024 sc1\n"
      "v_mfma_f32_16x16x32_bf16 v[56:59], a[236:239], v[128:131], v[56:59]\n"
      "v_mfma_f32_16x16x32_bf16 v[60:63], a[240:243], v[128:131], v[60:63]\n"
      "ds_read_b128 v[128:131], v2 offset:3072\n"
      "s_waitcnt lgkmcnt(3)\n"
      "v_mfma_f32_16x16x32_bf16 v[64:67], a[228:231], v[132:135], v[64:67]\n"
      "v_mfma_f32_16x16x32_bf16 v[68:71], a[232:235], v[132:135], v[68:71]\n"
      "buffer_load_dwordx4 a[220:223], v1, s[16:19], s89 offen offset:2048 sc1\n"
      "v_mfma_f32_16x16x32_bf16 v[72:75], a[236:239], v[132:135], v[72:75]\n"
      "v_mfma_f32_16x16x32_bf16 v[76:79], a[240:243], v[132:135], v[76:79]\n"
      "ds_read_b128 v[132:135], v2 offset:7168\n"
      "s_waitcnt lgkmcnt(3)\n"
      "v_mfma_f32_16x16x32_bf16 v[80:83], a[228:231], v[136:139], v[80:83]\n"
      "v_mfma_f32_16x16x32_bf16 v[84:87], a[232:235], v[136:139], v[84:87]\n"
      "buffer_load_dwordx4 a[224:227], v1, s[16:19], s89 offen offset:3072 sc1\n"
      "v_mfma_f32_16x16x32_bf16 v[88:91], a[236:239], v[136:139], v[88:91]\n"
      "v_mfma_f32_16x16x32_bf16 v[92:95], a[240:243], v[136:139], v[92:95]\n"
      "ds_read_b128 v[136:139], v2 offset:11264\n"
      "s_waitcnt lgkmcnt(3)\n"
      "v_mfma_f32_16x16x32_bf16 v[96:99], a[228:231], v[140:143], v[96:99]\n"
      "v_mfma_f32_16x16x32_bf16 v[100:103], a[232:235], v[140:143], v[100:103]\n"
      "s_add_u32 s89, s89, 4096\n"
      "buffer_load_dwordx4 a[228:231], v1, s[16:19], s89 offen sc1\n"
      "v_mfma_f32_16x16x32_bf16 v[104:107], a[236:239], v[140:143], v[104:107]\n"
      "v_mfma_f32_16x16x32_bf16 v[108:111], a[240:243], v[140:143], v[108:111]\n"
      "ds_read_b128 v[140:143], v2 offset:15360\n"
      "s_waitcnt lgkmcnt(3)\n"
      "v_mfma_f32_16x16x32_bf16 v[48:51], a[244:247], v[128:131], v[48:51]\n"
      "v_mfma_f32_16x16x32_bf16 v[52:55], a[248:251], v[128:131], v[52:55]\n"
      "buffer_load_dwordx4 a[232:235], v1, s[16:19], s89 offen offset:1024 sc1\n"
      "v_mfma_f32_16x16x32_bf16 v[56:59], a[252:255], v[128:131], v[56:59]\n"
      "v_mfma_f32_16x16x32_bf16 v[60:63], v[176:179], v[128:131], v[60:63]\n"
      "s_waitcnt lgkmcnt(2)\n"
      "v_mfma_f32_16x16x32_bf16 v[64:67], a[244:247], v[132:135], v[64:67]\n"
      "v_mfma_f32_16x16x32_bf16 v[68:71], a[248:251], v[132:135], v[68:71]\n"
      "buffer_load_dwordx4 a[236:239], v1, s[16:19], s89 offen offset:2048 sc1\n"
      "v_mfma_f32_16x16x32_bf16 v[72:75], a[252:255], v[132:135], v[72:75]\n"
      "v_mfma_f32_16x16x32_bf16 v[76:79], v[176:179], v[132:135], v[76:79]\n"
      "s_waitcnt lgkmcnt(0)\n"
      "s_barrier\n"
      "ds_read_b128 v[128:131], v2 offset:20480\n"
      "ds_read_b128 v[132:135], v2 offset:24576\n"
      "v_mfma_f32_16x16x32_bf16 v[80:83], a[244:247], v[136:139], v[80:83]\n"
      "buffer_load_dwordx4 a[240:243], v1, s[16:19], s89 offen offset:3072 sc1\n"
      "v_mfma_f32_16x16x32_bf16 v[84:87], a[248:251], v[136:139], v[84:87]\n"
      "v_mfma_f32_16x16x32_bf16 v[88:91], a[252:255], v[136:139], v[88:91]\n"
      "v_mfma_f32_16x16x32_bf16 v[92:95], v[176:179], v[136:139], v[92:95]\n"
      "ds_read_b128 v[136:139], v2 offset:28672\n"
      "v_mfma_f32_16x16x32_bf16 v[96:99], a[244:247], v[140:143], v[96:99]\n"
      "v_mfma_f32_16x16x32_bf16 v[100:103], a[248:251], v[140:143], v[100:103]\n"
      "s_add_u32 s89, s89, 4096\n"
      "buffer_load_dwordx4 a[244:247], v1, s[16:19], s89 offen sc1\n"
      "v_mfma_f32_16x16x32_bf16 v[104:107], a[252:255], v[140:143], v[104:107]\n"
      "v_mfma_f32_16x16x32_bf16 v[108:111], v[176:179], v[140:143], v[108:111]\n"
      "ds_read_b128 v[140:143], v2 offset:32768\n"
      "s_waitcnt lgkmcnt(3)\n"
      "v_mfma_f32_16x16x32_bf16 v[48:51], v[180:183], v[128:131], v[48:51]\n"
      "v_mfma_f32_16x16x32_bf16 v[52:55], v[184:187], v[128:131], v[52:55]\n"
      "buffer_load_dwordx4 a[248:251], v1, s[16:19], s89 offen offset:1024 sc1\n"
      "v_mfma_f32_16x16x32_bf16 v[56:59], v[188:191], v[128:131], v[56:59]\n"
      "v_mfma_f32_16x16x32_bf16 v[60:63], v[192:195], v[128:131], v[60:63]\n"
      "ds_read_b128 v[128:131], v2 offset:21504\n"
      "s_waitcnt lgkmcnt(3)\n"
      "v_mfma_f32_16x16x32_bf16 v[64:67], v[180:183], v[132:135], v[64:67]\n"
      "v_mfma_f32_16x16x32_bf16 v[68:71], v[184:187], v[132:135], v[68:71]\n"
      "buffer_load_dwordx4 a[252:255], v1, s[16:19], s89 offen offset:2048 sc1\n"
      "v_mfma_f32_16x16x32_bf16 v[72:75], v[188:191], v[132:135], v[72:75]\n"
      "v_mfma_f32_16x16x32_bf16 v[76:79], v[192:195], v[132:135], v[76:79]\n"
      "ds_read_b128 v[132:135], v2 offset:25600\n"
      "s_waitcnt lgkmcnt(3)\n"
      "v_mfma_f32_16x16x32_bf16 v[80:83], v[180:183], v[136:139], v[80:83]\n"
      "v_mfma_f32_16x16x32_bf16 v[84:87], v[184:187], v[136:139], v[84:87]\n"
      "buffer_load_dwordx4 v[176:179], v1, s[16:19], s89 offen offset:3072 sc1\n"
      "v_mfma_f32_16x16x32_bf16 v[88:91], v[188:191], v[136:139], v[88:91]\n"
      "v_mfma_f32_16x16x32_bf16 v[92:95], v[192:195], v[136:139], v[92:95]\n"
      "ds_read_b128 v[136:139], v2 offset:29696\n"
      "s_waitcnt lgkmcnt(3)\n"
      "v_mfma_f32_16x16x32_bf16 v[96:99], v[180:183], v[140:143], v[96:99]\n"
      "v_mfma_f32_16x16x32_bf16 v[100:103], v[184:187], v[140:143], v[100:103]\n"
      "s_add_u32 s89, s89, 4096\n"
      "buffer_load_dwordx4 v[180:183], v1, s[16:19], s89 offen sc1\n"
      "v_mfma_f32_16x16x32_bf16 v[104:107], v[188:191], v[140:143], v[104:107]\n"
      "v_mfma_f32_16x16x32_bf16 v[108:111], v[192:195], v[140:143], v[108:111]\n"
      "ds_read_b128 v[140:143], v2 offset:33792\n"
      "s_waitcnt lgkmcnt(3)\n"
      "v_mfma_f32_16x16x32_bf16 v[48:51], v[196:199], v[128:131], v[48:51]\n"
      "v_mfma_f32_16x16x32_bf16 v[52:55], v[200:203], v[128:131], v[52:55]\n"
      "s_add_u32 s99, s59, 15949824\n"
      "buffer_load_dwordx4 v[188:191], v1, s[16:19], s99 offen sc1\n"
      "v_mfma_f32_16x16x32_bf16 v[56:59], v[204:207], v[128:131], v[56:59]\n"
      "v_mfma_f32_16x16x32_bf16 v[60:63], v[208:211], v[128:131], v[60:63]\n"
      "ds_read_b128 v[128:131], v2 offset:22528\n"
      "s_waitcnt lgkmcnt(3)\n"
      "v_mfma_f32_16x16x32_bf16 v[64:67], v[196:199], v[132:135], v[64:67]\n"
      "v_mfma_f32_16x16x32_bf16 v[68:71], v[200:203], v[132:135], v[68:71]\n"
      "buffer_load_dwordx4 v[192:195], v1, s[16:19], s99 offen offset:1024 sc1\n"
      "v_mfma_f32_16x16x32_bf16 v[72:75], v[204:207], v[132:135], v[72:75]\n"
      "v_mfma_f32_16x16x32_bf16 v[76:79], v[208:211], v[132:135], v[76:79]\n"
      "ds_read_b128 v[132:135], v2 offset:26624\n"
      "s_waitcnt lgkmcnt(3)\n"
      "v_mfma_f32_16x16x32_bf16 v[80:83], v[196:199], v[136:139], v[80:83]\n"
      "v_mfma_f32_16x16x32_bf16 v[84:87], v[200:203], v[136:139], v[84:87]\n"
      "buffer_load_dwordx4 v[184:187], v1, s[16:19], s89 offen offset:1024 sc1\n"
      "v_mfma_f32_16x16x32_bf16 v[88:91], v[204:207], v[136:139], v[88:91]\n"
      "v_mfma_f32_16x16x32_bf16 v[92:95], v[208:211], v[136:139], v[92:95]\n"
      "ds_read_b128 v[136:139], v2 offset:30720\n"
      "s_waitcnt lgkmcnt(3)\n"
      "v_mfma_f32_16x16x32_bf16 v[96:99], v[196:199], v[140:143], v[96:99]\n"
      "v_mfma_f32_16x16x32_bf16 v[100:103], v[200:203], v[140:143], v[100:103]\n"
      "buffer_load_dwordx4 v[196:199], v1, s[16:19], s99 offen offset:2048 sc1\n"
      "v_mfma_f32_16x16x32_bf16 v[104:107], v[204:207], v[140:143], v[104:107]\n"
      "v_mfma_f32_16x16x32_bf16 v[108:111], v[208:211], v[140:143], v[108:111]\n"
      "ds_read_b128 v[140:143], v2 offset:34816\n"
      "s_waitcnt lgkmcnt(3)\n"
      "v_mfma_f32_16x16x32_bf16 v[48:51], v[212:215], v[128:131], v[48:51]\n"
      "v_mfma_f32_16x16x32_bf16 v[52:55], v[216:219], v[128:131], v[52:55]\n"
      "buffer_load_dwordx4 v[200:203], v1, s[16:19], s99 offen offset:3072 sc1\n"
      "v_mfma_f32_16x16x32_bf16 v[56:59], v[220:223], v[128:131], v[56:59]\n"
      "v_mfma_f32_16x16x32_bf16 v[60:63], v[224:227], v[128:131], v[60:63]\n"
      "ds_read_b128 v[128:131], v2 offset:23552\n"
      "s_waitcnt lgkmcnt(3)\n"
      "v_mfma_f32_16x16x32_bf16 v[64:67], v[212:215], v[132:135], v[64:67]\n"
      "v_mfma_f32_16x16x32_bf16 v[68:71], v[216:219], v[132:135], v[68:71]\n"
      "s_add_u32 s99, s99, 4096\n"
      "buffer_load_dwordx4 v[204:207], v1, s[16:19], s99 offen sc1\n"
      "v_mfma_f32_16x16x32_bf16 v[72:75], v[220:223], v[132:135], v[72:75]\n"
      "v_mfma_f32_16x16x32_bf16 v[76:79], v[224:227], v[132:135], v[76:79]\n"
      "ds_read_b128 v[132:135], v2 offset:27648\n"
      "s_waitcnt lgkmcnt(3)\n"
      "v_mfma_f32_16x16x32_bf16 v[80:83], v[212:215], v[136:139], v[80:83]\n"
      "v_mfma_f32_16x16x32_bf16 v[84:87], v[216:219], v[136:139], v[84:87]\n"
      "buffer_load_dwordx4 v[208:211], v1, s[16:19], s99 offen offset:1024 sc1\n"
      "v_mfma_f32_16x16x32_bf16 v[88:91], v[220:223], v[136:139], v[88:91]\n"
      "v_mfma_f32_16x16x32_bf16 v[92:95], v[224:227], v[136:139], v[92:95]\n"
      "ds_read_b128 v[136:139], v2 offset:31744\n"
      "s_waitcnt lgkmcnt(3)\n"
      "v_mfma_f32_16x16x32_bf16 v[96:99], v[212:215], v[140:143], v[96:99]\n"
      "v_mfma_f32_16x16x32_bf16 v[100:103], v[216:219], v[140:143], v[100:103]\n"
      "buffer_load_dwordx4 v[212:215], v1, s[16:19], s99 offen offset:2048 sc1\n"
      "v_mfma_f32_16x16x32_bf16 v[104:107], v[220:223], v[140:143], v[104:107]\n"
      "v_mfma_f32_16x16x32_bf16 v[108:111], v[224:227], v[140:143], v[108:111]\n"
      "ds_read_b128 v[140:143], v2 offset:35840\n"
      "s_waitcnt lgkmcnt(3)\n"
      "v_mfma_f32_16x16x32_bf16 v[48:51], v[228:231], v[128:131], v[48:51]\n"
      "v_mfma_f32_16x16x32_bf16 v[52:55], v[232:235], v[128:131], v[52:55]\n"
      "buffer_load_dwordx4 v[216:219], v1, s[16:19], s99 offen offset:3072 sc1\n"
      "v_mfma_f32_16x16x32_bf16 v[56:59], v[236:239], v[128:131], v[56:59]\n"
      "v_mfma_f32_16x16x32_bf16 v[60:63], v[240:243], v[128:131], v[60:63]\n"
      "s_waitcnt lgkmcnt(2)\n"
      "v_mfma_f32_16x16x32_bf16 v[64:67], v[228:231], v[132:135], v[64:67]\n"
      "v_mfma_f32_16x16x32_bf16 v[68:71], v[232:235], v[132:135], v[68:71]\n"
      "s_add_u32 s99, s99, 4096\n"
      "buffer_load_dwordx4 v[220:223], v1, s[16:19], s99 offen sc1\n"
      "v_mfma_f32_16x16x32_bf16 v[72:75], v[236:239], v[132:135], v[72:75]\n"
      "v_mfma_f32_16x16x32_bf16 v[76:79], v[240:243], v[132:135], v[76:79]\n"
      "s_waitcnt lgkmcnt(1)\n"
      "v_mfma_f32_16x16x32_bf16 v[80:83], v[228:231], v[136:139], v[80:83]\n"
      "v_mfma_f32_16x16x32_bf16 v[84:87], v[232:235], v[136:139], v[84:87]\n"
      "buffer_load_dwordx4 v[224:227], v1, s[16:19], s99 offen offset:1024 sc1\n"
      "v_mfma_f32_16x16x32_bf16 v[88:91], v[236:239], v[136:139], v[88:91]\n"
      "v_mfma_f32_16x16x32_bf16 v[92:95], v[240:243], v[136:139], v[92:95]\n"
      "s_waitcnt lgkmcnt(0)\n"
      "v_mfma_f32_16x16x32_bf16 v[96:99], v[228:231], v[140:143], v[96:99]\n"
      "v_mfma_f32_16x16x32_bf16 v[100:103], v[232:235], v[140:143], v[100:103]\n"
      "buffer_load_dwordx4 v[228:231], v1, s[16:19], s99 offen offset:2048 sc1\n"
      "v_mfma_f32_16x16x32_bf16 v[104:107], v[236:239], v[140:143], v[104:107]\n"
      "v_mfma_f32_16x16x32_bf16 v[108:111], v[240:243], v[140:143], v[108:111]\n"
      "s_waitcnt lgkmcnt(0)\n"
      "s_barrier\n"
      "buffer_load_dwordx4 v[232:235], v1, s[16:19], s99 offen offset:3072 sc1\n"
      "ds_read_b64 v[120:121], v25 offset:768\n"
      "ds_read_b32 v148, v25 offset:772\n"
      "ds_read_b64 v[122:123], v25 offset:896\n"
      "ds_read_b32 v150, v25 offset:900\n"
      "ds_read_b64 v[124:125], v25 offset:1024\n"
      "s_add_u32 s99, s99, 4096\n"
      "buffer_load_dwordx4 v[236:239], v1, s[16:19], s99 offen sc1\n"
      "ds_read_b32 v152, v25 offset:1028\n"
      "ds_read_b64 v[126:127], v25 offset:1152\n"
      "ds_read_b32 v154, v25 offset:1156\n"
      "ds_read_b128 v[112:115], v24 offset:128\n"
      "ds_read_b128 v[116:119], v24 offset:192\n"
      "s_add_u32 s99, s60, 15949824\n"
      "buffer_load_dwordx4 v[240:243], v1, s[16:19], s99 offen sc1\n"
      "s_add_u32 s80, s61, 19488768\n"
      "s_add_u32 s81, s61, 19544064\n"
      "s_waitcnt lgkmcnt(1)\n"
      "v_pk_fma_f32 v[48:49], v[112:113], v[148:149], v[48:49] op_sel_hi:[1,0,1]\n"
      "v_pk_fma_f32 v[50:51], v[114:115], v[148:149], v[50:51] op_sel_hi:[1,0,1]\n"
      "v_pk_fma_f32 v[64:65], v[112:113], v[150:151], v[64:65] op_sel_hi:[1,0,1]\n"
      "v_pk_fma_f32 v[66:67], v[114:115], v[150:151], v[66:67] op_sel_hi:[1,0,1]\n"
      "v_pk_fma_f32 v[80:81], v[112:113], v[152:153], v[80:81] op_sel_hi:[1,0,1]\n"
      "v_pk_fma_f32 v[82:83], v[114:115], v[152:153], v[82:83] op_sel_hi:[1,0,1]\n"
      "v_pk_fma_f32 v[96:97], v[112:113], v[154:155], v[96:97] op_sel_hi:[1,0,1]\n"
      "v_pk_fma_f32 v[98:99], v[114:115], v[154:155], v[98:99] op_sel_hi:[1,0,1]\n"
      "s_waitcnt lgkmcnt(0)\n"
      "v_pk_fma_f32 v[52:53], v[116:117], v[148:149], v[52:53] op_sel_hi:[1,0,1]\n"
      "v_pk_fma_f32 v[54:55], v[118:119], v[148:149], v[54:55] op_sel_hi:[1,0,1]\n"
      "v_pk_fma_f32 v[68:69], v[116:117], v[150:151], v[68:69] op_sel_hi:[1,0,1]\n"
      "v_pk_fma_f32 v[70:71], v[118:119], v[150:151], v[70:71] op_sel_hi:[1,0,1]\n"
      "v_pk_fma_f32 v[84:85], v[116:117], v[152:153], v[84:85] op_sel_hi:[1,0,1]\n"
      "v_pk_fma_f32 v[86:87], v[118:119], v[152:153], v[86:87] op_sel_hi:[1,0,1]\n"
      "v_pk_fma_f32 v[100:101], v[116:117], v[154:155], v[100:101] op_sel_hi:[1,0,1]\n"
      "v_pk_fma_f32 v[102:103], v[118:119], v[154:155], v[102:103] op_sel_hi:[1,0,1]\n"
      "ds_read_b128 v[112:115], v24 offset:256\n"
      "ds_read_b128 v[116:119], v24 offset:320\n"
      "v_pk_mul_f32 v[48:49], v[48:49], v[52:53]\n"
      "v_pk_mul_f32 v[50:51], v[50:51], v[54:55]\n"
      "v_pk_mul_f32 v[64:65], v[64:65], v[68:69]\n"
      "v_pk_mul_f32 v[66:67], v[66:67], v[70:71]\n"
      "v_pk_mul_f32 v[48:49], v[48:49], v[120:121] op_sel_hi:[1,0]\n"
      "v_pk_mul_f32 v[50:51], v[50:51], v[120:121] op_sel_hi:[1,0]\n"
      "v_pk_mul_f32 v[80:81], v[80:81], v[84:85]\n"
      "v_pk_mul_f32 v[82:83], v[82:83], v[86:87]\n"
      "v_pk_mul_f32 v[64:65], v[64:65], v[122:123] op_sel_hi:[1,0]\n"
      "v_pk_mul_f32 v[66:67], v[66:67], v[122:123] op_sel_hi:[1,0]\n"
      "v_cvt_pk_bf16_f32 v52, v48, v49\n"
      "v_cvt_pk_bf16_f32 v53, v50, v51\n"
      "v_pk_mul_f32 v[96:97], v[96:97], v[100:101]\n"
      "v_pk_mul_f32 v[98:99], v[98:99], v[102:103]\n"
      "v_pk_mul_f32 v[80:81], v[80:81], v[124:125] op_sel_hi:[1,0]\n"
      "v_pk_mul_f32 v[82:83], v[82:83], v[124:125] op_sel_hi:[1,0]\n"
      "v_cvt_pk_bf16_f32 v68, v64, v65\n"
      "v_cvt_pk_bf16_f32 v69, v66, v67\n"
      "v_pk_mul_f32 v[96:97], v[96:97], v[126:127] op_sel_hi:[1,0]\n"
      "v_pk_mul_f32 v[98:99], v[98:99], v[126:127] op_sel_hi:[1,0]\n"
      "v_cvt_pk_bf16_f32 v84, v80, v81\n"
      "v_cvt_pk_bf16_f32 v85, v82, v83\n"
      "v_cvt_pk_bf16_f32 v100, v96, v97\n"
      "v_cvt_pk_bf16_f32 v101, v98, v99\n"
      "s_waitcnt lgkmcnt(1)\n"
      "v_pk_fma_f32 v[56:57], v[112:113], v[148:149], v[56:57] op_sel_hi:[1,0,1]\n"
      "v_pk_fma_f32 v[58:59], v[114:115], v[148:149], v[58:59] op_sel_hi:[1,0,1]\n"
      "s_waitcnt lgkmcnt(0)\n"
      "v_pk_fma_f32 v[60:61], v[116:117], v[148:149], v[60:61] op_sel_hi:[1,0,1]\n"
      "v_pk_fma_f32 v[62:63], v[118:119], v[148:149], v[62:63] op_sel_hi:[1,0,1]\n"
      "v_pk_fma_f32 v[72:73], v[112:113], v[150:151], v[72:73] op_sel_hi:[1,0,1]\n"
      "v_pk_fma_f32 v[74:75], v[114:115], v[150:151], v[74:75] op_sel_hi:[1,0,1]\n"
      "v_pk_fma_f32 v[76:77], v[116:117], v[150:151], v[76:77] op_sel_hi:[1,0,1]\n"
      "v_pk_fma_f32 v[78:79], v[118:119], v[150:151], v[78:79] op_sel_hi:[1,0,1]\n"
      "v_pk_mul_f32 v[56:57], v[56:57], v[60:61]\n"
      "v_pk_mul_f32 v[58:59], v[58:59], v[62:63]\n"
      "v_pk_fma_f32 v[88:89], v[112:113], v[152:153], v[88:89] op_sel_hi:[1,0,1]\n"
      "v_pk_fma_f32 v[90:91], v[114:115], v[152:153], v[90:91] op_sel_hi:[1,0,1]\n"
      "v_pk_fma_f32 v[92:93], v[116:117], v[152:153], v[92:93] op_sel_hi:[1,0,1]\n"
      "v_pk_fma_f32 v[94:95], v[118:119], v[152:153], v[94:95] op_sel_hi:[1,0,1]\n"
      "v_pk_mul_f32 v[72:73], v[72:73], v[76:77]\n"
      "v_pk_mul_f32 v[74:75], v[74:75], v[78:79]\n"
      "v_pk_mul_f32 v[56:57], v[56:57], v[120:121] op_sel_hi:[1,0]\n"
      "v_pk_mul_f32 v[58:59], v[58:59], v[120:121] op_sel_hi:[1,0]\n"
      "v_pk_fma_f32 v[104:105], v[112:113], v[154:155], v[104:105] op_sel_hi:[1,0,1]\n"
      "v_pk_fma_f32 v[106:107], v[114:115], v[154:155], v[106:107] op_sel_hi:[1,0,1]\n"
      "v_pk_fma_f32 v[108:109], v[116:117], v[154:155], v[108:109] op_sel_hi:[1,0,1]\n"
      "v_pk_fma_f32 v[110:111], v[118:119], v[154:155], v[110:111] op_sel_hi:[1,0,1]\n"
      "v_pk_mul_f32 v[88:89], v[88:89], v[92:93]\n"
      "v_pk_mul_f32 v[90:91], v[90:91], v[94:95]\n"
      "v_pk_mul_f32 v[72:73], v[72:73], v[122:123] op_sel_hi:[1,0]\n"
      "v_pk_mul_f32 v[74:75], v[74:75], v[122:123] op_sel_hi:[1,0]\n"
      "v_cvt_pk_bf16_f32 v54, v56, v57\n"
      "v_cvt_pk_bf16_f32 v55, v58, v59\n"
      "v_pk_mul_f32 v[104:105], v[104:105], v[108:109]\n"
      "v_pk_mul_f32 v[106:107], v[106:107], v[110:111]\n"
      "v_pk_mul_f32 v[88:89], v[88:89], v[124:125] op_sel_hi:[1,0]\n"
      "v_pk_mul_f32 v[90:91], v[90:91], v[124:125] op_sel_hi:[1,0]\n"
      "v_cvt_pk_bf16_f32 v70, v72, v73\n"
      "v_cvt_pk_bf16_f32 v71, v74, v75\n"
      "buffer_store_dwordx4 v[52:55], v1, s[16:19], s80 offen sc1\n"
      "v_pk_mul_f32 v[104:105], v[104:105], v[126:127] op_sel_hi:[1,0]\n"
      "v_pk_mul_f32 v[106:107], v[106:107], v[126:127] op_sel_hi:[1,0]\n"
      "v_cvt_pk_bf16_f32 v86, v88, v89\n"
      "v_cvt_pk_bf16_f32 v87, v90, v91\n"
      "buffer_store_dwordx4 v[68:71], v1, s[16:19], s81 offen sc1\n"
      "s_add_u32 s80, s61, 19599360\n"
      "v_cvt_pk_bf16_f32 v102, v104, v105\n"
      "v_cvt_pk_bf16_f32 v103, v106, v107\n"
      "buffer_store_dwordx4 v[84:87], v1, s[16:19], s80 offen sc1\n"
      "s_add_u32 s81, s61, 19654656\n"
      "buffer_store_dwordx4 v[100:103], v1, s[16:19], s81 offen sc1\n"
      "s_cmp_lg_u32 s38, 0\n"
      "s_cbranch_scc1 LAST\n"
      "; ==== D0 final=False\n"
      "s_waitcnt vmcnt(18)\n"
      "v_mfma_f32_16x16x32_bf16 v[128:131], v[244:247], v[188:191], 0\n"
      "v_mfma_f32_16x16x32_bf16 v[132:135], v[248:251], v[188:191], 0\n"
      "v_mfma_f32_16x16x32_bf16 v[136:139], v[252:255], v[188:191], 0\n"
      "v_mfma_f32_16x16x32_bf16 v[140:143], a[0:3], v[188:191], 0\n"
      "v_mfma_f32_16x16x32_bf16 v[144:147], a[4:7], v[188:191], 0\n"
      "s_waitcnt vmcnt(17)\n"
      "v_mfma_f32_16x16x32_bf16 v[128:131], a[8:11], v[192:195], v[128:131]\n"
      "s_add_u32 s86, s59, 16005120\n"
      "buffer_load_dwordx4 v[188:191], v1, s[16:19], s86 offen sc1\n"
      "v_mfma_f32_16x16x32_bf16 v[132:135], a[12:15], v[192:195], v[132:135]\n"
      "v_mfma_f32_16x16x32_bf16 v[136:139], a[16:19], v[192:195], v[136:139]\n"
      "v_mfma_f32_16x16x32_bf16 v[140:143], a[20:23], v[192:195], v[140:143]\n"
      "v_mfma_f32_16x16x32_bf16 v[144:147], a[24:27], v[192:195], v[144:147]\n"
      "s_waitcnt vmcnt(16)\n"
      "v_mfma_f32_16x16x32_bf16 v[128:131], a[28:31], v[196:199], v[128:131]\n"
      "buffer_load_dwordx4 v[192:195], v1, s[16:19], s86 offen offset:1024 sc1\n"
      "v_mfma_f32_16x16x32_bf16 v[132:135], a[32:35], v[196:199], v[132:135]\n"
      "v_mfma_f32_16x16x32_bf16 v[136:139], a[36:39], v[196:199], v[136:139]\n"
      "v_mfma_f32_16x16x32_bf16 v[140:143], a[40:43], v[196:199], v[140:143]\n"
      "v_mfma_f32_16x16x32_bf16 v[144:147], a[44:47], v[196:199], v[144:147]\n"
      "s_waitcnt vmcnt(16)\n"
      "v_mfma_f32_16x16x32_bf16 v[128:131], a[48:51], v[200:203], v[128:131]\n"
      "buffer_load_dwordx4 v[196:199], v1, s[16:19], s86 offen offset:2048 sc1\n"
      "v_mfma_f32_16x16x32_bf16 v[132:135], a[52:55], v[200:203], v[132:135]\n"
      "v_mfma_f32_16x16x32_bf16 v[136:139], a[56:59], v[200:203], v[136:139]\n"
      "v_mfma_f32_16x16x32_bf16 v[140:143], a[60:63], v[200:203], v[140:143]\n"
      "v_mfma_f32_16x16x32_bf16 v[144:147], a[64:67], v[200:203], v[144:147]\n"
      "s_waitcnt vmcnt(16)\n"
      "v_mfma_f32_16x16x32_bf16 v[128:131], a[68:71], v[204:207], v[128:131]\n"
      "buffer_load_dwordx4 v[200:203], v1, s[16:19], s86 offen offset:3072 sc1\n"
      "v_mfma_f32_16x16x32_bf16 v[132:135], a[72:75], v[204:207], v[132:135]\n"
      "v_mfma_f32_16x16x32_bf16 v[136:139], a[76:79], v[204:207], v[136:139]\n"
      "v_mfma_f32_16x16x32_bf16 v[140:143], a[80:83], v[204:207], v[140:143]\n"
      "v_mfma_f32_16x16x32_bf16 v[144:147], a[84:87], v[204:207], v[144:147]\n"
      "s_waitcnt vmcnt(16)\n"
      "v_mfma_f32_16x16x32_bf16 v[128:131], a[88:91], v[208:211], v[128:131]\n"
      "s_add_u32 s86, s86, 4096\n"
      "buffer_load_dwordx4 v[204:207], v1, s[16:19], s86 offen sc1\n"
      "v_mfma_f32_16x16x32_bf16 v[132:135], a[92:95], v[208:211], v[132:135]\n"
      "v_mfma_f32_16x16x32_bf16 v[136:139], a[96:99], v[208:211], v[136:139]\n"
      "v_mfma_f32_16x16x32_bf16 v[140:143], a[100:103], v[208:211], v[140:143]\n"
      "v_mfma_f32_16x16x32_bf16 v[144:147], a[104:107], v[208:211], v[144:147]\n"
      "s_waitcnt vmcnt(16)\n"
      "v_mfma_f32_16x16x32_bf16 v[128:131], a[108:111], v[212:215], v[128:131]\n"
      "buffer_load_dwordx4 v[208:211], v1, s[16:19], s86 offen offset:1024 sc1\n"
      "v_mfma_f32_16x16x32_bf16 v[132:135], a[112:115], v[212:215], v[132:135]\n"
      "v_mfma_f32_16x16x32_bf16 v[136:139], a[116:119], v[212:215], v[136:139]\n"
      "v_mfma_f32_16x16x32_bf16 v[140:143], a[120:123], v[212:215], v[140:143]\n"
      "v_mfma_f32_16x16x32_bf16 v[144:147], a[124:127], v[212:215], v[144:147]\n"
      "s_waitcnt vmcnt(16)\n"
      "v_mfma_f32_16x16x32_bf16 v[128:131], a[128:131], v[216:219], v[128:131]\n"
      "s_waitcnt vmcnt(6)\n"
      "s_barrier\n"
      "s_add_u32 s76, s67, 2772992\n"
      "buffer_store_dwordx2 v[28:29], v21, s[16:19], s76 offen sc1\n"
      "v_mfma_f32_16x16x32_bf16 v[132:135], a[132:135], v[216:219], v[132:135]\n"
      "v_mfma_f32_16x16x32_bf16 v[136:139], a[136:139], v[216:219], v[136:139]\n"
      "v_mfma_f32_16x16x32_bf16 v[140:143], a[140:143], v[216:219], v[140:143]\n"
      "v_mfma_f32_16x16x32_bf16 v[144:147], a[144:147], v[216:219], v[144:147]\n"
      "buffer_load_dwordx4 v[212:215], v1, s[16:19], s86 offen offset:2048 sc1\n"
      "v_mfma_f32_16x16x32_bf16 v[128:131], a[148:151], v[220:223], v[128:131]\n"
      "v_mfma_f32_16x16x32_bf16 v[132:135], a[152:155], v[220:223], v[132:135]\n"
      "v_mfma_f32_16x16x32_bf16 v[136:139], a[156:159], v[220:223], v[136:139]\n"
      "v_mfma_f32_16x16x32_bf16 v[140:143], a[160:163], v[220:223], v[140:143]\n"
      "buffer_load_dwordx4 v[216:219], v1, s[16:19], s86 offen offset:3072 sc1\n"
      "v_mfma_f32_16x16x32_bf16 v[144:147], a[164:167], v[220:223], v[144:147]\n"
      "v_mfma_f32_16x16x32_bf16 v[128:131], a[168:171], v[224:227], v[128:131]\n"
      "v_mfma_f32_16x16x32_bf16 v[132:135], a[172:175], v[224:227], v[132:135]\n"
      "v_mfma_f32_16x16x32_bf16 v[136:139], a[176:179], v[224:227], v[136:139]\n"
      "s_add_u32 s86, s86, 4096\n"
      "buffer_load_dwordx4 v[220:223], v1, s[16:19], s86 offen sc1\n"
      "v_mfma_f32_16x16x32_bf16 v[140:143], a[180:183], v[224:227], v[140:143]\n"
      "v_mfma_f32_16x16x32_bf16 v[144:147], a[184:187], v[224:227], v[144:147]\n"
      "v_mfma_f32_16x16x32_bf16 v[128:131], a[188:191], v[228:231], v[128:131]\n"
      "v_mfma_f32_16x16x32_bf16 v[132:135], a[192:195], v[228:231], v[132:135]\n"
      "buffer_load_dwordx4 v[224:227], v1, s[16:19], s86 offen offset:1024 sc1\n"
      "v_mfma_f32_16x16x32_bf16 v[136:139], a[196:199], v[228:231], v[136:139]\n"
      "v_mfma_f32_16x16x32_bf16 v[140:143], a[200:203], v[228:231], v[140:143]\n"
      "v_mfma_f32_16x16x32_bf16 v[144:147], a[204:207], v[228:231], v[144:147]\n"
      "v_mfma_f32_16x16x32_bf16 v[128:131], a[208:211], v[232:235], v[128:131]\n"
      "buffer_load_dwordx4 v[228:231], v1, s[16:19], s86 offen offset:2048 sc1\n"
      "v_mfma_f32_16x16x32_bf16 v[132:135], a[212:215], v[232:235], v[132:135]\n"
      "v_mfma_f32_16x16x32_bf16 v[136:139], a[216:219], v[232:235], v[136:139]\n"
      "v_mfma_f32_16x16x32_bf16 v[140:143], a[220:223], v[232:235], v[140:143]\n"
      "v_mfma_f32_16x16x32_bf16 v[144:147], a[224:227], v[232:235], v[144:147]\n"
      "v_mfma_f32_16x16x32_bf16 v[128:131], a[228:231], v[236:239], v[128:131]\n"
      "buffer_load_dwordx4 v[232:235], v1, s[16:19], s86 offen offset:3072 sc1\n"
      "v_mfma_f32_16x16x32_bf16 v[132:135], a[232:235], v[236:239], v[132:135]\n"
      "v_mfma_f32_16x16x32_bf16 v[136:139], a[236:239], v[236:239], v[136:139]\n"
      "v_mfma_f32_16x16x32_bf16 v[140:143], a[240:243], v[236:239], v[140:143]\n"
      "v_mfma_f32_16x16x32_bf16 v[144:147], a[244:247], v[236:239], v[144:147]\n"
      "v_mfma_f32_16x16x32_bf16 v[128:131], a[248:251], v[240:243], v[128:131]\n"
      "s_add_u32 s86, s86, 4096\n"
      "buffer_load_dwordx4 v[236:239], v1, s[16:19], s86 offen sc1\n"
      "v_mfma_f32_16x16x32_bf16 v[132:135], a[252:255], v[240:243], v[132:135]\n"
      "v_mfma_f32_16x16x32_bf16 v[136:139], v[176:179], v[240:243], v[136:139]\n"
      "v_mfma_f32_16x16x32_bf16 v[140:143], v[180:183], v[240:243], v[140:143]\n"
      "v_mfma_f32_16x16x32_bf16 v[144:147], v[184:187], v[240:243], v[144:147]\n"
      "s_waitcnt vmcnt(13)\n"
      "v_mfma_f32_16x16x32_bf16 v[148:151], v[244:247], v[188:191], 0\n"
      "s_add_u32 s86, s60, 16005120\n"
      "buffer_load_dwordx4 v[240:243], v1, s[16:19], s86 offen sc1\n"
      "v_mfma_f32_16x16x32_bf16 v[152:155], v[248:251], v[188:191], 0\n"
      "s_add_u32 s87, s66, 2772992\n"
      "buffer_load_dwordx2 v[30:31], v19, s[16:19], s87 offen sc1\n"
      "v_mfma_f32_16x16x32_bf16 v[156:159], v[252:255], v[188:191], 0\n"
      "v_mfma_f32_16x16x32_bf16 v[160:163], a[0:3], v[188:191], 0\n"
      "ds_write_b128 v13, v[128:131]\n"
      "v_mfma_f32_16x16x32_bf16 v[164:167], a[4:7], v[188:191], 0\n"
      "ds_write_b128 v13, v[132:135] offset:1152\n"
      "s_waitcnt vmcnt(14)\n"
      "v_mfma_f32_16x16x32_bf16 v[148:151], a[8:11], v[192:195], v[148:151]\n"
      "ds_write_b128 v13, v[136:139] offset:2304\n"
      "v_mfma_f32_16x16x32_bf16 v[152:155], a[12:15], v[192:195], v[152:155]\n"
      "ds_write_b128 v13, v[140:143] offset:3456\n"
      "v_mfma_f32_16x16x32_bf16 v[156:159], a[16:19], v[192:195], v[156:159]\n"
      "ds_write_b128 v13, v[144:147] offset:4608\n"
      "v_mfma_f32_16x16x32_bf16 v[160:163], a[20:23], v[192:195], v[160:163]\n"
      "v_mfma_f32_16x16x32_bf16 v[164:167], a[24:27], v[192:195], v[164:167]\n"
      "s_waitcnt vmcnt(13)\n"
      "v_mfma_f32_16x16x32_bf16 v[148:151], a[28:31], v[196:199], v[148:151]\n"
      "v_mfma_f32_16x16x32_bf16 v[152:155], a[32:35], v[196:199], v[152:155]\n"
      "v_mfma_f32_16x16x32_bf16 v[156:159], a[36:39], v[196:199], v[156:159]\n"
      "v_mfma_f32_16x16x32_bf16 v[160:163], a[40:43], v[196:199], v[160:163]\n"
      "v_mfma_f32_16x16x32_bf16 v[164:167], a[44:47], v[196:199], v[164:167]\n"
      "s_waitcnt vmcnt(12)\n"
      "v_mfma_f32_16x16x32_bf16 v[148:151], a[48:51], v[200:203], v[148:151]\n"
      "v_mfma_f32_16x16x32_bf16 v[152:155], a[52:55], v[200:203], v[152:155]\n"
      "v_mfma_f32_16x16x32_bf16 v[156:159], a[56:59], v[200:203], v[156:159]\n"
      "v_mfma_f32_16x16x32_bf16 v[160:163], a[60:63], v[200:203], v[160:163]\n"
      "v_mfma_f32_16x16x32_bf16 v[164:167], a[64:67], v[200:203], v[164:167]\n"
      "s_waitcnt vmcnt(11)\n"
      "v_mfma_f32_16x16x32_bf16 v[148:151], a[68:71], v[204:207], v[148:151]\n"
      "v_mfma_f32_16x16x32_bf16 v[152:155], a[72:75], v[204:207], v[152:155]\n"
      "v_mfma_f32_16x16x32_bf16 v[156:159], a[76:79], v[204:207], v[156:159]\n"
      "v_mfma_f32_16x16x32_bf16 v[160:163], a[80:83], v[204:207], v[160:163]\n"
      "v_mfma_f32_16x16x32_bf16 v[164:167], a[84:87], v[204:207], v[164:167]\n"
      "s_waitcnt vmcnt(10)\n"
      "v_mfma_f32_16x16x32_bf16 v[148:151], a[88:91], v[208:211], v[148:151]\n"
      "v_mfma_f32_16x16x32_bf16 v[152:155], a[92:95], v[208:211], v[152:155]\n"
      "v_mfma_f32_16x16x32_bf16 v[156:159], a[96:99], v[208:211], v[156:159]\n"
      "v_mfma_f32_16x16x32_bf16 v[160:163], a[100:103], v[208:211], v[160:163]\n"
      "v_mfma_f32_16x16x32_bf16 v[164:167], a[104:107], v[208:211], v[164:167]\n"
      "s_waitcnt vmcnt(8)\n"
      "v_mfma_f32_16x16x32_bf16 v[148:151], a[108:111], v[212:215], v[148:151]\n"
      "v_mfma_f32_16x16x32_bf16 v[152:155], a[112:115], v[212:215], v[152:155]\n"
      "v_mfma_f32_16x16x32_bf16 v[156:159], a[116:119], v[212:215], v[156:159]\n"
      "v_mfma_f32_16x16x32_bf16 v[160:163], a[120:123], v[212:215], v[160:163]\n"
      "v_mfma_f32_16x16x32_bf16 v[164:167], a[124:127], v[212:215], v[164:167]\n"
      "s_waitcnt vmcnt(7)\n"
      "v_mfma_f32_16x16x32_bf16 v[148:151], a[128:131], v[216:219], v[148:151]\n"
      "v_mfma_f32_16x16x32_bf16 v[152:155], a[132:135], v[216:219], v[152:155]\n"
      "v_mfma_f32_16x16x32_bf16 v[156:159], a[136:139], v[216:219], v[156:159]\n"
      "v_mfma_f32_16x16x32_bf16 v[160:163], a[140:143], v[216:219], v[160:163]\n"
      "v_mfma_f32_16x16x32_bf16 v[164:167], a[144:147], v[216:219], v[164:167]\n"
      "s_waitcnt vmcnt(6)\n"
      "v_mfma_f32_16x16x32_bf16 v[148:151], a[148:151], v[220:223], v[148:151]\n"
      "v_mfma_f32_16x16x32_bf16 v[152:155], a[152:155], v[220:223], v[152:155]\n"
      "v_mfma_f32_16x16x32_bf16 v[156:159], a[156:159], v[220:223], v[156:159]\n"
      "v_mfma_f32_16x16x32_bf16 v[160:163], a[160:163], v[220:223], v[160:163]\n"
      "v_mfma_f32_16x16x32_bf16 v[164:167], a[164:167], v[220:223], v[164:167]\n"
      "s_waitcnt vmcnt(5)\n"
      "v_mfma_f32_16x16x32_bf16 v[148:151], a[168:171], v[224:227], v[148:151]\n"
      "v_mfma_f32_16x16x32_bf16 v[152:155], a[172:175], v[224:227], v[152:155]\n"
      "v_mfma_f32_16x16x32_bf16 v[156:159], a[176:179], v[224:227], v[156:159]\n"
      "v_mfma_f32_16x16x32_bf16 v[160:163], a[180:183], v[224:227], v[160:163]\n"
      "v_mfma_f32_16x16x32_bf16 v[164:167], a[184:187], v[224:227], v[164:167]\n"
      "s_waitcnt vmcnt(4)\n"
      "v_mfma_f32_16x16x32_bf16 v[148:151], a[188:191], v[228:231], v[148:151]\n"
      "v_mfma_f32_16x16x32_bf16 v[152:155], a[192:195], v[228:231], v[152:155]\n"
      "v_mfma_f32_16x16x32_bf16 v[156:159], a[196:199], v[228:231], v[156:159]\n"
      "v_mfma_f32_16x16x32_bf16 v[160:163], a[200:203], v[228:231], v[160:163]\n"
      "v_mfma_f32_16x16x32_bf16 v[164:167], a[204:207], v[228:231], v[164:167]\n"
      "s_waitcnt vmcnt(0)\n"
      "v_cmp_eq_u64 vcc, s[36:37], v[30:31]\n"
      "s_nop 3\n"
      "s_cmp_eq_u64 vcc, -1\n"
      "s_cbranch_scc1 Lpd_17\n"
      "Lps_18:\n"
      "s_sleep 1\n"
      "buffer_load_dwordx2 v[30:31], v19, s[16:19], s87 offen sc1\n"
      "s_waitcnt vmcnt(0)\n"
      "v_cmp_eq_u64 vcc, s[36:37], v[30:31]\n"
      "s_nop 3\n"
      "s_cmp_eq_u64 vcc, -1\n"
      "s_cbranch_scc1 Lpd_17\n"
      "s_add_u32 s42, s42, 1\n"
      "s_cmp_lt_u32 s42, 0x40000\n"
      "s_cbranch_scc1 Lps_18\n"
      "Lpd_17:\n"
      "s_add_u32 s99, s59, 19488768\n"
      "buffer_load_dwordx4 v[188:191], v1, s[16:19], s99 offen sc1\n"
      "v_mfma_f32_16x16x32_bf16 v[148:151], a[208:211], v[232:235], v[148:151]\n"
      "v_mfma_f32_16x16x32_bf16 v[152:155], a[212:215], v[232:235], v[152:155]\n"
      "v_mfma_f32_16x16x32_bf16 v[156:159], a[216:219], v[232:235], v[156:159]\n"
      "v_mfma_f32_16x16x32_bf16 v[160:163], a[220:223], v[232:235], v[160:163]\n"
      "buffer_load_dwordx4 v[192:195], v1, s[16:19], s99 offen offset:1024 sc1\n"
      "v_mfma_f32_16x16x32_bf16 v[164:167], a[224:227], v[232:235], v[164:167]\n"
      "v_mfma_f32_16x16x32_bf16 v[148:151], a[228:231], v[236:239], v[148:151]\n"
      "v_mfma_f32_16x16x32_bf16 v[152:155], a[232:235], v[236:239], v[152:155]\n"
      "v_mfma_f32_16x16x32_bf16 v[156:159], a[236:239], v[236:239], v[156:159]\n"
      "buffer_load_dwordx4 v[196:199], v1, s[16:19], s99 offen offset:2048 sc1\n"
      "v_mfma_f32_16x16x32_bf16 v[160:163], a[240:243], v[236:239], v[160:163]\n"
      "v_mfma_f32_16x16x32_bf16 v[164:167], a[244:247], v[236:239], v[164:167]\n"
      "v_mfma_f32_16x16x32_bf16 v[148:151], a[248:251], v[240:243], v[148:151]\n"
      "v_mfma_f32_16x16x32_bf16 v[152:155], a[252:255], v[240:243], v[152:155]\n"
      "buffer_load_dwordx4 v[200:203], v1, s[16:19], s99 offen offset:3072 sc1\n"
      "v_mfma_f32_16x16x32_bf16 v[156:159], v[176:179], v[240:243], v[156:159]\n"
      "v_mfma_f32_16x16x32_bf16 v[160:163], v[180:183], v[240:243], v[160:163]\n"
      "v_mfma_f32_16x16x32_bf16 v[164:167], v[184:187], v[240:243], v[164:167]\n"
      "s_nop 2\n"
      "ds_write_b128 v13, v[148:151] offset:5760\n"
      "s_add_u32 s99, s99, 4096\n"
      "buffer_load_dwordx4 v[204:207], v1, s[16:19], s99 offen sc1\n"
      "ds_write_b128 v13, v[152:155] offset:6912\n"
      "ds_write_b128 v13, v[156:159] offset:8064\n"
      "buffer_load_dwordx4 v[208:211], v1, s[16:19], s99 offen offset:1024 sc1\n"
      "ds_write_b128 v13, v[160:163] offset:9216\n"
      "ds_write_b128 v13, v[164:167] offset:10368\n"
      "buffer_load_dwordx4 v[212:215], v1, s[16:19], s99 offen offset:2048 sc1\n"
      "s_waitcnt lgkmcnt(0)\n"
      "s_barrier\n"
      "buffer_load_dwordx4 v[216:219], v1, s[16:19], s99 offen offset:3072 sc1\n"
      "ds_read_b128 v[64:67], v14\n"
      "ds_read_b128 v[68:71], v14 offset:11520\n"
      "ds_read_b128 v[72:75], v14 offset:23040\n"
      "ds_read_b128 v[76:79], v14 offset:34560\n"
      "ds_read_b128 v[144:147], v14 offset:256\n"
      "s_add_u32 s99, s99, 4096\n"
      "buffer_load_dwordx4 v[220:223], v1, s[16:19], s99 offen sc1\n"
      "ds_read_b128 v[148:151], v14 offset:11776\n"
      "ds_read_b128 v[152:155], v14 offset:23296\n"
      "ds_read_b128 v[156:159], v14 offset:34816\n"
      "v_lshlrev_b32 v128, 16, v32\n"
      "v_and_b32 v129, 0xffff0000, v32\n"
      "v_lshlrev_b32 v130, 16, v33\n"
      "v_and_b32 v131, 0xffff0000, v33\n"
      "v_mov_b32 v132, 0\n"
      "buffer_load_dwordx4 v[224:227], v1, s[16:19], s99 offen offset:1024 sc1\n"
      "v_mov_b32 v133, 0\n"
      "v_mov_b32 v134, 0\n"
      "v_mov_b32 v135, 0\n"
      "s_add_u32 s80, s46, 2785280\n"
      "s_add_u32 s81, s47, 5406720\n"
      "s_waitcnt lgkmcnt(6)\n"
      "v_pk_add_f32 v[64:65], v[64:65], v[68:69]\n"
      "v_pk_add_f32 v[66:67], v[66:67], v[70:71]\n"
      "s_waitcnt lgkmcnt(4)\n"
      "v_pk_add_f32 v[72:73], v[72:73], v[76:77]\n"
      "v_pk_add_f32 v[74:75], v[74:75], v[78:79]\n"
      "v_pk_add_f32 v[48:49], v[64:65], v[72:73]\n"
      "v_pk_add_f32 v[50:51], v[66:67], v[74:75]\n"
      "ds_read_b128 v[64:67], v14 offset:512\n"
      "buffer_load_dwordx4 v[228:231], v1, s[16:19], s99 offen offset:2048 sc1\n"
      "ds_read_b128 v[68:71], v14 offset:12032\n"
      "s_waitcnt lgkmcnt(4)\n"
      "v_pk_add_f32 v[144:145], v[144:145], v[148:149]\n"
      "s_waitcnt lgkmcnt(2)\n"
      "v_pk_add_f32 v[152:153], v[152:153], v[156:157]\n"
      "v_pk_add_f32 v[146:147], v[146:147], v[150:151]\n"
      "v_pk_add_f32 v[154:155], v[154:155], v[158:159]\n"
      "ds_read_b128 v[72:75], v14 offset:23552\n"
      "ds_read_b128 v[76:79], v14 offset:35072\n"
      "v_pk_add_f32 v[52:53], v[144:145], v[152:153]\n"
      "buffer_load_dwordx4 v[232:235], v1, s[16:19], s99 offen offset:3072 sc1\n"
      "v_pk_add_f32 v[54:55], v[146:147], v[154:155]\n"
      "ds_read_b128 v[144:147], v14 offset:768\n"
      "ds_read_b128 v[148:151], v14 offset:12288\n"
      "ds_read_b128 v[152:155], v14 offset:23808\n"
      "ds_read_b128 v[156:159], v14 offset:35328\n"
      "v_pk_add_f32 v[48:49], v[48:49], v[128:129]\n"
      "s_add_u32 s99, s99, 4096\n"
      "buffer_load_dwordx4 v[236:239], v1, s[16:19], s99 offen sc1\n"
      "v_pk_add_f32 v[50:51], v[50:51], v[130:131]\n"
      "v_cvt_pk_bf16_f32 v32, v48, v49\n"
      "v_cvt_pk_bf16_f32 v33, v50, v51\n"
      "v_lshlrev_b32 v128, 16, v34\n"
      "v_and_b32 v129, 0xffff0000, v34\n"
      "v_lshlrev_b32 v130, 16, v35\n"
      "v_and_b32 v131, 0xffff0000, v35\n"
      "v_pk_add_f32 v[52:53], v[52:53], v[128:129]\n"
      "v_pk_add_f32 v[54:55], v[54:55], v[130:131]\n"
      "v_dot2_f32_bf16 v132, v32, s78, v132\n"
      "v_dot2_f32_bf16 v133, v32, v32, v133\n"
      "v_dot2_f32_bf16 v134, v33, s78, v134\n"
      "v_dot2_f32_bf16 v135, v33, v33, v135\n"
      "s_add_u32 s99, s60, 19488768\n"
      "buffer_load_dwordx4 v[240:243], v1, s[16:19], s99 offen sc1\n"
      "v_cvt_pk_bf16_f32 v34, v52, v53\n"
      "v_cvt_pk_bf16_f32 v35, v54, v55\n"
      "v_lshlrev_b32 v128, 16, v36\n"
      "v_and_b32 v129, 0xffff0000, v36\n"
      "v_lshlrev_b32 v130, 16, v37\n"
      "v_and_b32 v131, 0xffff0000, v37\n"
      "v_dot2_f32_bf16 v132, v34, s78, v132\n"
      "v_dot2_f32_bf16 v133, v34, v34, v133\n"
      "s_waitcnt lgkmcnt(6)\n"
      "v_pk_add_f32 v[64:65], v[64:65], v[68:69]\n"
      "v_pk_add_f32 v[66:67], v[66:67], v[70:71]\n"
      "v_dot2_f32_bf16 v134, v35, s78, v134\n"
      "v_dot2_f32_bf16 v135, v35, v35, v135\n"
      "buffer_store_dwordx4 v[32:35], v15, s[16:19], s80 offen sc1\n"
      "s_waitcnt lgkmcnt(4)\n"
      "v_pk_add_f32 v[72:73], v[72:73], v[76:77]\n"
      "v_pk_add_f32 v[74:75], v[74:75], v[78:79]\n"
      "v_pk_add_f32 v[56:57], v[64:65], v[72:73]\n"
      "v_pk_add_f32 v[58:59], v[66:67], v[74:75]\n"
      "s_waitcnt lgkmcnt(2)\n"
      "v_pk_add_f32 v[144:145], v[144:145], v[148:149]\n"
      "v_pk_add_f32 v[146:147], v[146:147], v[150:151]\n"
      "s_waitcnt lgkmcnt(0)\n"
      "v_pk_add_f32 v[152:153], v[152:153], v[156:157]\n"
      "v_pk_add_f32 v[154:155], v[154:155], v[158:159]\n"
      "v_pk_add_f32 v[56:57], v[56:57], v[128:129]\n"
      "v_pk_add_f32 v[58:59], v[58:59], v[130:131]\n"
      "v_pk_add_f32 v[60:61], v[144:145], v[152:153]\n"
      "v_pk_add_f32 v[62:63], v[146:147], v[154:155]\n"
      "v_cvt_pk_bf16_f32 v36, v56, v57\n"
      "v_cvt_pk_bf16_f32 v37, v58, v59\n"
      "v_lshlrev_b32 v128, 16, v38\n"
      "v_and_b32 v129, 0xffff0000, v38\n"
      "v_lshlrev_b32 v130, 16, v39\n"
      "v_and_b32 v131, 0xffff0000, v39\n"
      "v_pk_add_f32 v[60:61], v[60:61], v[128:129]\n"
      "v_pk_add_f32 v[62:63], v[62:63], v[130:131]\n"
      "v_dot2_f32_bf16 v132, v36, s78, v132\n"
      "v_dot2_f32_bf16 v133, v36, v36, v133\n"
      "v_dot2_f32_bf16 v134, v37, s78, v134\n"
      "v_dot2_f32_bf16 v135, v37, v37, v135\n"
      "v_cvt_pk_bf16_f32 v38, v60, v61\n"
      "v_cvt_pk_bf16_f32 v39, v62, v63\n"
      "v_dot2_f32_bf16 v132, v38, s78, v132\n"
      "v_dot2_f32_bf16 v133, v38, v38, v133\n"
      "v_dot2_f32_bf16 v134, v39, s78, v134\n"
      "v_dot2_f32_bf16 v135, v39, v39, v135\n"
      "buffer_store_dwordx4 v[36:39], v15, s[16:19], s80 offen offset:256 sc1\n"
      "s_nop 1\n"
      "v_pk_add_f32 v[132:133], v[132:133], v[134:135]\n"
      "v_cndmask_b32 v132, 0, v132, s[82:83]\n"
      "v_cndmask_b32 v133, 0, v133, s[82:83]\n"
      "s_nop 0\n"
      "v_add_f32_dpp v132, v132, v132 row_ror:8 row_mask:0xf bank_mask:0xf\n"
      "v_add_f32_dpp v133, v133, v133 row_ror:8 row_mask:0xf bank_mask:0xf\n"
      "v_mov_b32 v136, v132\n"
      "v_mov_b32 v137, v133\n"
      "s_nop 0\n"
      "v_permlane16_swap_b32 v136, v132\n"
      "v_permlane16_swap_b32 v137, v133\n"
      "v_add_f32 v132, v132, v136\n"
      "v_add_f32 v133, v133, v137\n"
      "v_mov_b32 v136, v132\n"
      "v_mov_b32 v137, v133\n"
      "s_nop 0\n"
      "v_permlane32_swap_b32 v136, v132\n"
      "v_permlane32_swap_b32 v137, v133\n"
      "v_add_f32 v132, v132, v136\n"
      "v_add_f32 v133, v133, v137\n"
      "buffer_store_dwordx2 v[132:133], v16, s[16:19], s81 offen sc1\n"
      "; ==== D1 final=False\n"
      "s_waitcnt vmcnt(16)\n"
      "v_mfma_f32_16x16x32_bf16 v[88:91], v[244:247], v[188:191], 0\n"
      "v_mfma_f32_16x16x32_bf16 v[92:95], v[248:251], v[188:191], 0\n"
      "v_mfma_f32_16x16x32_bf16 v[96:99], v[252:255], v[188:191], 0\n"
      "v_mfma_f32_16x16x32_bf16 v[100:103], a[0:3], v[188:191], 0\n"
      "v_mfma_f32_16x16x32_bf16 v[104:107], a[4:7], v[188:191], 0\n"
      "s_waitcnt vmcnt(15)\n"
      "v_mfma_f32_16x16x32_bf16 v[88:91], a[8:11], v[192:195], v[88:91]\n"
      "s_add_u32 s86, s59, 19544064\n"
      "buffer_load_dwordx4 v[188:191], v1, s[16:19], s86 offen sc1\n"
      "v_mfma_f32_16x16x32_bf16 v[92:95], a[12:15], v[192:195], v[92:95]\n"
      "v_mfma_f32_16x16x32_bf16 v[96:99], a[16:19], v[192:195], v[96:99]\n"
      "v_mfma_f32_16x16x32_bf16 v[100:103], a[20:23], v[192:195], v[100:103]\n"
      "v_mfma_f32_16x16x32_bf16 v[104:107], a[24:27], v[192:195], v[104:107]\n"
      "s_waitcnt vmcnt(15)\n"
      "v_mfma_f32_16x16x32_bf16 v[88:91], a[28:31], v[196:199], v[88:91]\n"
      "buffer_load_dwordx4 v[192:195], v1, s[16:19], s86 offen offset:1024 sc1\n"
      "v_mfma_f32_16x16x32_bf16 v[92:95], a[32:35], v[196:199], v[92:95]\n"
      "v_mfma_f32_16x16x32_bf16 v[96:99], a[36:39], v[196:199], v[96:99]\n"
      "v_mfma_f32_16x16x32_bf16 v[100:103], a[40:43], v[196:199], v[100:103]\n"
      "v_mfma_f32_16x16x32_bf16 v[104:107], a[44:47], v[196:199], v[104:107]\n"
      "s_waitcnt vmcnt(15)\n"
      "v_mfma_f32_16x16x32_bf16 v[88:91], a[48:51], v[200:203], v[88:91]\n"
      "buffer_load_dwordx4 v[196:199], v1, s[16:19], s86 offen offset:2048 sc1\n"
      "v_mfma_f32_16x16x32_bf16 v[92:95], a[52:55], v[200:203], v[92:95]\n"
      "v_mfma_f32_16x16x32_bf16 v[96:99], a[56:59], v[200:203], v[96:99]\n"
      "v_mfma_f32_16x16x32_bf16 v[100:103], a[60:63], v[200:203], v[100:103]\n"
      "v_mfma_f32_16x16x32_bf16 v[104:107], a[64:67], v[200:203], v[104:107]\n"
      "s_waitcnt vmcnt(15)\n"
      "v_mfma_f32_16x16x32_bf16 v[88:91], a[68:71], v[204:207], v[88:91]\n"
      "buffer_load_dwordx4 v[200:203], v1, s[16:19], s86 offen offset:3072 sc1\n"
      "v_mfma_f32_16x16x32_bf16 v[92:95], a[72:75], v[204:207], v[92:95]\n"
      "v_mfma_f32_16x16x32_bf16 v[96:99], a[76:79], v[204:207], v[96:99]\n"
      "v_mfma_f32_16x16x32_bf16 v[100:103], a[80:83], v[204:207], v[100:103]\n"
      "v_mfma_f32_16x16x32_bf16 v[104:107], a[84:87], v[204:207], v[104:107]\n"
      "s_waitcnt vmcnt(15)\n"
      "v_mfma_f32_16x16x32_bf16 v[88:91], a[88:91], v[208:211], v[88:91]\n"
      "s_add_u32 s86, s86, 4096\n"
      "buffer_load_dwordx4 v[204:207], v1, s[16:19], s86 offen sc1\n"
      "v_mfma_f32_16x16x32_bf16 v[92:95], a[92:95], v[208:211], v[92:95]\n"
      "v_mfma_f32_16x16x32_bf16 v[96:99], a[96:99], v[208:211], v[96:99]\n"
      "v_mfma_f32_16x16x32_bf16 v[100:103], a[100:103], v[208:211], v[100:103]\n"
      "v_mfma_f32_16x16x32_bf16 v[104:107], a[104:107], v[208:211], v[104:107]\n"
      "s_waitcnt vmcnt(15)\n"
      "v_mfma_f32_16x16x32_bf16 v[88:91], a[108:111], v[212:215], v[88:91]\n"
      "buffer_load_dwordx4 v[208:211], v1, s[16:19], s86 offen offset:1024 sc1\n"
      "v_mfma_f32_16x16x32_bf16 v[92:95], a[112:115], v[212:215], v[92:95]\n"
      "v_mfma_f32_16x16x32_bf16 v[96:99], a[116:119], v[212:215], v[96:99]\n"
      "v_mfma_f32_16x16x32_bf16 v[100:103], a[120:123], v[212:215], v[100:103]\n"
      "v_mfma_f32_16x16x32_bf16 v[104:107], a[124:127], v[212:215], v[104:107]\n"
      "s_waitcnt vmcnt(15)\n"
      "v_mfma_f32_16x16x32_bf16 v[88:91], a[128:131], v[216:219], v[88:91]\n"
      "s_waitcnt vmcnt(6)\n"
      "s_barrier\n"
      "s_add_u32 s76, s67, 5562368\n"
      "buffer_store_dwordx2 v[28:29], v21, s[16:19], s76 offen sc1\n"
      "v_mfma_f32_16x16x32_bf16 v[92:95], a[132:135], v[216:219], v[92:95]\n"
      "v_mfma_f32_16x16x32_bf16 v[96:99], a[136:139], v[216:219], v[96:99]\n"
      "v_mfma_f32_16x16x32_bf16 v[100:103], a[140:143], v[216:219], v[100:103]\n"
      "v_mfma_f32_16x16x32_bf16 v[104:107], a[144:147], v[216:219], v[104:107]\n"
      "buffer_load_dwordx4 v[212:215], v1, s[16:19], s86 offen offset:2048 sc1\n"
      "v_mfma_f32_16x16x32_bf16 v[88:91], a[148:151], v[220:223], v[88:91]\n"
      "v_mfma_f32_16x16x32_bf16 v[92:95], a[152:155], v[220:223], v[92:95]\n"
      "v_mfma_f32_16x16x32_bf16 v[96:99], a[156:159], v[220:223], v[96:99]\n"
      "v_mfma_f32_16x16x32_bf16 v[100:103], a[160:163], v[220:223], v[100:103]\n"
      "buffer_load_dwordx4 v[216:219], v1, s[16:19], s86 offen offset:3072 sc1\n"
      "v_mfma_f32_16x16x32_bf16 v[104:107], a[164:167], v[220:223], v[104:107]\n"
      "v_mfma_f32_16x16x32_bf16 v[88:91], a[168:171], v[224:227], v[88:91]\n"
      "v_mfma_f32_16x16x32_bf16 v[92:95], a[172:175], v[224:227], v[92:95]\n"
      "v_mfma_f32_16x16x32_bf16 v[96:99], a[176:179], v[224:227], v[96:99]\n"
      "s_add_u32 s86, s86, 4096\n"
      "buffer_load_dwordx4 v[220:223], v1, s[16:19], s86 offen sc1\n"
      "v_mfma_f32_16x16x32_bf16 v[100:103], a[180:183], v[224:227], v[100:103]\n"
      "v_mfma_f32_16x16x32_bf16 v[104:107], a[184:187], v[224:227], v[104:107]\n"
      "v_mfma_f32_16x16x32_bf16 v[88:91], a[188:191], v[228:231], v[88:91]\n"
      "v_mfma_f32_16x16x32_bf16 v[92:95], a[192:195], v[228:231], v[92:95]\n"
      "buffer_load_dwordx4 v[224:227], v1, s[16:19], s86 offen offset:1024 sc1\n"
      "v_mfma_f32_16x16x32_bf16 v[96:99], a[196:199], v[228:231], v[96:99]\n"
      "v_mfma_f32_16x16x32_bf16 v[100:103], a[200:203], v[228:231], v[100:103]\n"
      "v_mfma_f32_16x16x32_bf16 v[104:107], a[204:207], v[228:231], v[104:107]\n"
      "v_mfma_f32_16x16x32_bf16 v[88:91], a[208:211], v[232:235], v[88:91]\n"
      "buffer_load_dwordx4 v[228:231], v1, s[16:19], s86 offen offset:2048 sc1\n"
      "v_mfma_f32_16x16x32_bf16 v[92:95], a[212:215], v[232:235], v[92:95]\n"
      "v_mfma_f32_16x16x32_bf16 v[96:99], a[216:219], v[232:235], v[96:99]\n"
      "v_mfma_f32_16x16x32_bf16 v[100:103], a[220:223], v[232:235], v[100:103]\n"
      "v_mfma_f32_16x16x32_bf16 v[104:107], a[224:227], v[232:235], v[104:107]\n"
      "v_mfma_f32_16x16x32_bf16 v[88:91], a[228:231], v[236:239], v[88:91]\n"
      "buffer_load_dwordx4 v[232:235], v1, s[16:19], s86 offen offset:3072 sc1\n"
      "v_mfma_f32_16x16x32_bf16 v[92:95], a[232:235], v[236:239], v[92:95]\n"
      "v_mfma_f32_16x16x32_bf16 v[96:99], a[236:239], v[236:239], v[96:99]\n"
      "v_mfma_f32_16x16x32_bf16 v[100:103], a[240:243], v[236:239], v[100:103]\n"
      "v_mfma_f32_16x16x32_bf16 v[104:107], a[244:247], v[236:239], v[104:107]\n"
      "v_mfma_f32_16x16x32_bf16 v[88:91], a[248:251], v[240:243], v[88:91]\n"
      "s_add_u32 s86, s86, 4096\n"
      "buffer_load_dwordx4 v[236:239], v1, s[16:19], s86 offen sc1\n"
      "v_mfma_f32_16x16x32_bf16 v[92:95], a[252:255], v[240:243], v[92:95]\n"
      "v_mfma_f32_16x16x32_bf16 v[96:99], v[176:179], v[240:243], v[96:99]\n"
      "v_mfma_f32_16x16x32_bf16 v[100:103], v[180:183], v[240:243], v[100:103]\n"
      "v_mfma_f32_16x16x32_bf16 v[104:107], v[184:187], v[240:243], v[104:107]\n"
      "s_waitcnt vmcnt(13)\n"
      "v_mfma_f32_16x16x32_bf16 v[108:111], v[244:247], v[188:191], 0\n"
      "s_add_u32 s86, s60, 19544064\n"
      "buffer_load_dwordx4 v[240:243], v1, s[16:19], s86 offen sc1\n"
      "v_mfma_f32_16x16x32_bf16 v[112:115], v[248:251], v[188:191], 0\n"
      "v_mfma_f32_16x16x32_bf16 v[116:119], v[252:255], v[188:191], 0\n"
      "v_mfma_f32_16x16x32_bf16 v[120:123], a[0:3], v[188:191], 0\n"
      "ds_write_b128 v13, v[88:91]\n"
      "v_mfma_f32_16x16x32_bf16 v[124:127], a[4:7], v[188:191], 0\n"
      "s_mov_b32 s35, s68\n"
      "buffer_load_dwordx4 a[0:3], v1, s[16:19], s35 offen sc1\n"
      "ds_write_b128 v13, v[92:95] offset:1152\n"
      "s_waitcnt vmcnt(14)\n"
      "v_mfma_f32_16x16x32_bf16 v[108:111], a[8:11], v[192:195], v[108:111]\n"
      "ds_write_b128 v13, v[96:99] offset:2304\n"
      "v_mfma_f32_16x16x32_bf16 v[112:115], a[12:15], v[192:195], v[112:115]\n"
      "ds_write_b128 v13, v[100:103] offset:3456\n"
      "v_mfma_f32_16x16x32_bf16 v[116:119], a[16:19], v[192:195], v[116:119]\n"
      "ds_write_b128 v13, v[104:107] offset:4608\n"
      "v_mfma_f32_16x16x32_bf16 v[120:123], a[20:23], v[192:195], v[120:123]\n"
      "buffer_load_dwordx4 a[4:7], v1, s[16:19], s35 offen offset:1024 sc1\n"
      "v_mfma_f32_16x16x32_bf16 v[124:127], a[24:27], v[192:195], v[124:127]\n"
      "s_waitcnt vmcnt(14)\n"
      "v_mfma_f32_16x16x32_bf16 v[108:111], a[28:31], v[196:199], v[108:111]\n"
      "v_mfma_f32_16x16x32_bf16 v[112:115], a[32:35], v[196:199], v[112:115]\n"
      "v_mfma_f32_16x16x32_bf16 v[116:119], a[36:39], v[196:199], v[116:119]\n"
      "buffer_load_dwordx4 a[8:11], v1, s[16:19], s35 offen offset:2048 sc1\n"
      "v_mfma_f32_16x16x32_bf16 v[120:123], a[40:43], v[196:199], v[120:123]\n"
      "v_mfma_f32_16x16x32_bf16 v[124:127], a[44:47], v[196:199], v[124:127]\n"
      "s_waitcnt vmcnt(14)\n"
      "v_mfma_f32_16x16x32_bf16 v[108:111], a[48:51], v[200:203], v[108:111]\n"
      "v_mfma_f32_16x16x32_bf16 v[112:115], a[52:55], v[200:203], v[112:115]\n"
      "buffer_load_dwordx4 a[12:15], v1, s[16:19], s35 offen offset:3072 sc1\n"
      "v_mfma_f32_16x16x32_bf16 v[116:119], a[56:59], v[200:203], v[116:119]\n"
      "v_mfma_f32_16x16x32_bf16 v[120:123], a[60:63], v[200:203], v[120:123]\n"
      "v_mfma_f32_16x16x32_bf16 v[124:127], a[64:67], v[200:203], v[124:127]\n"
      "s_waitcnt vmcnt(14)\n"
      "v_mfma_f32_16x16x32_bf16 v[108:111], a[68:71], v[204:207], v[108:111]\n"
      "s_add_u32 s35, s35, 4096\n"
      "buffer_load_dwordx4 a[16:19], v1, s[16:19], s35 offen sc1\n"
      "v_mfma_f32_16x16x32_bf16 v[112:115], a[72:75], v[204:207], v[112:115]\n"
      "v_mfma_f32_16x16x32_bf16 v[116:119], a[76:79], v[204:207], v[116:119]\n"
      "v_mfma_f32_16x16x32_bf16 v[120:123], a[80:83], v[204:207], v[120:123]\n"
      "v_mfma_f32_16x16x32_bf16 v[124:127], a[84:87], v[204:207], v[124:127]\n"
      "buffer_load_dwordx4 a[20:23], v1, s[16:19], s35 offen offset:1024 sc1\n"
      "s_waitcnt vmcnt(15)\n"
      "v_mfma_f32_16x16x32_bf16 v[108:111], a[88:91], v[208:211], v[108:111]\n"
      "v_mfma_f32_16x16x32_bf16 v[112:115], a[92:95], v[208:211], v[112:115]\n"
      "v_mfma_f32_16x16x32_bf16 v[116:119], a[96:99], v[208:211], v[116:119]\n"
      "v_mfma_f32_16x16x32_bf16 v[120:123], a[100:103], v[208:211], v[120:123]\n"
      "buffer_load_dwordx4 a[24:27], v1, s[16:19], s35 offen offset:2048 sc1\n"
      "v_mfma_f32_16x16x32_bf16 v[124:127], a[104:107], v[208:211], v[124:127]\n"
      "s_waitcnt vmcnt(14)\n"
      "v_mfma_f32_16x16x32_bf16 v[108:111], a[108:111], v[212:215], v[108:111]\n"
      "v_mfma_f32_16x16x32_bf16 v[112:115], a[112:115], v[212:215], v[112:115]\n"
      "v_mfma_f32_16x16x32_bf16 v[116:119], a[116:119], v[212:215], v[116:119]\n"
      "buffer_load_dwordx4 a[28:31], v1, s[16:19], s35 offen offset:3072 sc1\n"
      "v_mfma_f32_16x16x32_bf16 v[120:123], a[120:123], v[212:215], v[120:123]\n"
      "v_mfma_f32_16x16x32_bf16 v[124:127], a[124:127], v[212:215], v[124:127]\n"
      "s_waitcnt vmcnt(14)\n"
      "v_mfma_f32_16x16x32_bf16 v[108:111], a[128:131], v[216:219], v[108:111]\n"
      "v_mfma_f32_16x16x32_bf16 v[112:115], a[132:135], v[216:219], v[112:115]\n"
      "s_add_u32 s35, s35, 4096\n"
      "buffer_load_dwordx4 a[32:35], v1, s[16:19], s35 offen sc1\n"
      "v_mfma_f32_16x16x32_bf16 v[116:119], a[136:139], v[216:219], v[116:119]\n"
      "v_mfma_f32_16x16x32_bf16 v[120:123], a[140:143], v[216:219], v[120:123]\n"
      "v_mfma_f32_16x16x32_bf16 v[124:127], a[144:147], v[216:219], v[124:127]\n"
      "s_waitcnt vmcnt(14)\n"
      "v_mfma_f32_16x16x32_bf16 v[108:111], a[148:151], v[220:223], v[108:111]\n"
      "buffer_load_dwordx4 a[36:39], v1, s[16:19], s35 offen offset:1024 sc1\n"
      "v_mfma_f32_16x16x32_bf16 v[112:115], a[152:155], v[220:223], v[112:115]\n"
      "v_mfma_f32_16x16x32_bf16 v[116:119], a[156:159], v[220:223], v[116:119]\n"
      "v_mfma_f32_16x16x32_bf16 v[120:123], a[160:163], v[220:223], v[120:123]\n"
      "v_mfma_f32_16x16x32_bf16 v[124:127], a[164:167], v[220:223], v[124:127]\n"
      "buffer_load_dwordx4 a[40:43], v1, s[16:19], s35 offen offset:2048 sc1\n"
      "s_waitcnt vmcnt(15)\n"
      "v_mfma_f32_16x16x32_bf16 v[108:111], a[168:171], v[224:227], v[108:111]\n"
      "v_mfma_f32_16x16x32_bf16 v[112:115], a[172:175], v[224:227], v[112:115]\n"
      "v_mfma_f32_16x16x32_bf16 v[116:119], a[176:179], v[224:227], v[116:119]\n"
      "v_mfma_f32_16x16x32_bf16 v[120:123], a[180:183], v[224:227], v[120:123]\n"
      "buffer_load_dwordx4 a[44:47], v1, s[16:19], s35 offen offset:3072 sc1\n"
      "v_mfma_f32_16x16x32_bf16 v[124:127], a[184:187], v[224:227], v[124:127]\n"
      "s_waitcnt vmcnt(15)\n"
      "v_mfma_f32_16x16x32_bf16 v[108:111], a[188:191], v[228:231], v[108:111]\n"
      "v_mfma_f32_16x16x32_bf16 v[112:115], a[192:195], v[228:231], v[112:115]\n"
      "v_mfma_f32_16x16x32_bf16 v[116:119], a[196:199], v[228:231], v[116:119]\n"
      "s_add_u32 s35, s35, 4096\n"
      "buffer_load_dwordx4 a[48:51], v1, s[16:19], s35 offen sc1\n"
      "v_mfma_f32_16x16x32_bf16 v[120:123], a[200:203], v[228:231], v[120:123]\n"
      "v_mfma_f32_16x16x32_bf16 v[124:127], a[204:207], v[228:231], v[124:127]\n"
      "s_waitcnt vmcnt(15)\n"
      "v_mfma_f32_16x16x32_bf16 v[108:111], a[208:211], v[232:235], v[108:111]\n"
      "v_mfma_f32_16x16x32_bf16 v[112:115], a[212:215], v[232:235], v[112:115]\n"
      "buffer_load_dwordx4 a[52:55], v1, s[16:19], s35 offen offset:1024 sc1\n"
      "v_mfma_f32_16x16x32_bf16 v[116:119], a[216:219], v[232:235], v[116:119]\n"
      "v_mfma_f32_16x16x32_bf16 v[120:123], a[220:223], v[232:235], v[120:123]\n"
      "v_mfma_f32_16x16x32_bf16 v[124:127], a[224:227], v[232:235], v[124:127]\n"
      "s_waitcnt vmcnt(15)\n"
      "v_mfma_f32_16x16x32_bf16 v[108:111], a[228:231], v[236:239], v[108:111]\n"
      "s_add_u32 s87, s66, 5562368\n"
      "buffer_load_dwordx2 v[30:31], v20, s[16:19], s87 offen sc1\n"
      "v_mfma_f32_16x16x32_bf16 v[112:115], a[232:235], v[236:239], v[112:115]\n"
      "v_mfma_f32_16x16x32_bf16 v[116:119], a[236:239], v[236:239], v[116:119]\n"
      "v_mfma_f32_16x16x32_bf16 v[120:123], a[240:243], v[236:239], v[120:123]\n"
      "v_mfma_f32_16x16x32_bf16 v[124:127], a[244:247], v[236:239], v[124:127]\n"
      "buffer_load_dwordx4 a[56:59], v1, s[16:19], s35 offen offset:2048 sc1\n"
      "s_waitcnt vmcnt(16)\n"
      "v_mfma_f32_16x16x32_bf16 v[108:111], a[248:251], v[240:243], v[108:111]\n"
      "v_mfma_f32_16x16x32_bf16 v[112:115], a[252:255], v[240:243], v[112:115]\n"
      "v_mfma_f32_16x16x32_bf16 v[116:119], v[176:179], v[240:243], v[116:119]\n"
      "v_mfma_f32_16x16x32_bf16 v[120:123], v[180:183], v[240:243], v[120:123]\n"
      "buffer_load_dwordx4 a[60:63], v1, s[16:19], s35 offen offset:3072 sc1\n"
      "v_mfma_f32_16x16x32_bf16 v[124:127], v[184:187], v[240:243], v[124:127]\n"
      "s_nop 2\n"
      "ds_write_b128 v13, v[108:111] offset:5760\n"
      "ds_write_b128 v13, v[112:115] offset:6912\n"
      "s_add_u32 s35, s35, 4096\n"
      "buffer_load_dwordx4 a[64:67], v1, s[16:19], s35 offen sc1\n"
      "ds_write_b128 v13, v[116:119] offset:8064\n"
      "ds_write_b128 v13, v[120:123] offset:9216\n"
      "buffer_load_dwordx4 a[68:71], v1, s[16:19], s35 offen offset:1024 sc1\n"
      "ds_write_b128 v13, v[124:127] offset:10368\n"
      "s_waitcnt lgkmcnt(0)\n"
      "s_barrier\n"
      "buffer_load_dwordx4 a[72:75], v1, s[16:19], s35 offen offset:2048 sc1\n"
      "ds_read_b128 v[88:91], v14\n"
      "ds_read_b128 v[92:95], v14 offset:11520\n"
      "ds_read_b128 v[96:99], v14 offset:23040\n"
      "ds_read_b128 v[100:103], v14 offset:34560\n"
      "ds_read_b128 v[104:107], v14 offset:256\n"
      "buffer_load_dwordx4 a[76:79], v1, s[16:19], s35 offen offset:3072 sc1\n"
      "ds_read_b128 v[108:111], v14 offset:11776\n"
      "ds_read_b128 v[112:115], v14 offset:23296\n"
      "ds_read_b128 v[116:119], v14 offset:34816\n"
      "v_lshlrev_b32 v120, 16, v41\n"
      "v_and_b32 v121, 0xffff0000, v41\n"
      "v_mov_b32 v122, 0\n"
      "v_mov_b32 v123, 0\n"
      "v_mov_b32 v124, 0\n"
      "s_add_u32 s35, s35, 4096\n"
      "buffer_load_dwordx4 a[80:83], v1, s[16:19], s35 offen sc1\n"
      "v_mov_b32 v125, 0\n"
      "s_add_u32 s80, s46, 4096000\n"
      "s_add_u32 s81, s47, 5472256\n"
      "s_waitcnt lgkmcnt(6)\n"
      "v_pk_add_f32 v[88:89], v[88:89], v[92:93]\n"
      "v_pk_add_f32 v[90:91], v[90:91], v[94:95]\n"
      "s_waitcnt lgkmcnt(4)\n"
      "v_pk_add_f32 v[96:97], v[96:97], v[100:101]\n"
      "v_pk_add_f32 v[98:99], v[98:99], v[102:103]\n"
      "v_pk_add_f32 v[148:149], v[88:89], v[96:97]\n"
      "v_pk_add_f32 v[150:151], v[90:91], v[98:99]\n"
      "ds_read_b128 v[88:91], v14 offset:512\n"
      "s_waitcnt lgkmcnt(3)\n"
      "v_pk_add_f32 v[104:105], v[104:105], v[108:109]\n"
      "s_waitcnt lgkmcnt(1)\n"
      "v_pk_add_f32 v[112:113], v[112:113], v[116:117]\n"
      "buffer_load_dwordx4 a[84:87], v1, s[16:19], s35 offen offset:1024 sc1\n"
      "v_pk_add_f32 v[106:107], v[106:107], v[110:111]\n"
      "v_pk_add_f32 v[114:115], v[114:115], v[118:119]\n"
      "ds_read_b128 v[92:95], v14 offset:12032\n"
      "ds_read_b128 v[96:99], v14 offset:23552\n"
      "v_pk_add_f32 v[152:153], v[104:105], v[112:113]\n"
      "v_pk_add_f32 v[154:155], v[106:107], v[114:115]\n"
      "ds_read_b128 v[100:103], v14 offset:35072\n"
      "ds_read_b128 v[104:107], v14 offset:768\n"
      "buffer_load_dwordx4 a[88:91], v1, s[16:19], s35 offen offset:2048 sc1\n"
      "ds_read_b128 v[108:111], v14 offset:12288\n"
      "ds_read_b128 v[112:115], v14 offset:23808\n"
      "ds_read_b128 v[116:119], v14 offset:35328\n"
      "v_pk_add_f32 v[150:151], v[150:151], v[120:121]\n"
      "v_lshlrev_b32 v120, 16, v43\n"
      "v_cvt_pk_bf16_f32 v41, v150, v151\n"
      "v_and_b32 v121, 0xffff0000, v43\n"
      "v_dot2_f32_bf16 v124, v41, s78, v124\n"
      "buffer_load_dwordx4 a[92:95], v1, s[16:19], s35 offen offset:3072 sc1\n"
      "v_pk_add_f32 v[154:155], v[154:155], v[120:121]\n"
      "v_dot2_f32_bf16 v125, v41, v41, v125\n"
      "v_cvt_pk_bf16_f32 v43, v154, v155\n"
      "v_lshlrev_b32 v120, 16, v45\n"
      "v_and_b32 v121, 0xffff0000, v45\n"
      "v_dot2_f32_bf16 v124, v43, s78, v124\n"
      "v_dot2_f32_bf16 v125, v43, v43, v125\n"
      "s_waitcnt lgkmcnt(6)\n"
      "v_pk_add_f32 v[88:89], v[88:89], v[92:93]\n"
      "v_pk_add_f32 v[90:91], v[90:91], v[94:95]\n"
      "s_waitcnt lgkmcnt(4)\n"
      "v_pk_add_f32 v[96:97], v[96:97], v[100:101]\n"
      "v_pk_add_f32 v[98:99], v[98:99], v[102:103]\n"
      "v_pk_add_f32 v[156:157], v[88:89], v[96:97]\n"
      "v_pk_add_f32 v[158:159], v[90:91], v[98:99]\n"
      "s_waitcnt lgkmcnt(0)\n"
      "v_pk_add_f32 v[114:115], v[114:115], v[118:119]\n"
      "v_lshlrev_b32 v118, 16, v40\n"
      "v_and_b32 v119, 0xffff0000, v40\n"
      "v_pk_add_f32 v[104:105], v[104:105], v[108:109]\n"
      "v_pk_add_f32 v[148:149], v[148:149], v[118:119]\n"
      "v_lshlrev_b32 v118, 16, v42\n"
      "v_cvt_pk_bf16_f32 v40, v148, v149\n"
      "v_and_b32 v119, 0xffff0000, v42\n"
      "v_dot2_f32_bf16 v122, v40, s78, v122\n"
      "v_pk_add_f32 v[152:153], v[152:153], v[118:119]\n"
      "v_dot2_f32_bf16 v123, v40, v40, v123\n"
      "v_cvt_pk_bf16_f32 v42, v152, v153\n"
      "v_lshlrev_b32 v118, 16, v44\n"
      "v_and_b32 v119, 0xffff0000, v44\n"
      "v_pk_add_f32 v[112:113], v[112:113], v[116:117]\n"
      "v_pk_add_f32 v[106:107], v[106:107], v[110:111]\n"
      "v_pk_add_f32 v[156:157], v[156:157], v[118:119]\n"
      "v_pk_add_f32 v[158:159], v[158:159], v[120:121]\n"
      "v_dot2_f32_bf16 v122, v42, s78, v122\n"
      "v_dot2_f32_bf16 v123, v42, v42, v123\n"
      "v_pk_add_f32 v[160:161], v[104:105], v[112:113]\n"
      "v_pk_add_f32 v[162:163], v[106:107], v[114:115]\n"
      "v_cvt_pk_bf16_f32 v44, v156, v157\n"
      "v_cvt_pk_bf16_f32 v45, v158, v159\n"
      "v_lshlrev_b32 v118, 16, v46\n"
      "v_and_b32 v119, 0xffff0000, v46\n"
      "v_lshlrev_b32 v120, 16, v47\n"
      "v_and_b32 v121, 0xffff0000, v47\n"
      "v_pk_add_f32 v[160:161], v[160:161], v[118:119]\n"
      "v_pk_add_f32 v[162:163], v[162:163], v[120:121]\n"
      "v_dot2_f32_bf16 v122, v44, s78, v122\n"
      "v_dot2_f32_bf16 v123, v44, v44, v123\n"
      "v_dot2_f32_bf16 v124, v45, s78, v124\n"
      "v_dot2_f32_bf16 v125, v45, v45, v125\n"
      "v_cvt_pk_bf16_f32 v46, v160, v161\n"
      "v_cvt_pk_bf16_f32 v47, v162, v163\n"
      "v_dot2_f32_bf16 v122, v46, s78, v122\n"
      "v_dot2_f32_bf16 v123, v46, v46, v123\n"
      "v_dot2_f32_bf16 v124, v47, s78, v124\n"
      "v_dot2_f32_bf16 v125, v47, v47, v125\n"
      "buffer_store_dwordx4 v[40:43], v15, s[16:19], s80 offen sc1\n"
      "buffer_store_dwordx4 v[44:47], v15, s[16:19], s80 offen offset:256 sc1\n"
      "s_nop 0\n"
      "v_pk_add_f32 v[122:123], v[122:123], v[124:125]\n"
      "v_cndmask_b32 v122, 0, v122, s[82:83]\n"
      "v_cndmask_b32 v123, 0, v123, s[82:83]\n"
      "s_nop 0\n"
      "v_add_f32_dpp v122, v122, v122 row_ror:8 row_mask:0xf bank_mask:0xf\n"
      "v_add_f32_dpp v123, v123, v123 row_ror:8 row_mask:0xf bank_mask:0xf\n"
      "v_mov_b32 v126, v122\n"
      "v_mov_b32 v127, v123\n"
      "s_nop 0\n"
      "v_permlane16_swap_b32 v126, v122\n"
      "v_permlane16_swap_b32 v127, v123\n"
      "v_add_f32 v122, v122, v126\n"
      "v_add_f32 v123, v123, v127\n"
      "v_mov_b32 v126, v122\n"
      "v_mov_b32 v127, v123\n"
      "s_nop 0\n"
      "v_permlane32_swap_b32 v126, v122\n"
      "v_permlane32_swap_b32 v127, v123\n"
      "v_add_f32 v122, v122, v126\n"
      "v_add_f32 v123, v123, v127\n"
      "buffer_store_dwordx2 v[122:123], v16, s[16:19], s81 offen sc1\n"
      "s_waitcnt vmcnt(13)\n"
      "v_cmp_eq_u64 vcc, s[36:37], v[30:31]\n"
      "s_nop 3\n"
      "s_cmp_eq_u64 vcc, -1\n"
      "s_cbranch_scc1 Lpd_19\n"
      "Lps_20:\n"
      "s_sleep 1\n"
      "buffer_load_dwordx2 v[30:31], v20, s[16:19], s87 offen sc1\n"
      "s_waitcnt vmcnt(0)\n"
      "v_cmp_eq_u64 vcc, s[36:37], v[30:31]\n"
      "s_nop 3\n"
      "s_cmp_eq_u64 vcc, -1\n"
      "s_cbranch_scc1 Lpd_19\n"
      "s_add_u32 s42, s42, 1\n"
      "s_cmp_lt_u32 s42, 0x40000\n"
      "s_cbranch_scc1 Lps_20\n"
      "Lpd_19:\n"
      "s_add_u32 s98, s44, 2785280\n"
      "s_add_u32 m0, s74, 0\n"
      "s_nop 0\n"
      "buffer_load_dwordx4 v1, s[16:19], s98 offen sc1 lds\n"
      "buffer_load_dwordx4 v1, s[16:19], s98 offen offset:1024 sc1 lds\n"
      "buffer_load_dwordx4 v1, s[16:19], s98 offen offset:2048 sc1 lds\n"
      "buffer_load_dwordx4 v1, s[16:19], s98 offen offset:3072 sc1 lds\n"
      "s_add_u32 s98, s57, 2785280\n"
      "s_add_u32 m0, s75, 0\n"
      "s_nop 0\n"
      "buffer_load_dwordx4 v1, s[16:19], s98 offen sc1 lds\n"
      "s_add_u32 s98, s44, 2789376\n"
      "s_add_u32 m0, s74, 20480\n"
      "s_nop 0\n"
      "buffer_load_dwordx4 v1, s[16:19], s98 offen sc1 lds\n"
      "buffer_load_dwordx4 v1, s[16:19], s98 offen offset:1024 sc1 lds\n"
      "buffer_load_dwordx4 v1, s[16:19], s98 offen offset:2048 sc1 lds\n"
      "buffer_load_dwordx4 v1, s[16:19], s98 offen offset:3072 sc1 lds\n"
      "s_add_u32 s98, s57, 2789376\n"
      "s_add_u32 m0, s75, 20480\n"
      "s_nop 0\n"
      "buffer_load_dwordx4 v1, s[16:19], s98 offen sc1 lds\n"
      "s_add_u32 s44, s44, 2785280\n"
      "s_add_u32 s57, s57, 2785280\n"
      "s_add_u32 s45, s45, 2785280\n"
      "s_add_u32 s65, s65, 2785280\n"
      "s_add_u32 s46, s46, 2785280\n"
      "s_add_u32 s47, s47, 2785280\n"
      "s_add_u32 s66, s66, 2785280\n"
      "s_add_u32 s67, s67, 2785280\n"
      "s_mov_b32 s38, 256\n"
      "s_branch LOOP\n"
      "LAST:\n"
      "; ==== D0 final=True\n"
      "s_waitcnt vmcnt(18)\n"
      "v_mfma_f32_16x16x32_bf16 v[128:131], v[244:247], v[188:191], 0\n"
      "v_mfma_f32_16x16x32_bf16 v[132:135], v[248:251], v[188:191], 0\n"
      "v_mfma_f32_16x16x32_bf16 v[136:139], v[252:255], v[188:191], 0\n"
      "v_mfma_f32_16x16x32_bf16 v[140:143], a[0:3], v[188:191], 0\n"
      "v_mfma_f32_16x16x32_bf16 v[144:147], a[4:7], v[188:191], 0\n"
      "s_waitcnt vmcnt(17)\n"
      "v_mfma_f32_16x16x32_bf16 v[128:131], a[8:11], v[192:195], v[128:131]\n"
      "s_add_u32 s86, s59, 16005120\n"
      "buffer_load_dwordx4 v[188:191], v1, s[16:19], s86 offen sc1\n"
      "v_mfma_f32_16x16x32_bf16 v[132:135], a[12:15], v[192:195], v[132:135]\n"
      "v_mfma_f32_16x16x32_bf16 v[136:139], a[16:19], v[192:195], v[136:139]\n"
      "v_mfma_f32_16x16x32_bf16 v[140:143], a[20:23], v[192:195], v[140:143]\n"
      "v_mfma_f32_16x16x32_bf16 v[144:147], a[24:27], v[192:195], v[144:147]\n"
      "s_waitcnt vmcnt(16)\n"
      "v_mfma_f32_16x16x32_bf16 v[128:131], a[28:31], v[196:199], v[128:131]\n"
      "buffer_load_dwordx4 v[192:195], v1, s[16:19], s86 offen offset:1024 sc1\n"
      "v_mfma_f32_16x16x32_bf16 v[132:135], a[32:35], v[196:199], v[132:135]\n"
      "v_mfma_f32_16x16x32_bf16 v[136:139], a[36:39], v[196:199], v[136:139]\n"
      "v_mfma_f32_16x16x32_bf16 v[140:143], a[40:43], v[196:199], v[140:143]\n"
      "v_mfma_f32_16x16x32_bf16 v[144:147], a[44:47], v[196:199], v[144:147]\n"
      "s_waitcnt vmcnt(16)\n"
      "v_mfma_f32_16x16x32_bf16 v[128:131], a[48:51], v[200:203], v[128:131]\n"
      "buffer_load_dwordx4 v[196:199], v1, s[16:19], s86 offen offset:2048 sc1\n"
      "v_mfma_f32_16x16x32_bf16 v[132:135], a[52:55], v[200:203], v[132:135]\n"
      "v_mfma_f32_16x16x32_bf16 v[136:139], a[56:59], v[200:203], v[136:139]\n"
      "v_mfma_f32_16x16x32_bf16 v[140:143], a[60:63], v[200:203], v[140:143]\n"
      "v_mfma_f32_16x16x32_bf16 v[144:147], a[64:67], v[200:203], v[144:147]\n"
      "s_waitcnt vmcnt(16)\n"
      "v_mfma_f32_16x16x32_bf16 v[128:131], a[68:71], v[204:207], v[128:131]\n"
      "buffer_load_dwordx4 v[200:203], v1, s[16:19], s86 offen offset:3072 sc1\n"
      "v_mfma_f32_16x16x32_bf16 v[132:135], a[72:75], v[204:207], v[132:135]\n"
      "v_mfma_f32_16x16x32_bf16 v[136:139], a[76:79], v[204:207], v[136:139]\n"
      "v_mfma_f32_16x16x32_bf16 v[140:143], a[80:83], v[204:207], v[140:143]\n"
      "v_mfma_f32_16x16x32_bf16 v[144:147], a[84:87], v[204:207], v[144:147]\n"
      "s_waitcnt vmcnt(16)\n"
      "v_mfma_f32_16x16x32_bf16 v[128:131], a[88:91], v[208:211], v[128:131]\n"
      "s_add_u32 s86, s86, 4096\n"
      "buffer_load_dwordx4 v[204:207], v1, s[16:19], s86 offen sc1\n"
      "v_mfma_f32_16x16x32_bf16 v[132:135], a[92:95], v[208:211], v[132:135]\n"
      "v_mfma_f32_16x16x32_bf16 v[136:139], a[96:99], v[208:211], v[136:139]\n"
      "v_mfma_f32_16x16x32_bf16 v[140:143], a[100:103], v[208:211], v[140:143]\n"
      "v_mfma_f32_16x16x32_bf16 v[144:147], a[104:107], v[208:211], v[144:147]\n"
      "s_waitcnt vmcnt(16)\n"
      "v_mfma_f32_16x16x32_bf16 v[128:131], a[108:111], v[212:215], v[128:131]\n"
      "buffer_load_dwordx4 v[208:211], v1, s[16:19], s86 offen offset:1024 sc1\n"
      "v_mfma_f32_16x16x32_bf16 v[132:135], a[112:115], v[212:215], v[132:135]\n"
      "v_mfma_f32_16x16x32_bf16 v[136:139], a[116:119], v[212:215], v[136:139]\n"
      "v_mfma_f32_16x16x32_bf16 v[140:143], a[120:123], v[212:215], v[140:143]\n"
      "v_mfma_f32_16x16x32_bf16 v[144:147], a[124:127], v[212:215], v[144:147]\n"
      "s_waitcnt vmcnt(16)\n"
      "v_mfma_f32_16x16x32_bf16 v[128:131], a[128:131], v[216:219], v[128:131]\n"
      "s_waitcnt vmcnt(6)\n"
      "s_barrier\n"
      "s_add_u32 s76, s67, 2772992\n"
      "buffer_store_dwordx2 v[28:29], v21, s[16:19], s76 offen sc1\n"
      "v_mfma_f32_16x16x32_bf16 v[132:135], a[132:135], v[216:219], v[132:135]\n"
      "v_mfma_f32_16x16x32_bf16 v[136:139], a[136:139], v[216:219], v[136:139]\n"
      "v_mfma_f32_16x16x32_bf16 v[140:143], a[140:143], v[216:219], v[140:143]\n"
      "v_mfma_f32_16x16x32_bf16 v[144:147], a[144:147], v[216:219], v[144:147]\n"
      "buffer_load_dwordx4 v[212:215], v1, s[16:19], s86 offen offset:2048 sc1\n"
      "v_mfma_f32_16x16x32_bf16 v[128:131], a[148:151], v[220:223], v[128:131]\n"
      "v_mfma_f32_16x16x32_bf16 v[132:135], a[152:155], v[220:223], v[132:135]\n"
      "v_mfma_f32_16x16x32_bf16 v[136:139], a[156:159], v[220:223], v[136:139]\n"
      "v_mfma_f32_16x16x32_bf16 v[140:143], a[160:163], v[220:223], v[140:143]\n"
      "buffer_load_dwordx4 v[216:219], v1, s[16:19], s86 offen offset:3072 sc1\n"
      "v_mfma_f32_16x16x32_bf16 v[144:147], a[164:167], v[220:223], v[144:147]\n"
      "v_mfma_f32_16x16x32_bf16 v[128:131], a[168:171], v[224:227], v[128:131]\n"
      "v_mfma_f32_16x16x32_bf16 v[132:135], a[172:175], v[224:227], v[132:135]\n"
      "v_mfma_f32_16x16x32_bf16 v[136:139], a[176:179], v[224:227], v[136:139]\n"
      "s_add_u32 s86, s86, 4096\n"
      "buffer_load_dwordx4 v[220:223], v1, s[16:19], s86 offen sc1\n"
      "v_mfma_f32_16x16x32_bf16 v[140:143], a[180:183], v[224:227], v[140:143]\n"
      "v_mfma_f32_16x16x32_bf16 v[144:147], a[184:187], v[224:227], v[144:147]\n"
      "v_mfma_f32_16x16x32_bf16 v[128:131], a[188:191], v[228:231], v[128:131]\n"
      "v_mfma_f32_16x16x32_bf16 v[132:135], a[192:195], v[228:231], v[132:135]\n"
      "buffer_load_dwordx4 v[224:227], v1, s[16:19], s86 offen offset:1024 sc1\n"
      "v_mfma_f32_16x16x32_bf16 v[136:139], a[196:199], v[228:231], v[136:139]\n"
      "v_mfma_f32_16x16x32_bf16 v[140:143], a[200:203], v[228:231], v[140:143]\n"
      "v_mfma_f32_16x16x32_bf16 v[144:147], a[204:207], v[228:231], v[144:147]\n"
      "v_mfma_f32_16x16x32_bf16 v[128:131], a[208:211], v[232:235], v[128:131]\n"
      "buffer_load_dwordx4 v[228:231], v1, s[16:19], s86 offen offset:2048 sc1\n"
      "v_mfma_f32_16x16x32_bf16 v[132:135], a[212:215], v[232:235], v[132:135]\n"
      "v_mfma_f32_16x16x32_bf16 v[136:139], a[216:219], v[232:235], v[136:139]\n"
      "v_mfma_f32_16x16x32_bf16 v[140:143], a[220:223], v[232:235], v[140:143]\n"
      "v_mfma_f32_16x16x32_bf16 v[144:147], a[224:227], v[232:235], v[144:147]\n"
      "v_mfma_f32_16x16x32_bf16 v[128:131], a[228:231], v[236:239], v[128:131]\n"
      "buffer_load_dwordx4 v[232:235], v1, s[16:19], s86 offen offset:3072 sc1\n"
      "v_mfma_f32_16x16x32_bf16 v[132:135], a[232:235], v[236:239], v[132:135]\n"
      "v_mfma_f32_16x16x32_bf16 v[136:139], a[236:239], v[236:239], v[136:139]\n"
      "v_mfma_f32_16x16x32_bf16 v[140:143], a[240:243], v[236:239], v[140:143]\n"
      "v_mfma_f32_16x16x32_bf16 v[144:147], a[244:247], v[236:239], v[144:147]\n"
      "v_mfma_f32_16x16x32_bf16 v[128:131], a[248:251], v[240:243], v[128:131]\n"
      "s_add_u32 s86, s86, 4096\n"
      "buffer_load_dwordx4 v[236:239], v1, s[16:19], s86 offen sc1\n"
      "v_mfma_f32_16x16x32_bf16 v[132:135], a[252:255], v[240:243], v[132:135]\n"
      "v_mfma_f32_16x16x32_bf16 v[136:139], v[176:179], v[240:243], v[136:139]\n"
      "v_mfma_f32_16x16x32_bf16 v[140:143], v[180:183], v[240:243], v[140:143]\n"
      "v_mfma_f32_16x16x32_bf16 v[144:147], v[184:187], v[240:243], v[144:147]\n"
      "s_waitcnt vmcnt(13)\n"
      "v_mfma_f32_16x16x32_bf16 v[148:151], v[244:247], v[188:191], 0\n"
      "s_add_u32 s86, s60, 16005120\n"
      "buffer_load_dwordx4 v[240:243], v1, s[16:19], s86 offen sc1\n"
      "v_mfma_f32_16x16x32_bf16 v[152:155], v[248:251], v[188:191], 0\n"
      "s_add_u32 s87, s66, 2772992\n"
      "buffer_load_dwordx2 v[30:31], v19, s[16:19], s87 offen sc1\n"
      "v_mfma_f32_16x16x32_bf16 v[156:159], v[252:255], v[188:191], 0\n"
      "v_mfma_f32_16x16x32_bf16 v[160:163], a[0:3], v[188:191], 0\n"
      "ds_write_b128 v13, v[128:131]\n"
      "v_mfma_f32_16x16x32_bf16 v[164:167], a[4:7], v[188:191], 0\n"
      "ds_write_b128 v13, v[132:135] offset:1152\n"
      "s_waitcnt vmcnt(14)\n"
      "v_mfma_f32_16x16x32_bf16 v[148:151], a[8:11], v[192:195], v[148:151]\n"
      "ds_write_b128 v13, v[136:139] offset:2304\n"
      "v_mfma_f32_16x16x32_bf16 v[152:155], a[12:15], v[192:195], v[152:155]\n"
      "ds_write_b128 v13, v[140:143] offset:3456\n"
      "v_mfma_f32_16x16x32_bf16 v[156:159], a[16:19], v[192:195], v[156:159]\n"
      "ds_write_b128 v13, v[144:147] offset:4608\n"
      "v_mfma_f32_16x16x32_bf16 v[160:163], a[20:23], v[192:195], v[160:163]\n"
      "v_mfma_f32_16x16x32_bf16 v[164:167], a[24:27], v[192:195], v[164:167]\n"
      "s_waitcnt vmcnt(13)\n"
      "v_mfma_f32_16x16x32_bf16 v[148:151], a[28:31], v[196:199], v[148:151]\n"
      "v_mfma_f32_16x16x32_bf16 v[152:155], a[32:35], v[196:199], v[152:155]\n"
      "v_mfma_f32_16x16x32_bf16 v[156:159], a[36:39], v[196:199], v[156:159]\n"
      "v_mfma_f32_16x16x32_bf16 v[160:163], a[40:43], v[196:199], v[160:163]\n"
      "v_mfma_f32_16x16x32_bf16 v[164:167], a[44:47], v[196:199], v[164:167]\n"
      "s_waitcnt vmcnt(12)\n"
      "v_mfma_f32_16x16x32_bf16 v[148:151], a[48:51], v[200:203], v[148:151]\n"
      "v_mfma_f32_16x16x32_bf16 v[152:155], a[52:55], v[200:203], v[152:155]\n"
      "v_mfma_f32_16x16x32_bf16 v[156:159], a[56:59], v[200:203], v[156:159]\n"
      "v_mfma_f32_16x16x32_bf16 v[160:163], a[60:63], v[200:203], v[160:163]\n"
      "v_mfma_f32_16x16x32_bf16 v[164:167], a[64:67], v[200:203], v[164:167]\n"
      "s_waitcnt vmcnt(11)\n"
      "v_mfma_f32_16x16x32_bf16 v[148:151], a[68:71], v[204:207], v[148:151]\n"
      "v_mfma_f32_16x16x32_bf16 v[152:155], a[72:75], v[204:207], v[152:155]\n"
      "v_mfma_f32_16x16x32_bf16 v[156:159], a[76:79], v[204:207], v[156:159]\n"
      "v_mfma_f32_16x16x32_bf16 v[160:163], a[80:83], v[204:207], v[160:163]\n"
      "v_mfma_f32_16x16x32_bf16 v[164:167], a[84:87], v[204:207], v[164:167]\n"
      "s_waitcnt vmcnt(10)\n"
      "v_mfma_f32_16x16x32_bf16 v[148:151], a[88:91], v[208:211], v[148:151]\n"
      "v_mfma_f32_16x16x32_bf16 v[152:155], a[92:95], v[208:211], v[152:155]\n"
      "v_mfma_f32_16x16x32_bf16 v[156:159], a[96:99], v[208:211], v[156:159]\n"
      "v_mfma_f32_16x16x32_bf16 v[160:163], a[100:103], v[208:211], v[160:163]\n"
      "v_mfma_f32_16x16x32_bf16 v[164:167], a[104:107], v[208:211], v[164:167]\n"
      "s_waitcnt vmcnt(8)\n"
      "v_mfma_f32_16x16x32_bf16 v[148:151], a[108:111], v[212:215], v[148:151]\n"
      "v_mfma_f32_16x16x32_bf16 v[152:155], a[112:115], v[212:215], v[152:155]\n"
      "v_mfma_f32_16x16x32_bf16 v[156:159], a[116:119], v[212:215], v[156:159]\n"
      "v_mfma_f32_16x16x32_bf16 v[160:163], a[120:123], v[212:215], v[160:163]\n"
      "v_mfma_f32_16x16x32_bf16 v[164:167], a[124:127], v[212:215], v[164:167]\n"
      "s_waitcnt vmcnt(7)\n"
      "v_mfma_f32_16x16x32_bf16 v[148:151], a[128:131], v[216:219], v[148:151]\n"
      "v_mfma_f32_16x16x32_bf16 v[152:155], a[132:135], v[216:219], v[152:155]\n"
      "v_mfma_f32_16x16x32_bf16 v[156:159], a[136:139], v[216:219], v[156:159]\n"
      "v_mfma_f32_16x16x32_bf16 v[160:163], a[140:143], v[216:219], v[160:163]\n"
      "v_mfma_f32_16x16x32_bf16 v[164:167], a[144:147], v[216:219], v[164:167]\n"
      "s_waitcnt vmcnt(6)\n"
      "v_mfma_f32_16x16x32_bf16 v[148:151], a[148:151], v[220:223], v[148:151]\n"
      "v_mfma_f32_16x16x32_bf16 v[152:155], a[152:155], v[220:223], v[152:155]\n"
      "v_mfma_f32_16x16x32_bf16 v[156:159], a[156:159], v[220:223], v[156:159]\n"
      "v_mfma_f32_16x16x32_bf16 v[160:163], a[160:163], v[220:223], v[160:163]\n"
      "v_mfma_f32_16x16x32_bf16 v[164:167], a[164:167], v[220:223], v[164:167]\n"
      "s_waitcnt vmcnt(5)\n"
      "v_mfma_f32_16x16x32_bf16 v[148:151], a[168:171], v[224:227], v[148:151]\n"
      "v_mfma_f32_16x16x32_bf16 v[152:155], a[172:175], v[224:227], v[152:155]\n"
      "v_mfma_f32_16x16x32_bf16 v[156:159], a[176:179], v[224:227], v[156:159]\n"
      "v_mfma_f32_16x16x32_bf16 v[160:163], a[180:183], v[224:227], v[160:163]\n"
      "v_mfma_f32_16x16x32_bf16 v[164:167], a[184:187], v[224:227], v[164:167]\n"
      "s_waitcnt vmcnt(4)\n"
      "v_mfma_f32_16x16x32_bf16 v[148:151], a[188:191], v[228:231], v[148:151]\n"
      "v_mfma_f32_16x16x32_bf16 v[152:155], a[192:195], v[228:231], v[152:155]\n"
      "v_mfma_f32_16x16x32_bf16 v[156:159], a[196:199], v[228:231], v[156:159]\n"
      "v_mfma_f32_16x16x32_bf16 v[160:163], a[200:203], v[228:231], v[160:163]\n"
      "v_mfma_f32_16x16x32_bf16 v[164:167], a[204:207], v[228:231], v[164:167]\n"
      "s_waitcnt vmcnt(0)\n"
      "v_cmp_eq_u64 vcc, s[36:37], v[30:31]\n"
      "s_nop 3\n"
      "s_cmp_eq_u64 vcc, -1\n"
      "s_cbranch_scc1 Lpd_21\n"
      "Lps_22:\n"
      "s_sleep 1\n"
      "buffer_load_dwordx2 v[30:31], v19, s[16:19], s87 offen sc1\n"
      "s_waitcnt vmcnt(0)\n"
      "v_cmp_eq_u64 vcc, s[36:37], v[30:31]\n"
      "s_nop 3\n"
      "s_cmp_eq_u64 vcc, -1\n"
      "s_cbranch_scc1 Lpd_21\n"
      "s_add_u32 s42, s42, 1\n"
      "s_cmp_lt_u32 s42, 0x40000\n"
      "s_cbranch_scc1 Lps_22\n"
      "Lpd_21:\n"
      "s_add_u32 s99, s59, 19488768\n"
      "buffer_load_dwordx4 v[188:191], v1, s[16:19], s99 offen sc1\n"
      "v_mfma_f32_16x16x32_bf16 v[148:151], a[208:211], v[232:235], v[148:151]\n"
      "v_mfma_f32_16x16x32_bf16 v[152:155], a[212:215], v[232:235], v[152:155]\n"
      "v_mfma_f32_16x16x32_bf16 v[156:159], a[216:219], v[232:235], v[156:159]\n"
      "v_mfma_f32_16x16x32_bf16 v[160:163], a[220:223], v[232:235], v[160:163]\n"
      "buffer_load_dwordx4 v[192:195], v1, s[16:19], s99 offen offset:1024 sc1\n"
      "v_mfma_f32_16x16x32_bf16 v[164:167], a[224:227], v[232:235], v[164:167]\n"
      "v_mfma_f32_16x16x32_bf16 v[148:151], a[228:231], v[236:239], v[148:151]\n"
      "v_mfma_f32_16x16x32_bf16 v[152:155], a[232:235], v[236:239], v[152:155]\n"
      "v_mfma_f32_16x16x32_bf16 v[156:159], a[236:239], v[236:239], v[156:159]\n"
      "buffer_load_dwordx4 v[196:199], v1, s[16:19], s99 offen offset:2048 sc1\n"
      "v_mfma_f32_16x16x32_bf16 v[160:163], a[240:243], v[236:239], v[160:163]\n"
      "v_mfma_f32_16x16x32_bf16 v[164:167], a[244:247], v[236:239], v[164:167]\n"
      "v_mfma_f32_16x16x32_bf16 v[148:151], a[248:251], v[240:243], v[148:151]\n"
      "v_mfma_f32_16x16x32_bf16 v[152:155], a[252:255], v[240:243], v[152:155]\n"
      "buffer_load_dwordx4 v[200:203], v1, s[16:19], s99 offen offset:3072 sc1\n"
      "v_mfma_f32_16x16x32_bf16 v[156:159], v[176:179], v[240:243], v[156:159]\n"
      "v_mfma_f32_16x16x32_bf16 v[160:163], v[180:183], v[240:243], v[160:163]\n"
      "v_mfma_f32_16x16x32_bf16 v[164:167], v[184:187], v[240:243], v[164:167]\n"
      "s_nop 2\n"
      "ds_write_b128 v13, v[148:151] offset:5760\n"
      "s_add_u32 s99, s99, 4096\n"
      "buffer_load_dwordx4 v[204:207], v1, s[16:19], s99 offen sc1\n"
      "ds_write_b128 v13, v[152:155] offset:6912\n"
      "ds_write_b128 v13, v[156:159] offset:8064\n"
      "buffer_load_dwordx4 v[208:211], v1, s[16:19], s99 offen offset:1024 sc1\n"
      "ds_write_b128 v13, v[160:163] offset:9216\n"
      "ds_write_b128 v13, v[164:167] offset:10368\n"
      "buffer_load_dwordx4 v[212:215], v1, s[16:19], s99 offen offset:2048 sc1\n"
      "s_waitcnt lgkmcnt(0)\n"
      "s_barrier\n"
      "buffer_load_dwordx4 v[216:219], v1, s[16:19], s99 offen offset:3072 sc1\n"
      "ds_read_b128 v[64:67], v14\n"
      "ds_read_b128 v[68:71], v14 offset:11520\n"
      "ds_read_b128 v[72:75], v14 offset:23040\n"
      "ds_read_b128 v[76:79], v14 offset:34560\n"
      "ds_read_b128 v[144:147], v14 offset:256\n"
      "s_add_u32 s99, s99, 4096\n"
      "buffer_load_dwordx4 v[220:223], v1, s[16:19], s99 offen sc1\n"
      "ds_read_b128 v[148:151], v14 offset:11776\n"
      "ds_read_b128 v[152:155], v14 offset:23296\n"
      "ds_read_b128 v[156:159], v14 offset:34816\n"
      "v_lshlrev_b32 v128, 16, v32\n"
      "v_and_b32 v129, 0xffff0000, v32\n"
      "v_lshlrev_b32 v130, 16, v33\n"
      "v_and_b32 v131, 0xffff0000, v33\n"
      "s_waitcnt lgkmcnt(6)\n"
      "v_pk_add_f32 v[64:65], v[64:65], v[68:69]\n"
      "buffer_load_dwordx4 v[224:227], v1, s[16:19], s99 offen offset:1024 sc1\n"
      "v_pk_add_f32 v[66:67], v[66:67], v[70:71]\n"
      "s_waitcnt lgkmcnt(4)\n"
      "v_pk_add_f32 v[72:73], v[72:73], v[76:77]\n"
      "v_pk_add_f32 v[74:75], v[74:75], v[78:79]\n"
      "v_pk_add_f32 v[48:49], v[64:65], v[72:73]\n"
      "v_pk_add_f32 v[50:51], v[66:67], v[74:75]\n"
      "ds_read_b128 v[64:67], v14 offset:512\n"
      "s_waitcnt lgkmcnt(3)\n"
      "v_pk_add_f32 v[144:145], v[144:145], v[148:149]\n"
      "s_waitcnt lgkmcnt(1)\n"
      "v_pk_add_f32 v[152:153], v[152:153], v[156:157]\n"
      "v_pk_add_f32 v[146:147], v[146:147], v[150:151]\n"
      "v_pk_add_f32 v[154:155], v[154:155], v[158:159]\n"
      "ds_read_b128 v[68:71], v14 offset:12032\n"
      "buffer_load_dwordx4 v[228:231], v1, s[16:19], s99 offen offset:2048 sc1\n"
      "ds_read_b128 v[72:75], v14 offset:23552\n"
      "v_pk_add_f32 v[52:53], v[144:145], v[152:153]\n"
      "v_pk_add_f32 v[54:55], v[146:147], v[154:155]\n"
      "ds_read_b128 v[76:79], v14 offset:35072\n"
      "ds_read_b128 v[144:147], v14 offset:768\n"
      "ds_read_b128 v[148:151], v14 offset:12288\n"
      "buffer_load_dwordx4 v[232:235], v1, s[16:19], s99 offen offset:3072 sc1\n"
      "ds_read_b128 v[152:155], v14 offset:23808\n"
      "ds_read_b128 v[156:159], v14 offset:35328\n"
      "v_pk_add_f32 v[48:49], v[48:49], v[128:129]\n"
      "v_pk_add_f32 v[50:51], v[50:51], v[130:131]\n"
      "v_lshlrev_b32 v128, 16, v34\n"
      "v_and_b32 v129, 0xffff0000, v34\n"
      "v_lshlrev_b32 v130, 16, v35\n"
      "v_and_b32 v131, 0xffff0000, v35\n"
      "v_pk_add_f32 v[52:53], v[52:53], v[128:129]\n"
      "v_pk_add_f32 v[54:55], v[54:55], v[130:131]\n"
      "s_add_u32 s99, s99, 4096\n"
      "buffer_load_dwordx4 v[236:239], v1, s[16:19], s99 offen sc1\n"
      "v_lshlrev_b32 v128, 16, v36\n"
      "v_and_b32 v129, 0xffff0000, v36\n"
      "v_lshlrev_b32 v130, 16, v37\n"
      "v_and_b32 v131, 0xffff0000, v37\n"
      "v_cvt_pk_bf16_f32 v32, v48, v49\n"
      "v_cvt_pk_bf16_f32 v33, v50, v51\n"
      "v_cvt_pk_bf16_f32 v34, v52, v53\n"
      "v_cvt_pk_bf16_f32 v35, v54, v55\n"
      "buffer_store_dwordx4 v[32:35], v18, s[24:27], 0 offen\n"
      "s_waitcnt lgkmcnt(6)\n"
      "v_pk_add_f32 v[64:65], v[64:65], v[68:69]\n"
      "v_pk_add_f32 v[66:67], v[66:67], v[70:71]\n"
      "s_waitcnt lgkmcnt(4)\n"
      "v_pk_add_f32 v[72:73], v[72:73], v[76:77]\n"
      "v_pk_add_f32 v[74:75], v[74:75], v[78:79]\n"
      "v_pk_add_f32 v[56:57], v[64:65], v[72:73]\n"
      "v_pk_add_f32 v[58:59], v[66:67], v[74:75]\n"
      "s_waitcnt lgkmcnt(2)\n"
      "v_pk_add_f32 v[144:145], v[144:145], v[148:149]\n"
      "s_waitcnt lgkmcnt(0)\n"
      "v_pk_add_f32 v[152:153], v[152:153], v[156:157]\n"
      "v_pk_add_f32 v[146:147], v[146:147], v[150:151]\n"
      "v_pk_add_f32 v[154:155], v[154:155], v[158:159]\n"
      "v_pk_add_f32 v[56:57], v[56:57], v[128:129]\n"
      "v_pk_add_f32 v[58:59], v[58:59], v[130:131]\n"
      "s_add_u32 s99, s60, 19488768\n"
      "buffer_load_dwordx4 v[240:243], v1, s[16:19], s99 offen sc1\n"
      "v_pk_add_f32 v[60:61], v[144:145], v[152:153]\n"
      "v_pk_add_f32 v[62:63], v[146:147], v[154:155]\n"
      "v_lshlrev_b32 v128, 16, v38\n"
      "v_and_b32 v129, 0xffff0000, v38\n"
      "v_lshlrev_b32 v130, 16, v39\n"
      "v_and_b32 v131, 0xffff0000, v39\n"
      "v_pk_add_f32 v[60:61], v[60:61], v[128:129]\n"
      "v_pk_add_f32 v[62:63], v[62:63], v[130:131]\n"
      "v_cvt_pk_bf16_f32 v36, v56, v57\n"
      "v_cvt_pk_bf16_f32 v37, v58, v59\n"
      "v_cvt_pk_bf16_f32 v38, v60, v61\n"
      "v_cvt_pk_bf16_f32 v39, v62, v63\n"
      "buffer_store_dwordx4 v[36:39], v18, s[24:27], 0 offen offset:16\n"
      "; ==== D1 final=True\n"
      "s_waitcnt vmcnt(15)\n"
      "v_mfma_f32_16x16x32_bf16 v[88:91], v[244:247], v[188:191], 0\n"
      "v_mfma_f32_16x16x32_bf16 v[92:95], v[248:251], v[188:191], 0\n"
      "v_mfma_f32_16x16x32_bf16 v[96:99], v[252:255], v[188:191], 0\n"
      "v_mfma_f32_16x16x32_bf16 v[100:103], a[0:3], v[188:191], 0\n"
      "v_mfma_f32_16x16x32_bf16 v[104:107], a[4:7], v[188:191], 0\n"
      "s_waitcnt vmcnt(14)\n"
      "v_mfma_f32_16x16x32_bf16 v[88:91], a[8:11], v[192:195], v[88:91]\n"
      "s_add_u32 s86, s59, 19544064\n"
      "buffer_load_dwordx4 v[188:191], v1, s[16:19], s86 offen sc1\n"
      "v_mfma_f32_16x16x32_bf16 v[92:95], a[12:15], v[192:195], v[92:95]\n"
      "v_mfma_f32_16x16x32_bf16 v[96:99], a[16:19], v[192:195], v[96:99]\n"
      "v_mfma_f32_16x16x32_bf16 v[100:103], a[20:23], v[192:195], v[100:103]\n"
      "v_mfma_f32_16x16x32_bf16 v[104:107], a[24:27], v[192:195], v[104:107]\n"
      "s_waitcnt vmcnt(14)\n"
      "v_mfma_f32_16x16x32_bf16 v[88:91], a[28:31], v[196:199], v[88:91]\n"
      "buffer_load_dwordx4 v[192:195], v1, s[16:19], s86 offen offset:1024 sc1\n"
      "v_mfma_f32_16x16x32_bf16 v[92:95], a[32:35], v[196:199], v[92:95]\n"
      "v_mfma_f32_16x16x32_bf16 v[96:99], a[36:39], v[196:199], v[96:99]\n"
      "v_mfma_f32_16x16x32_bf16 v[100:103], a[40:43], v[196:199], v[100:103]\n"
      "v_mfma_f32_16x16x32_bf16 v[104:107], a[44:47], v[196:199], v[104:107]\n"
      "s_waitcnt vmcnt(14)\n"
      "v_mfma_f32_16x16x32_bf16 v[88:91], a[48:51], v[200:203], v[88:91]\n"
      "buffer_load_dwordx4 v[196:199], v1, s[16:19], s86 offen offset:2048 sc1\n"
      "v_mfma_f32_16x16x32_bf16 v[92:95], a[52:55], v[200:203], v[92:95]\n"
      "v_mfma_f32_16x16x32_bf16 v[96:99], a[56:59], v[200:203], v[96:99]\n"
      "v_mfma_f32_16x16x32_bf16 v[100:103], a[60:63], v[200:203], v[100:103]\n"
      "v_mfma_f32_16x16x32_bf16 v[104:107], a[64:67], v[200:203], v[104:107]\n"
      "s_waitcnt vmcnt(14)\n"
      "v_mfma_f32_16x16x32_bf16 v[88:91], a[68:71], v[204:207], v[88:91]\n"
      "buffer_load_dwordx4 v[200:203], v1, s[16:19], s86 offen offset:3072 sc1\n"
      "v_mfma_f32_16x16x32_bf16 v[92:95], a[72:75], v[204:207], v[92:95]\n"
      "v_mfma_f32_16x16x32_bf16 v[96:99], a[76:79], v[204:207], v[96:99]\n"
      "v_mfma_f32_16x16x32_bf16 v[100:103], a[80:83], v[204:207], v[100:103]\n"
      "v_mfma_f32_16x16x32_bf16 v[104:107], a[84:87], v[204:207], v[104:107]\n"
      "s_waitcnt vmcnt(14)\n"
      "v_mfma_f32_16x16x32_bf16 v[88:91], a[88:91], v[208:211], v[88:91]\n"
      "s_add_u32 s86, s86, 4096\n"
      "buffer_load_dwordx4 v[204:207], v1, s[16:19], s86 offen sc1\n"
      "v_mfma_f32_16x16x32_bf16 v[92:95], a[92:95], v[208:211], v[92:95]\n"
      "v_mfma_f32_16x16x32_bf16 v[96:99], a[96:99], v[208:211], v[96:99]\n"
      "v_mfma_f32_16x16x32_bf16 v[100:103], a[100:103], v[208:211], v[100:103]\n"
      "v_mfma_f32_16x16x32_bf16 v[104:107], a[104:107], v[208:211], v[104:107]\n"
      "s_waitcnt vmcnt(14)\n"
      "v_mfma_f32_16x16x32_bf16 v[88:91], a[108:111], v[212:215], v[88:91]\n"
      "buffer_load_dwordx4 v[208:211], v1, s[16:19], s86 offen offset:1024 sc1\n"
      "v_mfma_f32_16x16x32_bf16 v[92:95], a[112:115], v[212:215], v[92:95]\n"
      "v_mfma_f32_16x16x32_bf16 v[96:99], a[116:119], v[212:215], v[96:99]\n"
      "v_mfma_f32_16x16x32_bf16 v[100:103], a[120:123], v[212:215], v[100:103]\n"
      "v_mfma_f32_16x16x32_bf16 v[104:107], a[124:127], v[212:215], v[104:107]\n"
      "s_waitcnt vmcnt(14)\n"
      "v_mfma_f32_16x16x32_bf16 v[88:91], a[128:131], v[216:219], v[88:91]\n"
      "buffer_load_dwordx4 v[212:215], v1, s[16:19], s86 offen offset:2048 sc1\n"
      "v_mfma_f32_16x16x32_bf16 v[92:95], a[132:135], v[216:219], v[92:95]\n"
      "v_mfma_f32_16x16x32_bf16 v[96:99], a[136:139], v[216:219], v[96:99]\n"
      "v_mfma_f32_16x16x32_bf16 v[100:103], a[140:143], v[216:219], v[100:103]\n"
      "v_mfma_f32_16x16x32_bf16 v[104:107], a[144:147], v[216:219], v[104:107]\n"
      "s_waitcnt vmcnt(14)\n"
      "v_mfma_f32_16x16x32_bf16 v[88:91], a[148:151], v[220:223], v[88:91]\n"
      "buffer_load_dwordx4 v[216:219], v1, s[16:19], s86 offen offset:3072 sc1\n"
      "v_mfma_f32_16x16x32_bf16 v[92:95], a[152:155], v[220:223], v[92:95]\n"
      "v_mfma_f32_16x16x32_bf16 v[96:99], a[156:159], v[220:223], v[96:99]\n"
      "v_mfma_f32_16x16x32_bf16 v[100:103], a[160:163], v[220:223], v[100:103]\n"
      "v_mfma_f32_16x16x32_bf16 v[104:107], a[164:167], v[220:223], v[104:107]\n"
      "s_waitcnt vmcnt(14)\n"
      "v_mfma_f32_16x16x32_bf16 v[88:91], a[168:171], v[224:227], v[88:91]\n"
      "s_add_u32 s86, s86, 4096\n"
      "buffer_load_dwordx4 v[220:223], v1, s[16:19], s86 offen sc1\n"
      "v_mfma_f32_16x16x32_bf16 v[92:95], a[172:175], v[224:227], v[92:95]\n"
      "v_mfma_f32_16x16x32_bf16 v[96:99], a[176:179], v[224:227], v[96:99]\n"
      "v_mfma_f32_16x16x32_bf16 v[100:103], a[180:183], v[224:227], v[100:103]\n"
      "v_mfma_f32_16x16x32_bf16 v[104:107], a[184:187], v[224:227], v[104:107]\n"
      "s_waitcnt vmcnt(14)\n"
      "v_mfma_f32_16x16x32_bf16 v[88:91], a[188:191], v[228:231], v[88:91]\n"
      "buffer_load_dwordx4 v[224:227], v1, s[16:19], s86 offen offset:1024 sc1\n"
      "v_mfma_f32_16x16x32_bf16 v[92:95], a[192:195], v[228:231], v[92:95]\n"
      "v_mfma_f32_16x16x32_bf16 v[96:99], a[196:199], v[228:231], v[96:99]\n"
      "v_mfma_f32_16x16x32_bf16 v[100:103], a[200:203], v[228:231], v[100:103]\n"
      "v_mfma_f32_16x16x32_bf16 v[104:107], a[204:207], v[228:231], v[104:107]\n"
      "s_waitcnt vmcnt(14)\n"
      "v_mfma_f32_16x16x32_bf16 v[88:91], a[208:211], v[232:235], v[88:91]\n"
      "buffer_load_dwordx4 v[228:231], v1, s[16:19], s86 offen offset:2048 sc1\n"
      "v_mfma_f32_16x16x32_bf16 v[92:95], a[212:215], v[232:235], v[92:95]\n"
      "v_mfma_f32_16x16x32_bf16 v[96:99], a[216:219], v[232:235], v[96:99]\n"
      "v_mfma_f32_16x16x32_bf16 v[100:103], a[220:223], v[232:235], v[100:103]\n"
      "v_mfma_f32_16x16x32_bf16 v[104:107], a[224:227], v[232:235], v[104:107]\n"
      "s_waitcnt vmcnt(14)\n"
      "v_mfma_f32_16x16x32_bf16 v[88:91], a[228:231], v[236:239], v[88:91]\n"
      "buffer_load_dwordx4 v[232:235], v1, s[16:19], s86 offen offset:3072 sc1\n"
      "v_mfma_f32_16x16x32_bf16 v[92:95], a[232:235], v[236:239], v[92:95]\n"
      "v_mfma_f32_16x16x32_bf16 v[96:99], a[236:239], v[236:239], v[96:99]\n"
      "v_mfma_f32_16x16x32_bf16 v[100:103], a[240:243], v[236:239], v[100:103]\n"
      "v_mfma_f32_16x16x32_bf16 v[104:107], a[244:247], v[236:239], v[104:107]\n"
      "s_waitcnt vmcnt(13)\n"
      "v_mfma_f32_16x16x32_bf16 v[88:91], a[248:251], v[240:243], v[88:91]\n"
      "s_add_u32 s86, s86, 4096\n"
      "buffer_load_dwordx4 v[236:239], v1, s[16:19], s86 offen sc1\n"
      "v_mfma_f32_16x16x32_bf16 v[92:95], a[252:255], v[240:243], v[92:95]\n"
      "v_mfma_f32_16x16x32_bf16 v[96:99], v[176:179], v[240:243], v[96:99]\n"
      "v_mfma_f32_16x16x32_bf16 v[100:103], v[180:183], v[240:243], v[100:103]\n"
      "v_mfma_f32_16x16x32_bf16 v[104:107], v[184:187], v[240:243], v[104:107]\n"
      "s_waitcnt vmcnt(12)\n"
      "v_mfma_f32_16x16x32_bf16 v[108:111], v[244:247], v[188:191], 0\n"
      "s_add_u32 s86, s60, 19544064\n"
      "buffer_load_dwordx4 v[240:243], v1, s[16:19], s86 offen sc1\n"
      "s_barrier\n"
      "v_mfma_f32_16x16x32_bf16 v[112:115], v[248:251], v[188:191], 0\n"
      "v_mfma_f32_16x16x32_bf16 v[116:119], v[252:255], v[188:191], 0\n"
      "v_mfma_f32_16x16x32_bf16 v[120:123], a[0:3], v[188:191], 0\n"
      "ds_write_b128 v13, v[88:91]\n"
      "v_mfma_f32_16x16x32_bf16 v[124:127], a[4:7], v[188:191], 0\n"
      "ds_write_b128 v13, v[92:95] offset:1152\n"
      "s_waitcnt vmcnt(12)\n"
      "v_mfma_f32_16x16x32_bf16 v[108:111], a[8:11], v[192:195], v[108:111]\n"
      "ds_write_b128 v13, v[96:99] offset:2304\n"
      "v_mfma_f32_16x16x32_bf16 v[112:115], a[12:15], v[192:195], v[112:115]\n"
      "ds_write_b128 v13, v[100:103] offset:3456\n"
      "v_mfma_f32_16x16x32_bf16 v[116:119], a[16:19], v[192:195], v[116:119]\n"
      "ds_write_b128 v13, v[104:107] offset:4608\n"
      "v_mfma_f32_16x16x32_bf16 v[120:123], a[20:23], v[192:195], v[120:123]\n"
      "v_mfma_f32_16x16x32_bf16 v[124:127], a[24:27], v[192:195], v[124:127]\n"
      "s_waitcnt vmcnt(11)\n"
      "v_mfma_f32_16x16x32_bf16 v[108:111], a[28:31], v[196:199], v[108:111]\n"
      "v_mfma_f32_16x16x32_bf16 v[112:115], a[32:35], v[196:199], v[112:115]\n"
      "v_mfma_f32_16x16x32_bf16 v[116:119], a[36:39], v[196:199], v[116:119]\n"
      "v_mfma_f32_16x16x32_bf16 v[120:123], a[40:43], v[196:199], v[120:123]\n"
      "v_mfma_f32_16x16x32_bf16 v[124:127], a[44:47], v[196:199], v[124:127]\n"
      "s_waitcnt vmcnt(10)\n"
      "v_mfma_f32_16x16x32_bf16 v[108:111], a[48:51], v[200:203], v[108:111]\n"
      "v_mfma_f32_16x16x32_bf16 v[112:115], a[52:55], v[200:203], v[112:115]\n"
      "v_mfma_f32_16x16x32_bf16 v[116:119], a[56:59], v[200:203], v[116:119]\n"
      "v_mfma_f32_16x16x32_bf16 v[120:123], a[60:63], v[200:203], v[120:123]\n"
      "v_mfma_f32_16x16x32_bf16 v[124:127], a[64:67], v[200:203], v[124:127]\n"
      "s_waitcnt vmcnt(9)\n"
      "v_mfma_f32_16x16x32_bf16 v[108:111], a[68:71], v[204:207], v[108:111]\n"
      "v_mfma_f32_16x16x32_bf16 v[112:115], a[72:75], v[204:207], v[112:115]\n"
      "v_mfma_f32_16x16x32_bf16 v[116:119], a[76:79], v[204:207], v[116:119]\n"
      "v_mfma_f32_16x16x32_bf16 v[120:123], a[80:83], v[204:207], v[120:123]\n"
      "v_mfma_f32_16x16x32_bf16 v[124:127], a[84:87], v[204:207], v[124:127]\n"
      "s_waitcnt vmcnt(8)\n"
      "v_mfma_f32_16x16x32_bf16 v[108:111], a[88:91], v[208:211], v[108:111]\n"
      "v_mfma_f32_16x16x32_bf16 v[112:115], a[92:95], v[208:211], v[112:115]\n"
      "v_mfma_f32_16x16x32_bf16 v[116:119], a[96:99], v[208:211], v[116:119]\n"
      "v_mfma_f32_16x16x32_bf16 v[120:123], a[100:103], v[208:211], v[120:123]\n"
      "v_mfma_f32_16x16x32_bf16 v[124:127], a[104:107], v[208:211], v[124:127]\n"
      "s_waitcnt vmcnt(7)\n"
      "v_mfma_f32_16x16x32_bf16 v[108:111], a[108:111], v[212:215], v[108:111]\n"
      "v_mfma_f32_16x16x32_bf16 v[112:115], a[112:115], v[212:215], v[112:115]\n"
      "v_mfma_f32_16x16x32_bf16 v[116:119], a[116:119], v[212:215], v[116:119]\n"
      "v_mfma_f32_16x16x32_bf16 v[120:123], a[120:123], v[212:215], v[120:123]\n"
      "v_mfma_f32_16x16x32_bf16 v[124:127], a[124:127], v[212:215], v[124:127]\n"
      "s_waitcnt vmcnt(6)\n"
      "v_mfma_f32_16x16x32_bf16 v[108:111], a[128:131], v[216:219], v[108:111]\n"
      "v_mfma_f32_16x16x32_bf16 v[112:115], a[132:135], v[216:219], v[112:115]\n"
      "v_mfma_f32_16x16x32_bf16 v[116:119], a[136:139], v[216:219], v[116:119]\n"
      "v_mfma_f32_16x16x32_bf16 v[120:123], a[140:143], v[216:219], v[120:123]\n"
      "v_mfma_f32_16x16x32_bf16 v[124:127], a[144:147], v[216:219], v[124:127]\n"
      "s_waitcnt vmcnt(5)\n"
      "v_mfma_f32_16x16x32_bf16 v[108:111], a[148:151], v[220:223], v[108:111]\n"
      "v_mfma_f32_16x16x32_bf16 v[112:115], a[152:155], v[220:223], v[112:115]\n"
      "v_mfma_f32_16x16x32_bf16 v[116:119], a[156:159], v[220:223], v[116:119]\n"
      "v_mfma_f32_16x16x32_bf16 v[120:123], a[160:163], v[220:223], v[120:123]\n"
      "v_mfma_f32_16x16x32_bf16 v[124:127], a[164:167], v[220:223], v[124:127]\n"
      "s_waitcnt vmcnt(4)\n"
      "v_mfma_f32_16x16x32_bf16 v[108:111], a[168:171], v[224:227], v[108:111]\n"
      "v_mfma_f32_16x16x32_bf16 v[112:115], a[172:175], v[224:227], v[112:115]\n"
      "v_mfma_f32_16x16x32_bf16 v[116:119], a[176:179], v[224:227], v[116:119]\n"
      "v_mfma_f32_16x16x32_bf16 v[120:123], a[180:183], v[224:227], v[120:123]\n"
      "v_mfma_f32_16x16x32_bf16 v[124:127], a[184:187], v[224:227], v[124:127]\n"
      "s_waitcnt vmcnt(3)\n"
      "v_mfma_f32_16x16x32_bf16 v[108:111], a[188:191], v[228:231], v[108:111]\n"
      "v_mfma_f32_16x16x32_bf16 v[112:115], a[192:195], v[228:231], v[112:115]\n"
      "v_mfma_f32_16x16x32_bf16 v[116:119], a[196:199], v[228:231], v[116:119]\n"
      "v_mfma_f32_16x16x32_bf16 v[120:123], a[200:203], v[228:231], v[120:123]\n"
      "v_mfma_f32_16x16x32_bf16 v[124:127], a[204:207], v[228:231], v[124:127]\n"
      "s_waitcnt vmcnt(2)\n"
      "v_mfma_f32_16x16x32_bf16 v[108:111], a[208:211], v[232:235], v[108:111]\n"
      "v_mfma_f32_16x16x32_bf16 v[112:115], a[212:215], v[232:235], v[112:115]\n"
      "v_mfma_f32_16x16x32_bf16 v[116:119], a[216:219], v[232:235], v[116:119]\n"
      "v_mfma_f32_16x16x32_bf16 v[120:123], a[220:223], v[232:235], v[120:123]\n"
      "v_mfma_f32_16x16x32_bf16 v[124:127], a[224:227], v[232:235], v[124:127]\n"
      "s_waitcnt vmcnt(1)\n"
      "v_mfma_f32_16x16x32_bf16 v[108:111], a[228:231], v[236:239], v[108:111]\n"
      "v_mfma_f32_16x16x32_bf16 v[112:115], a[232:235], v[236:239], v[112:115]\n"
      "v_mfma_f32_16x16x32_bf16 v[116:119], a[236:239], v[236:239], v[116:119]\n"
      "v_mfma_f32_16x16x32_bf16 v[120:123], a[240:243], v[236:239], v[120:123]\n"
      "v_mfma_f32_16x16x32_bf16 v[124:127], a[244:247], v[236:239], v[124:127]\n"
      "s_waitcnt vmcnt(0)\n"
      "v_mfma_f32_16x16x32_bf16 v[108:111], a[248:251], v[240:243], v[108:111]\n"
      "v_mfma_f32_16x16x32_bf16 v[112:115], a[252:255], v[240:243], v[112:115]\n"
      "v_mfma_f32_16x16x32_bf16 v[116:119], v[176:179], v[240:243], v[116:119]\n"
      "v_mfma_f32_16x16x32_bf16 v[120:123], v[180:183], v[240:243], v[120:123]\n"
      "v_mfma_f32_16x16x32_bf16 v[124:127], v[184:187], v[240:243], v[124:127]\n"
      "s_nop 3\n"
      "ds_write_b128 v13, v[108:111] offset:5760\n"
      "ds_write_b128 v13, v[112:115] offset:6912\n"
      "ds_write_b128 v13, v[116:119] offset:8064\n"
      "ds_write_b128 v13, v[120:123] offset:9216\n"
      "ds_write_b128 v13, v[124:127] offset:10368\n"
      "s_waitcnt lgkmcnt(0)\n"
      "s_barrier\n"
      "ds_read_b128 v[88:91], v14\n"
      "ds_read_b128 v[92:95], v14 offset:11520\n"
      "ds_read_b128 v[96:99], v14 offset:23040\n"
      "ds_read_b128 v[100:103], v14 offset:34560\n"
      "ds_read_b128 v[104:107], v14 offset:256\n"
      "ds_read_b128 v[108:111], v14 offset:11776\n"
      "ds_read_b128 v[112:115], v14 offset:23296\n"
      "ds_read_b128 v[116:119], v14 offset:34816\n"
      "v_lshlrev_b32 v120, 16, v41\n"
      "v_and_b32 v121, 0xffff0000, v41\n"
      "s_waitcnt lgkmcnt(6)\n"
      "v_pk_add_f32 v[88:89], v[88:89], v[92:93]\n"
      "v_pk_add_f32 v[90:91], v[90:91], v[94:95]\n"
      "s_waitcnt lgkmcnt(4)\n"
      "v_pk_add_f32 v[96:97], v[96:97], v[100:101]\n"
      "v_pk_add_f32 v[98:99], v[98:99], v[102:103]\n"
      "v_pk_add_f32 v[148:149], v[88:89], v[96:97]\n"
      "v_pk_add_f32 v[150:151], v[90:91], v[98:99]\n"
      "ds_read_b128 v[88:91], v14 offset:512\n"
      "s_waitcnt lgkmcnt(3)\n"
      "v_pk_add_f32 v[104:105], v[104:105], v[108:109]\n"
      "s_waitcnt lgkmcnt(1)\n"
      "v_pk_add_f32 v[112:113], v[112:113], v[116:117]\n"
      "v_pk_add_f32 v[106:107], v[106:107], v[110:111]\n"
      "v_pk_add_f32 v[114:115], v[114:115], v[118:119]\n"
      "ds_read_b128 v[92:95], v14 offset:12032\n"
      "ds_read_b128 v[96:99], v14 offset:23552\n"
      "v_pk_add_f32 v[152:153], v[104:105], v[112:113]\n"
      "v_pk_add_f32 v[154:155], v[106:107], v[114:115]\n"
      "ds_read_b128 v[100:103], v14 offset:35072\n"
      "ds_read_b128 v[104:107], v14 offset:768\n"
      "ds_read_b128 v[108:111], v14 offset:12288\n"
      "ds_read_b128 v[112:115], v14 offset:23808\n"
      "ds_read_b128 v[116:119], v14 offset:35328\n"
      "v_pk_add_f32 v[150:151], v[150:151], v[120:121]\n"
      "v_lshlrev_b32 v120, 16, v43\n"
      "v_and_b32 v121, 0xffff0000, v43\n"
      "v_cvt_pk_bf16_f32 v41, v150, v151\n"
      "v_pk_add_f32 v[154:155], v[154:155], v[120:121]\n"
      "v_lshlrev_b32 v120, 16, v45\n"
      "v_and_b32 v121, 0xffff0000, v45\n"
      "v_cvt_pk_bf16_f32 v43, v154, v155\n"
      "s_waitcnt lgkmcnt(6)\n"
      "v_pk_add_f32 v[88:89], v[88:89], v[92:93]\n"
      "v_pk_add_f32 v[90:91], v[90:91], v[94:95]\n"
      "s_waitcnt lgkmcnt(4)\n"
      "v_pk_add_f32 v[96:97], v[96:97], v[100:101]\n"
      "v_pk_add_f32 v[98:99], v[98:99], v[102:103]\n"
      "v_pk_add_f32 v[156:157], v[88:89], v[96:97]\n"
      "v_pk_add_f32 v[158:159], v[90:91], v[98:99]\n"
      "s_waitcnt lgkmcnt(0)\n"
      "v_pk_add_f32 v[114:115], v[114:115], v[118:119]\n"
      "v_lshlrev_b32 v118, 16, v40\n"
      "v_and_b32 v119, 0xffff0000, v40\n"
      "v_pk_add_f32 v[104:105], v[104:105], v[108:109]\n"
      "v_pk_add_f32 v[148:149], v[148:149], v[118:119]\n"
      "v_lshlrev_b32 v118, 16, v42\n"
      "v_and_b32 v119, 0xffff0000, v42\n"
      "v_pk_add_f32 v[112:113], v[112:113], v[116:117]\n"
      "v_pk_add_f32 v[152:153], v[152:153], v[118:119]\n"
      "v_lshlrev_b32 v118, 16, v44\n"
      "v_and_b32 v119, 0xffff0000, v44\n"
      "v_pk_add_f32 v[106:107], v[106:107], v[110:111]\n"
      "v_pk_add_f32 v[156:157], v[156:157], v[118:119]\n"
      "v_pk_add_f32 v[158:159], v[158:159], v[120:121]\n"
      "v_pk_add_f32 v[160:161], v[104:105], v[112:113]\n"
      "v_pk_add_f32 v[162:163], v[106:107], v[114:115]\n"
      "v_lshlrev_b32 v118, 16, v46\n"
      "v_and_b32 v119, 0xffff0000, v46\n"
      "v_lshlrev_b32 v120, 16, v47\n"
      "v_and_b32 v121, 0xffff0000, v47\n"
      "v_pk_add_f32 v[160:161], v[160:161], v[118:119]\n"
      "v_pk_add_f32 v[162:163], v[162:163], v[120:121]\n"
      "v_cvt_pk_bf16_f32 v40, v148, v149\n"
      "v_cvt_pk_bf16_f32 v42, v152, v153\n"
      "v_cvt_pk_bf16_f32 v44, v156, v157\n"
      "v_cvt_pk_bf16_f32 v45, v158, v159\n"
      "v_cvt_pk_bf16_f32 v46, v160, v161\n"
      "v_cvt_pk_bf16_f32 v47, v162, v163\n"
      "buffer_store_dwordx4 v[40:43], v18, s[24:27], 0 offen offset:1280\n"
      "buffer_store_dwordx4 v[44:47], v18, s[24:27], 0 offen offset:1296\n"
      "s_waitcnt vmcnt(0) lgkmcnt(0)\n"
      "LCODEEND:\n"
      :: "s"(ka),"s"(tlo),"s"(thi),"s"(b),"s"(wv)
      : "v0", "v1", "v2", "v3", "v4", "v5", "v6", "v7", "v8", "v9", "v10", "v11", "v12", "v13", "v14", "v15", "v16", "v17", "v18", "v19", "v20", "v21", "v22", "v23", "v24", "v25", "v26", "v27", "v28", "v29", "v30", "v31", "v32", "v33", "v34", "v35", "v36", "v37", "v38", "v39", "v40", "v41", "v42", "v43", "v44", "v45", "v46", "v47", "v48", "v49", "v50", "v51", "v52", "v53", "v54", "v55", "v56", "v57", "v58", "v59", "v60", "v61", "v62", "v63", "v64", "v65", "v66", "v67", "v68", "v69", "v70", "v71", "v72", "v73", "v74", "v75", "v76", "v77", "v78", "v79", "v80", "v81", "v82", "v83", "v84", "v85", "v86", "v87", "v88", "v89", "v90", "v91", "v92", "v93", "v94", "v95", "v96", "v97", "v98", "v99", "v100", "v101", "v102", "v103", "v104", "v105", "v106", "v107", "v108", "v109", "v110", "v111", "v112", "v113", "v114", "v115", "v116", "v117", "v118", "v119", "v120", "v121", "v122", "v123", "v124", "v125", "v126", "v127", "v128", "v129", "v130", "v131", "v132", "v133", "v134", "v135", "v136", "v137", "v138", "v139", "v140", "v141", "v142", "v143", "v144", "v145", "v146", "v147", "v148", "v149", "v150", "v151", "v152", "v153", "v154", "v155", "v156", "v157", "v158", "v159", "v160", "v161", "v162", "v163", "v164", "v165", "v166", "v167", "v168", "v169", "v170", "v171", "v172", "v173", "v174", "v175", "v176", "v177", "v178", "v179", "v180", "v181", "v182", "v183", "v184", "v185", "v186", "v187", "v188", "v189", "v190", "v191", "v192", "v193", "v194", "v195", "v196", "v197", "v198", "v199", "v200", "v201", "v202", "v203", "v204", "v205", "v206", "v207", "v208", "v209", "v210", "v211", "v212", "v213", "v214", "v215", "v216", "v217", "v218", "v219", "v220", "v221", "v222", "v223", "v224", "v225", "v226", "v227", "v228", "v229", "v230", "v231", "v232", "v233", "v234", "v235", "v236", "v237", "v238", "v239", "v240", "v241", "v242", "v243", "v244", "v245", "v246", "v247", "v248", "v249", "v250", "v251", "v252", "v253", "v254", "v255", "a0", "a1", "a2", "a3", "a4", "a5", "a6", "a7", "a8", "a9", "a10", "a11", "a12", "a13", "a14", "a15", "a16", "a17", "a18", "a19", "a20", "a21", "a22", "a23", "a24", "a25", "a26", "a27", "a28", "a29", "a30", "a31", "a32", "a33", "a34", "a35", "a36", "a37", "a38", "a39", "a40", "a41", "a42", "a43", "a44", "a45", "a46", "a47", "a48", "a49", "a50", "a51", "a52", "a53", "a54", "a55", "a56", "a57", "a58", "a59", "a60", "a61", "a62", "a63", "a64", "a65", "a66", "a67", "a68", "a69", "a70", "a71", "a72", "a73", "a74", "a75", "a76", "a77", "a78", "a79", "a80", "a81", "a82", "a83", "a84", "a85", "a86", "a87", "a88", "a89", "a90", "a91", "a92", "a93", "a94", "a95", "a96", "a97", "a98", "a99", "a100", "a101", "a102", "a103", "a104", "a105", "a106", "a107", "a108", "a109", "a110", "a111", "a112", "a113", "a114", "a115", "a116", "a117", "a118", "a119", "a120", "a121", "a122", "a123", "a124", "a125", "a126", "a127", "a128", "a129", "a130", "a131", "a132", "a133", "a134", "a135", "a136", "a137", "a138", "a139", "a140", "a141", "a142", "a143", "a144", "a145", "a146", "a147", "a148", "a149", "a150", "a151", "a152", "a153", "a154", "a155", "a156", "a157", "a158", "a159", "a160", "a161", "a162", "a163", "a164", "a165", "a166", "a167", "a168", "a169", "a170", "a171", "a172", "a173", "a174", "a175", "a176", "a177", "a178", "a179", "a180", "a181", "a182", "a183", "a184", "a185", "a186", "a187", "a188", "a189", "a190", "a191", "a192", "a193", "a194", "a195", "a196", "a197", "a198", "a199", "a200", "a201", "a202", "a203", "a204", "a205", "a206", "a207", "a208", "a209", "a210", "a211", "a212", "a213", "a214", "a215", "a216", "a217", "a218", "a219", "a220", "a221", "a222", "a223", "a224", "a225", "a226", "a227", "a228", "a229", "a230", "a231", "a232", "a233", "a234", "a235", "a236", "a237", "a238", "a239", "a240", "a241", "a242", "a243", "a244", "a245", "a246", "a247", "a248", "a249", "a250", "a251", "a252", "a253", "a254", "a255", "s16", "s17", "s18", "s19", "s20", "s21", "s22", "s23", "s24", "s25", "s26", "s27", "s28", "s29", "s30", "s31", "s34", "s35", "s36", "s37", "s38", "s39", "s40", "s41", "s42", "s43", "s44", "s45", "s46", "s47", "s48", "s49", "s50", "s51", "s52", "s53", "s54", "s55", "s56", "s57", "s58", "s59", "s60", "s61", "s62", "s63", "s64", "s65", "s66", "s67", "s68", "s69", "s70", "s71", "s72", "s73", "s74", "s75", "s76", "s77", "s78", "s79", "s80", "s81", "s82", "s83", "s84", "s85", "s86", "s87", "s88", "s89", "s90", "s91", "s92", "s93", "s94", "s95", "s96", "s97", "s98", "s99", "vcc", "memory");
  }
}

extern "C" void kernel_launch(void* const* d_in, const int* in_sizes, int n_in, void* d_out, int out_size, void* d_ws, size_t ws_size, hipStream_t stream){
  Params p;
  p.x=(const u16*)d_in[0]; p.wqkv=(const u16*)d_in[1]; p.wo=(const u16*)d_in[2];
  p.wg=(const u16*)d_in[4]; p.wu=(const u16*)d_in[5]; p.wd=(const u16*)d_in[6];
  p.out=(u16*)d_out; p.ws=(char*)d_ws;
  (void)hipFuncSetAttribute((const void*)fused_kernel,hipFuncAttributeMaxDynamicSharedMemorySize,163840);
  fused_kernel<<<256,256,163840,stream>>>(p);
}
```
